```python
import torch

def _softmax(x, axis=-1):
    return torch.softmax(x, dim=axis)
def _gelu(x):
    return torch.nn.functional.gelu(x, approximate="tanh")
def _silu(x):
    return torch.nn.functional.silu(x)
def _sigmoid(x):
    return torch.sigmoid(x)
def _layernorm(x, axis=-1, eps=1e-5):
    m = x.mean(dim=axis, keepdim=True)
    v = x.var(dim=axis, keepdim=True, unbiased=False)
    return (x - m) / torch.sqrt(v + eps)
def _scatter_add(tgt, idx, src, axis=0):
    return tgt.clone().index_add_(axis, idx, src)
def _segment_sum(src, seg, n):
    out = torch.zeros((n,) + src.shape[1:], dtype=src.dtype)
    return out.index_add_(0, seg, src)
def _pad(x, pw):
    flat = tuple(v for pair in reversed(pw) for v in pair)
    return torch.nn.functional.pad(x, flat)

kernel_name = 'interleave_layers__sliding_local_dilated__ssm_conv_s4d__prompt'

def setup_inputs():

    _w = (lambda a: (lambda t: t.to(torch.bfloat16) if t.is_floating_point() else t)(torch.as_tensor(a)))
    def _A_setup():
        B, S, D = 1, 2048, 512
        return {
            "x": _w((lambda *s: torch.randn(*s, dtype=torch.bfloat16) * 0.05)(B, S, D)),
            "wqkv": _w((lambda *s: torch.randn(*s, dtype=torch.bfloat16) * 0.05)(D, 3 * D)),
            "wo": _w((lambda *s: torch.randn(*s, dtype=torch.bfloat16) * 0.05)(D, D)),
        }
    _A_in = _A_setup()

    def _B_setup():
        Bsz, L, D, N = 1, 2048, 512, 128
        return {
            "u": _w((lambda *s: torch.randn(*s, dtype=torch.bfloat16) * 0.05)(Bsz, L, D)),
            "log_A_real": _w((lambda *s: torch.randn(*s, dtype=torch.bfloat16) * 6.4)(D, N) * 0.5),
            "A_imag": _w((lambda *s: torch.randn(*s, dtype=torch.bfloat16) * 0.1)(D, N) * 3.14),
            "C_real": _w((lambda *s: torch.randn(*s, dtype=torch.bfloat16) * 0.1)(D, N) / N ** 0.5),
            "C_imag": _w((lambda *s: torch.randn(*s, dtype=torch.bfloat16) * 0.1)(D, N) / N ** 0.5),
            "log_dt": _w((lambda *s: torch.randn(*s, dtype=torch.bfloat16) * 0.1)(D) * 0.5 - 4.0),
            "D_skip": _w((lambda *s: torch.randn(*s, dtype=torch.bfloat16) * 0.1)(D)),
        }
    _B_in = _B_setup()

    return {
        "A_x": _A_in["x"],
        "A_wqkv": _A_in["wqkv"],
        "A_wo": _A_in["wo"],
        "B_u": _B_in["u"],
        "B_log_A_real": _B_in["log_A_real"],
        "B_A_imag": _B_in["A_imag"],
        "B_C_real": _B_in["C_real"],
        "B_C_imag": _B_in["C_imag"],
        "B_log_dt": _B_in["log_dt"],
        "B_D_skip": _B_in["D_skip"],
    }

def reference(A_x, A_wqkv, A_wo, B_u, B_log_A_real, B_A_imag, B_C_real, B_C_imag, B_log_dt, B_D_skip):
    def _A_ref(x, wqkv, wo):
        B, S, D = x.shape
        H = 32; Dh = D // H
        W, dil = 8, 2
        qkv = (lambda x, *d: x.permute(*d))((x @ wqkv).reshape(B, S, 3, H, Dh), 2, 0, 3, 1, 4)
        q, k, v = qkv[0], qkv[1], qkv[2]
        scale = Dh ** -0.5
        pos = torch.arange(S)
        off = torch.arange(W) * dil
        idx = pos[:, None] - off[None, :]
        mask = idx >= 0
        idx_c = torch.where(mask, idx, 0).reshape(-1)
        kg = k[:, :, idx_c, :].reshape(B, H, S, W, Dh)
        vg = v[:, :, idx_c, :].reshape(B, H, S, W, Dh)
        logits = torch.einsum("bhsd,bhswd->bhsw", q, kg) * scale
        logits = torch.where(mask[None, None, :, :], logits, -1e9)
        attn = _softmax(logits, axis=-1)
        o = torch.einsum("bhsw,bhswd->bhsd", attn, vg)
        return (lambda x, *d: x.permute(*d))(o, 0, 2, 1, 3).reshape(B, S, D) @ wo

    def _B_ref(u, log_A_real, A_imag, C_real, C_imag, log_dt, D_skip):
        L = u.shape[1]
        dt = torch.exp(log_dt)
        A = -torch.exp(log_A_real) + 1j * A_imag
        C = (C_real + 1j * C_imag) * dt[:, None]
        pos = (lambda x, dt: x.to(dt))(torch.arange(L), log_dt.dtype)
        P = torch.exp((A * dt[:, None])[:, :, None] * pos[None, None, :])
        K = 2.0 * torch.einsum("dn,dnl->dl", C, P).real
        L2 = 2 * L
        u_t = (lambda x, *d: x.permute(*d))(u, 0, 2, 1)
        Kf = (lambda x, *a, **k: torch.fft.rfft(x.float(), *a, **k))(K, L2)
        Uf = (lambda x, *a, **k: torch.fft.rfft(x.float(), *a, **k))(u_t, L2)
        y = (lambda x, *a, **k: torch.fft.irfft(x, *a, **k).to(torch.bfloat16))(Uf * Kf[None, :, :], L2)[:, :, :L]
        y = (lambda x, *d: x.permute(*d))(y, 0, 2, 1)
        return y + u * D_skip[None, None, :]

    x = A_x
    x = x + _A_ref(_layernorm(x, axis=-1), A_wqkv, A_wo)
    x = x + _B_ref(_layernorm(x, axis=-1), B_log_A_real, B_A_imag, B_C_real, B_C_imag, B_log_dt, B_D_skip)
    x = x + _A_ref(_layernorm(x, axis=-1), A_wqkv, A_wo)
    x = x + _B_ref(_layernorm(x, axis=-1), B_log_A_real, B_A_imag, B_C_real, B_C_imag, B_log_dt, B_D_skip)
    return x
```

```cpp
#include <hip/hip_runtime.h>
#include <hip/hip_bf16.h>
#include <stdint.h>
typedef unsigned short u16;
__device__ __forceinline__ float bf2f(u16 b){ return __builtin_bit_cast(float, (unsigned)b<<16); }
__device__ __forceinline__ u16 f2bf(float f){ unsigned u=__builtin_bit_cast(unsigned,f); u += 0x7FFFu + ((u>>16)&1u); return (u16)(u>>16); }
constexpr int S=2048, D=512, N=128;

__global__ void k_kgen(const u16* lar, const u16* aim, const u16* cre, const u16* cim, const u16* ldt, float* K){
  int d=blockIdx.x; __shared__ float ar[N], ai[N], cr[N], ci[N];
  float dt = bf2f(f2bf(expf(bf2f(ldt[d]))));
  if(threadIdx.x<N){ int n=threadIdx.x; float a=-bf2f(f2bf(expf(bf2f(lar[d*N+n])))); ar[n]=a*dt; ai[n]=bf2f(aim[d*N+n])*dt; cr[n]=bf2f(cre[d*N+n])*dt; ci[n]=bf2f(cim[d*N+n])*dt; }
  __syncthreads();
  for(int l=threadIdx.x;l<S;l+=blockDim.x){ float p=bf2f(f2bf((float)l)); float acc=0;
    for(int n=0;n<N;n++){ float e=__expf(ar[n]*p); float s,c; float th=ai[n]*p; s=__sinf(th); c=__cosf(th); acc += e*(cr[n]*c - ci[n]*s); }
    K[d*S+l]=2.f*acc; }
}
__global__ void k_ln(const u16* x, u16* y){ int t=blockIdx.x; __shared__ float red[2][4];
  float v0=bf2f(x[t*D+threadIdx.x]), v1=bf2f(x[t*D+256+threadIdx.x]);
  float s=v0+v1; for(int o=32;o;o>>=1) s+=__shfl_xor(s,o); if((threadIdx.x&63)==0) red[0][threadIdx.x>>6]=s; __syncthreads();
  float m=(red[0][0]+red[0][1]+red[0][2]+red[0][3])/D; float a=v0-m,b=v1-m; float q=a*a+b*b; for(int o=32;o;o>>=1) q+=__shfl_xor(q,o); if((threadIdx.x&63)==0) red[1][threadIdx.x>>6]=q; __syncthreads();
  float var=(red[1][0]+red[1][1]+red[1][2]+red[1][3])/D; float r=rsqrtf(var+1e-5f);
  y[t*D+threadIdx.x]=f2bf(a*r); y[t*D+256+threadIdx.x]=f2bf(b*r); }
__global__ void k_gemm(const u16* A, const u16* B, const u16* res, u16* C, int Nn){
  __shared__ float As[16][33], Bs[32][17]; int tx=threadIdx.x&15, ty=threadIdx.x>>4; int r=blockIdx.y*16+ty, c=blockIdx.x*16+tx; float acc=0;
  for(int k0=0;k0<512;k0+=32){ As[ty][tx]=bf2f(A[r*512+k0+tx]); As[ty][tx+16]=bf2f(A[r*512+k0+16+tx]); Bs[ty][tx]=bf2f(B[(k0+ty)*Nn+c]); Bs[ty+16][tx]=bf2f(B[(k0+16+ty)*Nn+c]); __syncthreads();
    for(int k=0;k<32;k++) acc+=As[ty][k]*Bs[k][tx]; __syncthreads(); }
  if(res) acc = bf2f(f2bf(acc)) + bf2f(res[r*Nn+c]); C[r*Nn+c]=f2bf(acc); }
__global__ void k_attn(const u16* qkv, u16* o){ int i=blockIdx.x*blockDim.x+threadIdx.x; int h=i&31, t=i>>5; float q[16]; for(int j=0;j<16;j++) q[j]=bf2f(qkv[t*1536+h*16+j]);
  float lg[8]; float m=-1e30f;
  #pragma unroll
  for(int w=0;w<8;w++){ int s=t-2*w; float a=-1e9f; if(s>=0){ a=0; for(int j=0;j<16;j++) a+=q[j]*bf2f(qkv[s*1536+512+h*16+j]); a*=0.25f;} lg[w]=a; m=fmaxf(m,a);} float sum=0;
  #pragma unroll
  for(int w=0;w<8;w++){ lg[w]=__expf(lg[w]-m); sum+=lg[w]; } float acc[16]; for(int j=0;j<16;j++) acc[j]=0;
  #pragma unroll
  for(int w=0;w<8;w++){ int s=t-2*w; if(s>=0) for(int j=0;j<16;j++) acc[j]+=lg[w]/sum*bf2f(qkv[s*1536+1024+h*16+j]); }
  for(int j=0;j<16;j++) o[t*512+h*16+j]=f2bf(acc[j]); }
__global__ void k_conv(const u16* u, const u16* x, const float* K, const u16* dsk, u16* out){ int d=blockIdx.x; __shared__ float us[S], ks[S];
  for(int l=threadIdx.x;l<S;l+=blockDim.x){ us[l]=bf2f(u[l*D+d]); ks[l]=K[d*S+l]; } __syncthreads(); float ds=bf2f(dsk[d]);
  for(int t=threadIdx.x;t<S;t+=blockDim.x){ float acc=0; for(int l=0;l<=t;l++) acc+=ks[l]*us[t-l]; out[t*D+d]=f2bf(bf2f(x[t*D+d]) + acc + us[t]*ds); } }
__global__ void k_empty(float* p){ if(p==nullptr) return; }
#define NEXTRA 0
extern "C" void kernel_launch(void* const* d_in, const int* in_sizes, int n_in, void* d_out, int out_size, void* d_ws, size_t ws_size, hipStream_t stream){
  const u16* x=(const u16*)d_in[0]; const u16* wqkv=(const u16*)d_in[1]; const u16* wo=(const u16*)d_in[2];
  char* ws=(char*)d_ws; float* K=(float*)ws; u16* xn=(u16*)(ws+(8<<20)); u16* qkv=(u16*)(ws+(12<<20)); u16* o=(u16*)(ws+(20<<20)); u16* x1=(u16*)(ws+(24<<20)); u16* x2=(u16*)(ws+(28<<20));
  k_kgen<<<512,256,0,stream>>>((const u16*)d_in[4],(const u16*)d_in[5],(const u16*)d_in[6],(const u16*)d_in[7],(const u16*)d_in[8],K);
  const u16* cur=x;
  for(int L=0;L<2;L++){
    k_ln<<<S,256,0,stream>>>(cur,xn);
    k_gemm<<<dim3(1536/16,S/16),256,0,stream>>>(xn,wqkv,nullptr,qkv,1536);
    k_attn<<<S*32/256,256,0,stream>>>(qkv,o);
    k_gemm<<<dim3(512/16,S/16),256,0,stream>>>(o,wo,cur,x1,512);
    k_ln<<<S,256,0,stream>>>(x1,xn);
    k_conv<<<512,256,0,stream>>>(xn,x1,K,(const u16*)d_in[9], L==0? x2 : (u16*)d_out);
    cur=x2;
  }
  for(int i=0;i<NEXTRA;i++) k_empty<<<256,256,0,stream>>>((float*)ws);
}
```

```cpp
#include <hip/hip_runtime.h>
#include <hip/hip_bf16.h>
#include <stdint.h>

#define GAS __attribute__((address_space(1)))
#define LAS __attribute__((address_space(3)))
#define DI __device__ __forceinline__
typedef unsigned short u16;
typedef unsigned v4u __attribute__((ext_vector_type(4)));
typedef unsigned v2u __attribute__((ext_vector_type(2)));
typedef float v4f __attribute__((ext_vector_type(4)));
typedef float v2f __attribute__((ext_vector_type(2)));
typedef short v4s __attribute__((ext_vector_type(4)));
typedef __bf16 v8b __attribute__((ext_vector_type(8)));
typedef __bf16 v2b __attribute__((ext_vector_type(2)));
typedef __amdgpu_buffer_rsrc_t rsrc_t;
typedef LAS unsigned char* lds_t;

#define STAMPS 0

constexpr int MB2 = 2 << 20;
constexpr int WS_O = 0;
constexpr int WS_X1 = 2 * MB2;
constexpr int WS_X2 = 4 * MB2;
constexpr int WS_ST = 5 * MB2;
constexpr int WS_RDY = WS_ST + 65536;
constexpr int WS_EP = WS_RDY + 4096;
constexpr int WS_STAMP = WS_EP + 4096;
constexpr int WS_END = WS_STAMP + 256 * 64 * 8;
constexpr int L_XI = 0;
constexpr int L_RED = 65536;
constexpr int L_KG = 0;
constexpr int KG_ROW = 528, KG_CH = 57 * KG_ROW;
constexpr int L_KC = 98304, KC_STR = 4160;
constexpr int L_U = L_KC + 8 * KC_STR, U_STR = 4608;
constexpr int L_KT = L_U + 2 * U_STR, KT_STR = 1344;
constexpr int L_SP = L_KT + 2 * KT_STR;
constexpr int L_SF = L_SP + 1536;
constexpr int L_S2 = L_SF + 1536;
constexpr int L_END = L_S2 + 512;
constexpr unsigned SENT = 0xFFFFFFFFu;
constexpr unsigned SPIN_CAP = 20000u;
constexpr int SC1 = 16;

struct Params {
  const u16 *x, *wqkv, *wo, *lar, *aim, *cre, *cim, *ldt, *dsk;
  u16* out;
  unsigned char* ws;
};

DI float asf(unsigned u) { return __builtin_bit_cast(float, u); }
DI unsigned asu(float f) { return __builtin_bit_cast(unsigned, f); }
DI float bflo(unsigned d) { return asf(d << 16); }
DI float bfhi(unsigned d) { return asf(d & 0xFFFF0000u); }
DI unsigned pk2(float lo, float hi) { v2f t = {lo, hi}; return __builtin_bit_cast(unsigned, __builtin_convertvector(t, v2b)); }
DI float bfr(float f) { return bflo(pk2(f, f)); }
DI v4f mfma32(v4u a, v4u b, v4f c) { return __builtin_amdgcn_mfma_f32_16x16x32_bf16(__builtin_bit_cast(v8b, a), __builtin_bit_cast(v8b, b), c, 0, 0, 0); }
DI v4f mfma16(v2u a, v2u b, v4f c) { return __builtin_amdgcn_mfma_f32_16x16x16bf16_1k(__builtin_bit_cast(v4s, a), __builtin_bit_cast(v4s, b), c, 0, 0, 0); }
DI rsrc_t mkrs(const void* p, int bytes) { return __builtin_amdgcn_make_buffer_rsrc((void*)p, 0, bytes, 0x00020000); }
template <int AUX> DI v4u ld16(rsrc_t r, int v, int s) { return __builtin_amdgcn_raw_buffer_load_b128(r, v, s, AUX); }
template <int AUX> DI void st16(v4u d, rsrc_t r, int v, int s) { __builtin_amdgcn_raw_buffer_store_b128(d, r, v, s, AUX); }
DI v4u nosent(v4u d) { d.x = min(d.x, SENT - 1); d.y = min(d.y, SENT - 1); d.z = min(d.z, SENT - 1); d.w = min(d.w, SENT - 1); return d; }
DI unsigned mx4(unsigned m, v4u v) { return max(max(max(m, v.x), v.y), max(v.z, v.w)); }
DI v4u lr16(lds_t p) { return *(LAS v4u*)p; }
DI v4f lr16f(lds_t p) { return *(LAS v4f*)p; }
DI v2u lr8(lds_t p) { return *(LAS v2u*)p; }
DI v4u trfrag(lds_t p) {
  v2u a = __builtin_bit_cast(v2u, __builtin_amdgcn_ds_read_tr16_b64_v4i16((LAS v4s*)p));
  v2u b = __builtin_bit_cast(v2u, __builtin_amdgcn_ds_read_tr16_b64_v4i16((LAS v4s*)(p + 512)));
  return v4u{a.x, a.y, b.x, b.y};
}
DI float xmax(float m) {
  auto a = __builtin_amdgcn_permlane16_swap(asu(m), asu(m), false, false); m = fmaxf(asf(a[0]), asf(a[1]));
  auto b = __builtin_amdgcn_permlane32_swap(asu(m), asu(m), false, false); return fmaxf(asf(b[0]), asf(b[1]));
}
DI float xsum(float m) {
  auto a = __builtin_amdgcn_permlane16_swap(asu(m), asu(m), false, false); m = asf(a[0]) + asf(a[1]);
  auto b = __builtin_amdgcn_permlane32_swap(asu(m), asu(m), false, false); return asf(b[0]) + asf(b[1]);
}
DI float fold32(float a, float b) { auto r = __builtin_amdgcn_permlane32_swap(asu(a), asu(b), false, false); return asf(r[0]) + asf(r[1]); }
DI float fold16(float a, float b) { auto r = __builtin_amdgcn_permlane16_swap(asu(a), asu(b), false, false); return asf(r[0]) + asf(r[1]); }
template <int CTRL> DI float dppadd(float x) { return x + asf(__builtin_amdgcn_update_dpp(0, asu(x), CTRL, 0xf, 0xf, true)); }
DI v2f cmul(v2f a, v2f w) { return v2f{a.x * w.x - a.y * w.y, a.x * w.y + a.y * w.x}; }
DI v2f cexpf2(float re, float im) {
  const float e = __builtin_amdgcn_exp2f(re * 1.44269504088896341f);
  const float t = __builtin_amdgcn_fractf(im * 0.159154943091895336f);
  return v2f{e * __builtin_amdgcn_cosf(t), e * __builtin_amdgcn_sinf(t)};
}
#define FRESH() int tid = threadIdx.x; asm volatile("" : "+v"(tid)); const int lane = tid & 63, l15 = lane & 15, g = lane >> 4; (void)l15; (void)g
#define LBAR() asm volatile("s_waitcnt lgkmcnt(0)\n\ts_barrier" ::: "memory")
#define DRAIN() asm volatile("s_waitcnt vmcnt(0)" ::: "memory")
#define STAMP(i)

__global__ void __launch_bounds__(256, 1) fused_ab2(Params p) {
  extern __shared__ __attribute__((aligned(16))) unsigned char smem[];
  lds_t L = (lds_t)smem;
  const int tid = threadIdx.x, lane = tid & 63, w = __builtin_amdgcn_readfirstlane(tid >> 6);
  const int wg = (blockIdx.x & 7) * 32 + (blockIdx.x >> 3), grp = wg >> 3, mem = wg & 7;
  const int l15 = lane & 15, g = lane >> 4;
  const int sel = mem >> 2, cg = mem & 3;
  const rsrc_t rws = mkrs(p.ws, WS_END), rx = mkrs(p.x, MB2), rwq = mkrs(p.wqkv, 512 * 3072), rwo = mkrs(p.wo, 512 * 1024), rout = mkrs(p.out, MB2);
  unsigned cap = SPIN_CAP;
  STAMP(0);
  const unsigned tag = __builtin_amdgcn_readfirstlane(__builtin_amdgcn_raw_buffer_load_b32(rws, WS_EP, 0, SC1)) + 1u;

  {
    const v4u s = {SENT, SENT, SENT, SENT};
#pragma unroll
    for (int i = 0; i < 10; ++i) st16<SC1>(s, rws, tid * 16, (i >> 1) * MB2 + wg * 8192 + (i & 1) * 4096);
    if (tid < 16) st16<SC1>(s, rws, tid * 16, WS_ST + wg * 256);
    DRAIN();
    LBAR();
    if (tid == 0) __builtin_amdgcn_raw_buffer_store_b32(tag, rws, WS_RDY + wg * 4, 0, SC1);
  }
  STAMP(1);
  v4u wq[48];
  {
    const int r = 8 * w + (lane >> 3), swz = ((r >> 1) & 1) | (((r >> 3) & 1) << 1);
    const int src = r * 3072 + mem * 128 + ((((lane & 7) >> 1) ^ swz) * 32) + (lane & 1) * 16;
    const int r2 = 8 * g + (l15 >> 2), swz2 = ((r2 >> 1) & 1) | (((r2 >> 3) & 1) << 1);
    const int tro = r2 * 128 + ((w ^ swz2) * 32) + (lane & 3) * 8;
#pragma unroll
    for (int rd = 0; rd < 2; ++rd) {
#pragma unroll
      for (int i = 0; i < 24; ++i)
        __builtin_amdgcn_raw_ptr_buffer_load_lds(rwq, (LAS void*)(L + i * 4096 + w * 1024), 16, src, (rd * 8 + i / 3) * 32 * 3072 + (i % 3) * 1024, 0, 0);
      DRAIN();
      LBAR();
#pragma unroll
      for (int j = 0; j < 8; ++j)
#pragma unroll
        for (int pp = 0; pp < 3; ++pp) wq[(rd * 8 + j) * 3 + pp] = trfrag(L + tro + (((j & 3) ^ w) * 12288) + (j >> 2) * 49152 + pp * 4096);
      LBAR();
    }
  }
  v4u wo[32];
  {
    const int src = (128 * w + 16 * (g >> 1) + 4 * (g & 1)) * 1024 + cg * 256 + l15 * 16;
#pragma unroll
    for (int ks = 0; ks < 4; ++ks) {
      v4u raw[8];
#pragma unroll
      for (int j = 0; j < 8; ++j) raw[j] = ld16<0>(rwo, src, (32 * ks + (j & 3) + 8 * (j >> 2)) * 1024);
#pragma unroll
      for (int c = 0; c < 8; ++c)
#pragma unroll
        for (int jj = 0; jj < 4; ++jj)
          wo[ks * 8 + c][jj] = __builtin_amdgcn_perm(raw[2 * jj + 1][c >> 1], raw[2 * jj][c >> 1], (c & 1) ? 0x07060302u : 0x05040100u);
    }
  }
#pragma unroll
  for (int i = 0; i < 48; ++i) asm volatile("" : "+a"(wq[i]));
  const v4u ONES = {0x3F803F80u, 0x3F803F80u, 0x3F803F80u, 0x3F803F80u};
  const v4f Z4 = {0.f, 0.f, 0.f, 0.f};
  v4f csq = Z4, csk = Z4, csv4 = Z4;
#pragma unroll
  for (int j = 0; j < 16; ++j) { csq = mfma32(wq[3 * j], ONES, csq); csk = mfma32(wq[3 * j + 1], ONES, csk); csv4 = mfma32(ONES, wq[3 * j + 2], csv4); }
  const float csv = csv4[0];
  STAMP(2);

  {
    const int ch = tid >> 7, n = tid & 127, gi = wg * 256 + tid;
    const float dt = bfr(__expf(bflo(p.ldt[2 * wg + ch])));
    const float ar = -bfr(__expf(bflo(p.lar[gi]))) * dt, ai = bflo(p.aim[gi]) * dt;
    const v2f c0 = {2.f * bflo(p.cre[gi]) * dt, 2.f * bflo(p.cim[gi]) * dt};
    const v2f w1 = cexpf2(ar, ai), w8 = cexpf2(8.f * ar, 8.f * ai), w128 = cexpf2(128.f * ar, 128.f * ai);
    const v2f w2 = cmul(w1, w1), w4 = cmul(w2, w2);
    lds_t kg = L + L_KG + ch * KG_CH + n * 4;
#define KGPUT(row, z) *(LAS unsigned*)(kg + (row) * KG_ROW) = pk2((z).x, (z).y)
    v2f G = c0;
#pragma unroll
    for (int i = 0; i <= 16; ++i) {
      const int row0 = i < 2 ? 8 * i : i < 4 ? 16 + 4 * (i - 2) : i < 8 ? 24 + 2 * (i - 4) : 32 + (i - 8);
      const int cnt = i < 2 ? 8 : i < 4 ? 4 : i < 8 ? 2 : 1;
      v2f z = G;
      KGPUT(row0, z);
#pragma unroll
      for (int r = 1; r < cnt; ++r) { z = cmul(z, i < 2 ? w1 : i < 4 ? w2 : w4); KGPUT(row0 + r, z); }
      if (i < 16) G = cmul(G, w128);
    }
    v2f y = {1.f, 0.f};
    const v2f w8c = {w8.x, -w8.y};
#pragma unroll
    for (int j = 0; j < 16; ++j) { KGPUT(41 + j, y); if (j < 15) y = cmul(y, w8c); }
    if (tid < 2) *(LAS u16*)(L + L_KT + tid * KT_STR + 664 * 2) = 0;
    if (tid < 60) *(LAS v4u*)(L + L_U + (tid / 30) * U_STR + (tid % 30) * 16) = v4u{0, 0, 0, 0};
    if (tid < 16) *(LAS v4u*)(L + L_KC + (tid >> 1) * KC_STR + 4096 + (tid & 1) * 16) = v4u{0, 0, 0, 0};
    LBAR();
    {
      lds_t kb = L + L_KG + (w >> 1) * KG_CH + l15 * KG_ROW + g * 16;
      const int t0 = (w & 1) * 32;
      v4f a0 = Z4, a1 = Z4;
#pragma unroll
      for (int s = 0; s < 8; ++s) {
        const v4u cf = lr16(kb + 41 * KG_ROW + s * 64);
        a0 = mfma32(lr16(kb + t0 * KG_ROW + s * 64), cf, a0);
        a1 = mfma32(lr16(kb + 16 * KG_ROW + s * 64), cf, a1);
      }
      lds_t kt = L + L_KT + (w >> 1) * KT_STR;
#pragma unroll
      for (int r = 0; r < 4; ++r) {
        const int rho = 4 * g + r;
        const int i0 = (w & 1) ? (rho <= 8 ? 512 + 16 * rho + l15 : 668) : 128 * (rho >> 3) + (rho & 7) + 8 * l15;
        const int i1 = rho < 8 ? 256 + 64 * (rho >> 2) + (rho & 3) + 4 * l15 : 384 + 32 * ((rho - 8) >> 1) + (rho & 1) + 2 * l15;
        *(LAS u16*)(kt + i0 * 2) = (u16)pk2(a0[r], a0[r]);
        *(LAS u16*)(kt + i1 * 2) = (u16)pk2(a1[r], a1[r]);
      }
    }
    LBAR();
    {
      int ix[11];
#pragma unroll
      for (int e = 0; e < 11; ++e) {
        const int l = 2047 - 8 * tid - e;
        const int hi = (int)(pk2((float)l, 0.f) & 0xFFFFu) - (134 << 7) + 128;
        ix[e] = 2 * (l < 0 ? 664 : l < 128 ? l : hi);
      }
#pragma unroll
      for (int c2 = 0; c2 < 2; ++c2) {
        unsigned v[11];
#pragma unroll
        for (int e = 0; e < 11; ++e) v[e] = *(LAS u16*)(L + L_KT + c2 * KT_STR + ix[e]);
#pragma unroll
        for (int c = 0; c < 4; ++c) {
          v4u d;
#pragma unroll
          for (int j = 0; j < 4; ++j) d[j] = v[3 - c + 2 * j] | (v[4 - c + 2 * j] << 16);
          *(LAS v4u*)(L + L_KC + (c2 * 4 + c) * KC_STR + tid * 16) = d;
        }
      }
    }
    LBAR();
  }
  const unsigned dskp = *(const GAS unsigned*)(p.dsk + 2 * wg);
  const float dsk0 = bflo(dskp), dsk1 = bfhi(dskp);
  STAMP(3);
  for (unsigned sp = 0;; ++sp) {
    asm volatile("" ::: "memory");
    const v4u f = ld16<SC1>(rws, lane * 16, WS_RDY);
    if (__all(f.x == tag && f.y == tag && f.z == tag && f.w == tag)) break;
    if (sp >= cap) { cap = 0; break; }
  }
  if (wg == 0 && tid == 0) __builtin_amdgcn_raw_buffer_store_b32(tag, rws, WS_EP, 0, SC1);
  STAMP(4);

  const bool par = lane & 1;
  const bool cur0 = 4 * g + (lane & 1) <= l15, cur1 = 4 * g + (lane & 1) + 2 <= l15;
  const unsigned selA = par ? 0x01000c0cu : 0x0c0c0100u, selB = par ? 0x03020c0cu : 0x0c0c0302u, selZ = 0x0c0c0c0cu;
  const unsigned sel0 = cur0 ? selZ : selA, sel1 = cur1 ? selZ : selB, sel2 = cur0 ? selA : selZ, sel3 = cur1 ? selB : selZ;
  const int head = 4 * mem + w;
  v4u resA = {0, 0, 0, 0}, resB = {0, 0, 0, 0};

#pragma clang loop unroll(disable)
  for (int layer = 0; layer < 2; ++layer) {
#pragma clang loop unroll(disable)
    for (int h = 0; h < 2; ++h) {
      FRESH();
      const int ftok = l15 ^ ((l15 >> 3) << 1);
#pragma unroll
      for (int i = 0; i < 48; ++i) asm volatile("" : "+a"(wq[i]));
      lds_t XI = L + L_XI + h * 49152;
      const int tile0 = 64 * h + 2 * grp;
      const bool first = tile0 == 0;
      if (layer == 0) {
        v4u xv[12];
#pragma unroll
        for (int i = 0; i < 12; ++i) {
          const int T = (i >> 2) == 0 && first ? 0 : tile0 - 1 + (i >> 2);
          xv[i] = ld16<0>(rx, lane * 16, T * 16384 + (4 * (i & 3) + w) * 1024);
        }
#pragma unroll
        for (int i = 0; i < 12; ++i) {
          const int tok = 4 * (i & 3) + w, ft = tok ^ ((tok >> 3) << 1);
          *(LAS v4u*)(XI + (i >> 2) * 16384 + tok * 1024 + ((lane ^ ft) * 16)) = xv[i];
        }
      } else {
        v4u xv[12];
        for (unsigned sp = 0;; ++sp) {
          asm volatile("" ::: "memory");
          unsigned m = 0;
#pragma unroll
          for (int i = 0; i < 12; ++i) {
            const int T = (i >> 2) == 0 && first ? 0 : tile0 - 1 + (i >> 2);
            xv[i] = ld16<SC1>(rws, lane * 16, WS_X2 + T * 16384 + w * 4096 + (i & 3) * 1024);
          }
#pragma unroll
          for (int i = 0; i < 12; ++i) m = mx4(m, xv[i]);
          if (!__any(m == SENT)) break;
          if (sp >= cap) { cap = 0; break; }
        }
#pragma unroll
        for (int t = 0; t < 3; ++t)
#pragma unroll
          for (int j = 0; j < 4; ++j) {
            const int tok = 4 * (lane & 3) + j, ft = tok ^ ((tok >> 3) << 1), c = 16 * w + (lane >> 2);
            *(LAS v4u*)(XI + t * 16384 + tok * 1024 + ((c ^ ft) * 16)) = v4u{xv[4 * t][j], xv[4 * t + 1][j], xv[4 * t + 2][j], xv[4 * t + 3][j]};
          }
      }
      LBAR();
      STAMP(8 + layer * 24 + h * 2);
      v4f kH = Z4, vH = Z4, q0 = Z4, k0 = Z4, v0 = Z4, q1 = Z4, k1 = Z4, v1 = Z4;
      float aT[3], bT[3];
      v4f aR[3], bR[3];
#pragma unroll
      for (int jj = 0; jj < 4; ++jj) {
        lds_t xb = XI + l15 * 1024 + (((4 * (jj ^ w) + g) ^ ftok) * 16);
        v4f gr[3] = {Z4, Z4, Z4}, sm[3] = {Z4, Z4, Z4};
        v4f p0, p1;
        if (jj == 2) { const int ll = lane < 48 ? lane : 0; p0 = lr16f(L + L_SP + ll * 32); p1 = lr16f(L + L_SP + ll * 32 + 16); }
        if (jj == 3) {
#pragma unroll
          for (int t = 0; t < 3; ++t) {
            lds_t sf = L + L_SF + w * 384 + t * 64;
            aT[t] = *(LAS float*)(sf + l15 * 4); bT[t] = *(LAS float*)(sf + 192 + l15 * 4);
            aR[t] = lr16f(sf + g * 16); bR[t] = lr16f(sf + 192 + g * 16);
          }
        }
#pragma unroll
        for (int m = 0; m < 4; ++m) {
          const int j = 4 * m + jj;
          const v4u xh = lr16(xb + m * 256), x0 = lr16(xb + m * 256 + 16384), x1 = lr16(xb + m * 256 + 32768);
          if (jj == 0) {
            gr[0] = mfma32(xh, xh, gr[0]); sm[0] = mfma32(ONES, xh, sm[0]);
            gr[1] = mfma32(x0, x0, gr[1]); sm[1] = mfma32(ONES, x0, sm[1]);
            gr[2] = mfma32(x1, x1, gr[2]); sm[2] = mfma32(ONES, x1, sm[2]);
          }
          kH = mfma32(wq[3 * j + 1], xh, kH); vH = mfma32(xh, wq[3 * j + 2], vH);
          q0 = mfma32(wq[3 * j], x0, q0); k0 = mfma32(wq[3 * j + 1], x0, k0); v0 = mfma32(x0, wq[3 * j + 2], v0);
          q1 = mfma32(wq[3 * j], x1, q1); k1 = mfma32(wq[3 * j + 1], x1, k1); v1 = mfma32(x1, wq[3 * j + 2], v1);
        }
        if (jj == 0) {
#pragma unroll
          for (int t = 0; t < 3; ++t) {
            const float d = (lane & 2) ? ((lane & 1) ? gr[t][3] : gr[t][2]) : ((lane & 1) ? gr[t][1] : gr[t][0]);
            if ((l15 >> 2) == g) *(LAS v2f*)(L + L_SP + (t * 16 + l15) * 32 + w * 8) = v2f{sm[t][0], d};
          }
        }
        if (jj == 1) LBAR();
        if (jj == 2) {
          const float mean = (p0[0] + p0[2] + p1[0] + p1[2]) * (1.f / 512.f);
          const float var = fmaxf((p0[1] + p0[3] + p1[1] + p1[3]) * (1.f / 512.f) - mean * mean, 0.f);
          const float a = __builtin_amdgcn_rsqf(var + 1e-5f);
          if (lane < 48) { *(LAS float*)(L + L_SF + w * 384 + lane * 4) = a; *(LAS float*)(L + L_SF + w * 384 + 192 + lane * 4) = -mean * a; }
        }
      }
      resB = resA;
      {
        const int pp = 16 * w + (lane >> 2), c = 16 * cg + (pp >> 2);
#pragma unroll
        for (int j = 0; j < 4; ++j) {
          const int tok = 4 * (lane & 3) + j, ft = tok ^ ((tok >> 3) << 1);
          resA[j] = *(LAS unsigned*)(XI + (1 + sel) * 16384 + tok * 1024 + ((c ^ ft) * 16) + (pp & 3) * 4);
        }
      }
      const float QS = 0.25f * 1.44269504088896341f;
      auto fixT = [&](v4f a, v4f cs, float sa, float sb) { const v4f r = a * sa + cs * sb; return v2u{pk2(r[0], r[1]), pk2(r[2], r[3])}; };
      auto fixR = [&](v4f a, v4f sa, v4f sb) { const v4f r = a * sa + sb * csv; return v2u{pk2(r[0], r[1]), pk2(r[2], r[3])}; };
      const v2u kb[3] = {fixT(kH, csk, aT[0], bT[0]), fixT(k0, csk, aT[1], bT[1]), fixT(k1, csk, aT[2], bT[2])};
      const v2u qb[2] = {fixT(q0, csq, aT[1] * QS, bT[1] * QS), fixT(q1, csq, aT[2] * QS, bT[2] * QS)};
      const v2u vb[3] = {fixR(vH, aR[0], bR[0]), fixR(v0, aR[1], bR[1]), fixR(v1, aR[2], bR[2])};
      v2u ob[2];
#pragma unroll
      for (int t = 0; t < 2; ++t) {
        const v4f sP = mfma16(kb[t], qb[t], Z4), sC = mfma16(kb[t + 1], qb[t], Z4);
        float p0s = par ? sP[1] : sP[0], p1s = par ? sP[3] : sP[2];
        if (t == 0 && first) { p0s = -1.44e9f; p1s = -1.44e9f; }
        const float s0 = cur0 ? (par ? sC[1] : sC[0]) : p0s, s1 = cur1 ? (par ? sC[3] : sC[2]) : p1s;
        const float mxv = xmax(fmaxf(s0, s1));
        const float e0 = __builtin_amdgcn_exp2f(s0 - mxv), e1 = __builtin_amdgcn_exp2f(s1 - mxv);
        const unsigned pk = pk2(e0, e1);
        const float inv = __builtin_amdgcn_rcpf(xsum(bflo(pk) + bfhi(pk)));
        const v4u pf = {__builtin_amdgcn_perm(0u, pk, sel0), __builtin_amdgcn_perm(0u, pk, sel1), __builtin_amdgcn_perm(0u, pk, sel2), __builtin_amdgcn_perm(0u, pk, sel3)};
        const v4f o = mfma32(v4u{vb[t].x, vb[t].y, vb[t + 1].x, vb[t + 1].y}, pf, Z4) * inv;
        ob[t] = v2u{pk2(o[0], o[1]), pk2(o[2], o[3])};
      }
      {
        auto rx0 = __builtin_amdgcn_permlane32_swap(ob[0].x, ob[1].x, false, false);
        auto ry0 = __builtin_amdgcn_permlane32_swap(ob[0].y, ob[1].y, false, false);
        st16<SC1>(nosent(v4u{rx0[0], ry0[0], rx0[1], ry0[1]}), rws, (lane >> 5) * 16384 + (lane & 31) * 16, WS_O + layer * MB2 + tile0 * 16384 + head * 512);
      }
      STAMP(9 + layer * 24 + h * 2);
    }
#pragma clang loop unroll(disable)
    for (int h = 0; h < 2; ++h) {
      FRESH();
      const int tile = 64 * h + 2 * grp + sel;
      v4u of[4];
      for (unsigned sp = 0;; ++sp) {
        asm volatile("" ::: "memory");
        unsigned m = 0;
#pragma unroll
        for (int i = 0; i < 4; ++i) of[i] = ld16<SC1>(rws, lane * 16 + i * 1024, WS_O + layer * MB2 + tile * 16384 + w * 4096);
#pragma unroll
        for (int i = 0; i < 4; ++i) m = mx4(m, of[i]);
        if (!__any(m == SENT)) break;
        if (sp >= cap) { cap = 0; break; }
      }
      STAMP(12 + layer * 24 + h * 2);
      if (h == 0) LBAR();
#pragma unroll
      for (int c = 0; c < 8; ++c) {
        v4f a = Z4;
#pragma unroll
        for (int ks = 0; ks < 4; ++ks) a = mfma32(of[ks], wo[ks * 8 + c], a);
        *(LAS v4f*)(L + L_RED + w * 8192 + c * 1024 + g * 256 + ((l15 ^ (g + 4 * (c >> 1))) * 16)) = a;
      }
      LBAR();
      v4f y0, y1;
      {
        lds_t rb = L + L_RED + ((lane >> 2) & 3) * 2048 + (lane & 3) * 256 + (((4 * w + g) ^ l15) * 16);
        y0 = (lr16f(rb) + lr16f(rb + 8192)) + (lr16f(rb + 16384) + lr16f(rb + 24576));
        y1 = (lr16f(rb + 1024) + lr16f(rb + 9216)) + (lr16f(rb + 17408) + lr16f(rb + 25600));
      }
      v4u xo;
      float s[4], q[4];
#pragma unroll
      for (int j = 0; j < 4; ++j) {
        const float a = y0[j] + bflo(resB[j]), b = y1[j] + bfhi(resB[j]);
        xo[j] = pk2(a, b); s[j] = a + b; q[j] = a * a + b * b;
      }
      resB = resA;
      st16<SC1>(nosent(xo), rws, (lane >> 2) * 8192 + (lane & 3) * 16, WS_X1 + layer * MB2 + (64 * cg + 16 * w) * 8192 + tile * 64);
      {
        float sr = fold16(fold32(s[0], s[2]), fold32(s[1], s[3])), qr = fold16(fold32(q[0], q[2]), fold32(q[1], q[3]));
        sr = dppadd<0x128>(sr); qr = dppadd<0x128>(qr);
        sr = dppadd<0x124>(sr); qr = dppadd<0x124>(qr);
        if ((lane & 12) == 0) *(LAS v2f*)(L + L_S2 + (4 * (lane & 3) + g) * 32 + w * 8) = v2f{sr, qr};
      }
      LBAR();
      if (tid < 16) {
        const v4f p0 = lr16f(L + L_S2 + tid * 32), p1 = lr16f(L + L_S2 + tid * 32 + 16);
        const float mean = (p0[0] + p0[2] + p1[0] + p1[2]) * (1.f / 128.f);
        const float var = fmaxf((p0[1] + p0[3] + p1[1] + p1[3]) * (1.f / 128.f) - mean * mean, 0.f);
        const unsigned lo = pk2(mean, mean) & 0xFFFFu;
        const unsigned vb_ = asu(var) + 0x8000u;
        const unsigned d = ((vb_ < lo ? 0u : vb_ - lo) & 0xFFFF0000u) | lo;
        __builtin_amdgcn_raw_buffer_store_b32(min(d, SENT - 1), rws, tid * 4, WS_ST + layer * 32768 + cg * 8192 + tile * 64, SC1);
      }
      STAMP(13 + layer * 24 + h * 2);
    }
    v4f cD[4] = {Z4, Z4, Z4, Z4};
    v4u kf[16];
    lds_t kp;
    {
      FRESH();
      kp = L + L_KC + ((w >> 1) * 4 + (l15 & 3)) * KC_STR + 2 * (2028 - 32 * (w & 1) - (l15 & 12) + 8 * g) - 128 * 31;
#pragma unroll
      for (int a = 0; a < 16; ++a) { const v2u lo = lr8(kp + 128 * (31 - a)), hi = lr8(kp + 128 * (31 - a) + 8); kf[a] = v4u{lo.x, lo.y, hi.x, hi.y}; if (a < 10) asm volatile("" : "+a"(kf[a])); }
    }
#pragma clang loop unroll(disable)
    for (int h = 0; h < 2; ++h) {
      FRESH();
      v4u xd, st[4];
      for (unsigned sp = 0;; ++sp) {
        asm volatile("" ::: "memory");
        xd = ld16<SC1>(rws, tid * 16, WS_X1 + layer * MB2 + wg * 8192 + h * 4096);
#pragma unroll
        for (int i = 0; i < 4; ++i) st[i] = ld16<SC1>(rws, tid * 16, WS_ST + layer * 32768 + i * 8192 + h * 4096);
        unsigned m = mx4(0, xd);
#pragma unroll
        for (int i = 0; i < 4; ++i) m = mx4(m, st[i]);
        if (!__any(m == SENT)) break;
        if (sp >= cap) { cap = 0; break; }
      }
      STAMP(16 + layer * 24 + h * 3);
      float u0[4], u1[4], b0[4], b1[4];
#pragma unroll
      for (int j = 0; j < 4; ++j) {
        const float m0 = bflo(st[0][j]), m1 = bflo(st[1][j]), m2 = bflo(st[2][j]), m3 = bflo(st[3][j]);
        const float mean = ((m0 + m1) + (m2 + m3)) * 0.25f;
        const float t = ((m0 * m0 + asf(st[0][j])) + (m1 * m1 + asf(st[1][j]))) + ((m2 * m2 + asf(st[2][j])) + (m3 * m3 + asf(st[3][j])));
        const float a = __builtin_amdgcn_rsqf(fmaxf(t * 0.25f - mean * mean, 0.f) + 1e-5f);
        const float x0 = bflo(xd[j]), x1 = bfhi(xd[j]);
        u0[j] = (x0 - mean) * a; u1[j] = (x1 - mean) * a;
        b0[j] = x0 + u0[j] * dsk0; b1[j] = x1 + u1[j] * dsk1;
      }
      *(LAS v2u*)(L + L_U + 480 + h * 2048 + tid * 8) = v2u{pk2(u0[0], u0[1]), pk2(u0[2], u0[3])};
      *(LAS v2u*)(L + L_U + U_STR + 480 + h * 2048 + tid * 8) = v2u{pk2(u1[0], u1[1]), pk2(u1[2], u1[3])};
      LBAR();
      lds_t ub = L + L_U + (w >> 1) * U_STR + 480 - 32 - 64 * (6 + (w & 1)) + l15 * 32 + g * 16;
#pragma unroll
      for (int e = 0; e < 16; ++e) {
        const v4u bf = lr16(ub + h * 2048 + 128 * e);
#pragma unroll
        for (int i = 0; i < 4; ++i) { const int a = 4 * i + 3 - e; if (a >= 0) cD[i] = mfma32(kf[a], bf, cD[i]); }
      }
#pragma unroll
      for (int i = 0; i < 4; ++i) *(LAS v4f*)(L + L_RED + w * 4096 + i * 1024 + g * 256 + ((l15 ^ (4 * g)) * 16)) = cD[i];
      LBAR();
      {
        lds_t rb = L + L_RED + w * 1024 + (lane & 3) * 256 + (((lane >> 2) ^ (4 * (lane & 3))) * 16);
        const v4f y0 = lr16f(rb) + lr16f(rb + 4096), y1 = lr16f(rb + 8192) + lr16f(rb + 12288);
        v4u xo;
#pragma unroll
        for (int j = 0; j < 4; ++j) xo[j] = pk2(b0[j] + y0[j], b1[j] + y1[j]);
        if (layer == 0) {
          const int slot = (wg & ~63) + 16 * (wg & 3) + ((wg >> 2) & 15);
          st16<SC1>(nosent(xo), rws, (tid >> 2) * 16384 + (tid & 3) * 16, WS_X2 + h * (64 * 16384) + slot * 64);
        } else {
#pragma unroll
          for (int j = 0; j < 4; ++j) __builtin_amdgcn_raw_buffer_store_b32(xo[j], rout, tid * 4096 + j * 1024, h * (1024 * 1024) + wg * 4, 0);
        }
      }
      STAMP(17 + layer * 24 + h * 3);
      if (h == 0) {
#pragma unroll
        for (int i = 0; i < 4; ++i) cD[i] = Z4;
#pragma unroll
        for (int rho = 0; rho < 4; ++rho) {
          v4u ku[4];
#pragma unroll
          for (int u = 0; u < 4; ++u) { const int a = 19 - rho + 4 * u; const v2u lo = lr8(kp + 128 * (31 - a)), hi = lr8(kp + 128 * (31 - a) + 8); ku[u] = v4u{lo.x, lo.y, hi.x, hi.y}; }
#pragma unroll
          for (int ee = 0; ee < 4; ++ee) {
            const int e = rho + 4 * ee;
            const v4u bf = lr16(ub + 128 * e);
#pragma unroll
            for (int i = 0; i < 4; ++i) { const int a = 4 * i + 19 - e; cD[i] = mfma32(a >= 16 ? ku[i - ee] : kf[a], bf, cD[i]); }
          }
        }
        STAMP(18 + layer * 24);
      }
    }
  }
  STAMP(5);
}

extern "C" void kernel_launch(void* const* d_in, const int* in_sizes, int n_in, void* d_out, int out_size, void* d_ws, size_t ws_size,
                              hipStream_t stream) {
  Params p;
  p.x = (const u16*)d_in[0]; p.wqkv = (const u16*)d_in[1]; p.wo = (const u16*)d_in[2];
  p.lar = (const u16*)d_in[4]; p.aim = (const u16*)d_in[5]; p.cre = (const u16*)d_in[6]; p.cim = (const u16*)d_in[7];
  p.ldt = (const u16*)d_in[8]; p.dsk = (const u16*)d_in[9];
  p.out = (u16*)d_out; p.ws = (unsigned char*)d_ws;
  (void)hipFuncSetAttribute((const void*)fused_ab2, hipFuncAttributeMaxDynamicSharedMemorySize, L_END);
  fused_ab2<<<256, 256, L_END, stream>>>(p);
}
```

```cpp
#define PF 1
#include <hip/hip_runtime.h>
#include <hip/hip_bf16.h>
#include <stdint.h>

#define GAS __attribute__((address_space(1)))
#define LAS __attribute__((address_space(3)))
#define DI __device__ __forceinline__
typedef unsigned short u16;
typedef unsigned v4u __attribute__((ext_vector_type(4)));
typedef unsigned v2u __attribute__((ext_vector_type(2)));
typedef float v4f __attribute__((ext_vector_type(4)));
typedef float v2f __attribute__((ext_vector_type(2)));
typedef short v4s __attribute__((ext_vector_type(4)));
typedef __bf16 v8b __attribute__((ext_vector_type(8)));
typedef __bf16 v2b __attribute__((ext_vector_type(2)));
typedef __amdgpu_buffer_rsrc_t rsrc_t;
typedef LAS unsigned char* lds_t;

#define STAMPS 0

constexpr int MB2 = 2 << 20;
constexpr int WS_O = 0;
constexpr int WS_X1 = 2 * MB2;
constexpr int WS_X2 = 4 * MB2;
constexpr int WS_ST = 5 * MB2;
constexpr int WS_RDY = WS_ST + 65536;
constexpr int WS_EP = WS_RDY + 4096;
constexpr int WS_STAMP = WS_EP + 4096;
constexpr int WS_END = WS_STAMP + 256 * 64 * 8;
constexpr int L_XI = 0;
constexpr int L_RED = 65536;
constexpr int L_KG = 0;
constexpr int KG_ROW = 528, KG_CH = 57 * KG_ROW;
constexpr int L_KC = 98304, KC_STR = 4160;
constexpr int L_U = L_KC + 8 * KC_STR, U_STR = 4608;
constexpr int L_KT = L_U + 2 * U_STR, KT_STR = 1344;
constexpr int L_SP = L_KT + 2 * KT_STR;
constexpr int L_SF = L_SP + 1536;
constexpr int L_S2 = L_SF + 1536;
constexpr int L_END = L_S2 + 512;
constexpr unsigned SENT = 0xFFFFFFFFu;
constexpr unsigned SPIN_CAP = 20000u;
constexpr int SC1 = 16;

struct Params {
  const u16 *x, *wqkv, *wo, *lar, *aim, *cre, *cim, *ldt, *dsk;
  u16* out;
  unsigned char* ws;
};

DI float asf(unsigned u) { return __builtin_bit_cast(float, u); }
DI unsigned asu(float f) { return __builtin_bit_cast(unsigned, f); }
DI float bflo(unsigned d) { return asf(d << 16); }
DI float bfhi(unsigned d) { return asf(d & 0xFFFF0000u); }
DI unsigned pk2(float lo, float hi) { v2f t = {lo, hi}; return __builtin_bit_cast(unsigned, __builtin_convertvector(t, v2b)); }
DI float bfr(float f) { return bflo(pk2(f, f)); }
DI v4f mfma32(v4u a, v4u b, v4f c) { return __builtin_amdgcn_mfma_f32_16x16x32_bf16(__builtin_bit_cast(v8b, a), __builtin_bit_cast(v8b, b), c, 0, 0, 0); }
DI v4f mfma16(v2u a, v2u b, v4f c) { return __builtin_amdgcn_mfma_f32_16x16x16bf16_1k(__builtin_bit_cast(v4s, a), __builtin_bit_cast(v4s, b), c, 0, 0, 0); }
DI rsrc_t mkrs(const void* p, int bytes) { return __builtin_amdgcn_make_buffer_rsrc((void*)p, 0, bytes, 0x00020000); }
template <int AUX> DI v4u ld16(rsrc_t r, int v, int s) { return __builtin_amdgcn_raw_buffer_load_b128(r, v, s, AUX); }
template <int AUX> DI void st16(v4u d, rsrc_t r, int v, int s) { __builtin_amdgcn_raw_buffer_store_b128(d, r, v, s, AUX); }
DI v4u nosent(v4u d) { d.x = min(d.x, SENT - 1); d.y = min(d.y, SENT - 1); d.z = min(d.z, SENT - 1); d.w = min(d.w, SENT - 1); return d; }
DI unsigned mx4(unsigned m, v4u v) { return max(max(max(m, v.x), v.y), max(v.z, v.w)); }
DI v4u lr16(lds_t p) { return *(LAS v4u*)p; }
DI v4f lr16f(lds_t p) { return *(LAS v4f*)p; }
DI v2u lr8(lds_t p) { return *(LAS v2u*)p; }
DI v4u trfrag(lds_t p) {
  v2u a = __builtin_bit_cast(v2u, __builtin_amdgcn_ds_read_tr16_b64_v4i16((LAS v4s*)p));
  v2u b = __builtin_bit_cast(v2u, __builtin_amdgcn_ds_read_tr16_b64_v4i16((LAS v4s*)(p + 512)));
  return v4u{a.x, a.y, b.x, b.y};
}
DI float xmax(float m) {
  auto a = __builtin_amdgcn_permlane16_swap(asu(m), asu(m), false, false); m = fmaxf(asf(a[0]), asf(a[1]));
  auto b = __builtin_amdgcn_permlane32_swap(asu(m), asu(m), false, false); return fmaxf(asf(b[0]), asf(b[1]));
}
DI float xsum(float m) {
  auto a = __builtin_amdgcn_permlane16_swap(asu(m), asu(m), false, false); m = asf(a[0]) + asf(a[1]);
  auto b = __builtin_amdgcn_permlane32_swap(asu(m), asu(m), false, false); return asf(b[0]) + asf(b[1]);
}
DI float fold32(float a, float b) { auto r = __builtin_amdgcn_permlane32_swap(asu(a), asu(b), false, false); return asf(r[0]) + asf(r[1]); }
DI float fold16(float a, float b) { auto r = __builtin_amdgcn_permlane16_swap(asu(a), asu(b), false, false); return asf(r[0]) + asf(r[1]); }
template <int CTRL> DI float dppadd(float x) { return x + asf(__builtin_amdgcn_update_dpp(0, asu(x), CTRL, 0xf, 0xf, true)); }
DI v2f cmul(v2f a, v2f w) { return v2f{a.x * w.x - a.y * w.y, a.x * w.y + a.y * w.x}; }
DI v2f cexpf2(float re, float im) {
  const float e = __builtin_amdgcn_exp2f(re * 1.44269504088896341f);
  const float t = __builtin_amdgcn_fractf(im * 0.159154943091895336f);
  return v2f{e * __builtin_amdgcn_cosf(t), e * __builtin_amdgcn_sinf(t)};
}
#define FRESH() int tid = threadIdx.x; asm volatile("" : "+v"(tid)); const int lane = tid & 63, l15 = lane & 15, g = lane >> 4; (void)l15; (void)g
#define LBAR() asm volatile("s_waitcnt lgkmcnt(0)\n\ts_barrier" ::: "memory")
#define DRAIN() asm volatile("s_waitcnt vmcnt(0)" ::: "memory")
#define STAMP(i)

__global__ void __launch_bounds__(256, 1) fused_ab2(Params p) {
  extern __shared__ __attribute__((aligned(16))) unsigned char smem[];
  lds_t L = (lds_t)smem;
  const int tid = threadIdx.x, lane = tid & 63, w = __builtin_amdgcn_readfirstlane(tid >> 6);
  const int wg = (blockIdx.x & 7) * 32 + (blockIdx.x >> 3), grp = wg >> 3, mem = wg & 7;
  const int l15 = lane & 15, g = lane >> 4;
  const int sel = mem >> 2, cg = mem & 3;
  const rsrc_t rws = mkrs(p.ws, WS_END), rx = mkrs(p.x, MB2), rwq = mkrs(p.wqkv, 512 * 3072), rwo = mkrs(p.wo, 512 * 1024), rout = mkrs(p.out, MB2);
  unsigned cap = SPIN_CAP;
  STAMP(0);
  v4u pfc = {0, 0, 0, 0}; unsigned pfo = 0;
  {
    unsigned long long pc; asm volatile("s_getpc_b64 %0" : "=s"(pc));
    const rsrc_t rc = mkrs((const void*)(pc & ~255ull), 22016);
    if (PF & 1) pfc = ld16<0>(rc, lane * 16 + (wg & 7) * 4096, w * 1024);
    if (PF & 2) pfo = __builtin_amdgcn_raw_buffer_load_b32(rout, tid * 8192, 0, 0);
  }
  const unsigned tag = __builtin_amdgcn_readfirstlane(__builtin_amdgcn_raw_buffer_load_b32(rws, WS_EP, 0, SC1)) + 1u;

  {
    const v4u s = {SENT, SENT, SENT, SENT};
#pragma unroll
    for (int i = 0; i < 10; ++i) st16<SC1>(s, rws, tid * 16, (i >> 1) * MB2 + wg * 8192 + (i & 1) * 4096);
    if (tid < 16) st16<SC1>(s, rws, tid * 16, WS_ST + wg * 256);
    DRAIN();
    LBAR();
    if (tid == 0) __builtin_amdgcn_raw_buffer_store_b32(tag, rws, WS_RDY + wg * 4, 0, SC1);
  }
  asm volatile("" :: "v"(pfc), "v"(pfo));
  STAMP(1);
  v4u wq[48];
  {
    const int r = 8 * w + (lane >> 3), swz = ((r >> 1) & 1) | (((r >> 3) & 1) << 1);
    const int src = r * 3072 + mem * 128 + ((((lane & 7) >> 1) ^ swz) * 32) + (lane & 1) * 16;
    const int r2 = 8 * g + (l15 >> 2), swz2 = ((r2 >> 1) & 1) | (((r2 >> 3) & 1) << 1);
    const int tro = r2 * 128 + ((w ^ swz2) * 32) + (lane & 3) * 8;
#pragma unroll
    for (int rd = 0; rd < 2; ++rd) {
#pragma unroll
      for (int i = 0; i < 24; ++i)
        __builtin_amdgcn_raw_ptr_buffer_load_lds(rwq, (LAS void*)(L + i * 4096 + w * 1024), 16, src, (rd * 8 + i / 3) * 32 * 3072 + (i % 3) * 1024, 0, 0);
      DRAIN();
      LBAR();
#pragma unroll
      for (int j = 0; j < 8; ++j)
#pragma unroll
        for (int pp = 0; pp < 3; ++pp) wq[(rd * 8 + j) * 3 + pp] = trfrag(L + tro + (((j & 3) ^ w) * 12288) + (j >> 2) * 49152 + pp * 4096);
      LBAR();
    }
  }
  v4u wo[32];
  {
    const int src = (128 * w + 16 * (g >> 1) + 4 * (g & 1)) * 1024 + cg * 256 + l15 * 16;
#pragma unroll
    for (int ks = 0; ks < 4; ++ks) {
      v4u raw[8];
#pragma unroll
      for (int j = 0; j < 8; ++j) raw[j] = ld16<0>(rwo, src, (32 * ks + (j & 3) + 8 * (j >> 2)) * 1024);
#pragma unroll
      for (int c = 0; c < 8; ++c)
#pragma unroll
        for (int jj = 0; jj < 4; ++jj)
          wo[ks * 8 + c][jj] = __builtin_amdgcn_perm(raw[2 * jj + 1][c >> 1], raw[2 * jj][c >> 1], (c & 1) ? 0x07060302u : 0x05040100u);
    }
  }
#pragma unroll
  for (int i = 0; i < 48; ++i) asm volatile("" : "+a"(wq[i]));
  const v4u ONES = {0x3F803F80u, 0x3F803F80u, 0x3F803F80u, 0x3F803F80u};
  const v4f Z4 = {0.f, 0.f, 0.f, 0.f};
  v4f csq = Z4, csk = Z4, csv4 = Z4;
#pragma unroll
  for (int j = 0; j < 16; ++j) { csq = mfma32(wq[3 * j], ONES, csq); csk = mfma32(wq[3 * j + 1], ONES, csk); csv4 = mfma32(ONES, wq[3 * j + 2], csv4); }
  const float csv = csv4[0];
  STAMP(2);

  {
    const int ch = tid >> 7, n = tid & 127, gi = wg * 256 + tid;
    const float dt = bfr(__expf(bflo(p.ldt[2 * wg + ch])));
    const float ar = -bfr(__expf(bflo(p.lar[gi]))) * dt, ai = bflo(p.aim[gi]) * dt;
    const v2f c0 = {2.f * bflo(p.cre[gi]) * dt, 2.f * bflo(p.cim[gi]) * dt};
    const v2f w1 = cexpf2(ar, ai), w8 = cexpf2(8.f * ar, 8.f * ai), w128 = cexpf2(128.f * ar, 128.f * ai);
    const v2f w2 = cmul(w1, w1), w4 = cmul(w2, w2);
    lds_t kg = L + L_KG + ch * KG_CH + n * 4;
#define KGPUT(row, z) *(LAS unsigned*)(kg + (row) * KG_ROW) = pk2((z).x, (z).y)
    v2f G = c0;
#pragma unroll
    for (int i = 0; i <= 16; ++i) {
      const int row0 = i < 2 ? 8 * i : i < 4 ? 16 + 4 * (i - 2) : i < 8 ? 24 + 2 * (i - 4) : 32 + (i - 8);
      const int cnt = i < 2 ? 8 : i < 4 ? 4 : i < 8 ? 2 : 1;
      v2f z = G;
      KGPUT(row0, z);
#pragma unroll
      for (int r = 1; r < cnt; ++r) { z = cmul(z, i < 2 ? w1 : i < 4 ? w2 : w4); KGPUT(row0 + r, z); }
      if (i < 16) G = cmul(G, w128);
    }
    v2f y = {1.f, 0.f};
    const v2f w8c = {w8.x, -w8.y};
#pragma unroll
    for (int j = 0; j < 16; ++j) { KGPUT(41 + j, y); if (j < 15) y = cmul(y, w8c); }
    if (tid < 2) *(LAS u16*)(L + L_KT + tid * KT_STR + 664 * 2) = 0;
    if (tid < 60) *(LAS v4u*)(L + L_U + (tid / 30) * U_STR + (tid % 30) * 16) = v4u{0, 0, 0, 0};
    if (tid < 16) *(LAS v4u*)(L + L_KC + (tid >> 1) * KC_STR + 4096 + (tid & 1) * 16) = v4u{0, 0, 0, 0};
    LBAR();
    {
      lds_t kb = L + L_KG + (w >> 1) * KG_CH + l15 * KG_ROW + g * 16;
      const int t0 = (w & 1) * 32;
      v4f a0 = Z4, a1 = Z4;
#pragma unroll
      for (int s = 0; s < 8; ++s) {
        const v4u cf = lr16(kb + 41 * KG_ROW + s * 64);
        a0 = mfma32(lr16(kb + t0 * KG_ROW + s * 64), cf, a0);
        a1 = mfma32(lr16(kb + 16 * KG_ROW + s * 64), cf, a1);
      }
      lds_t kt = L + L_KT + (w >> 1) * KT_STR;
#pragma unroll
      for (int r = 0; r < 4; ++r) {
        const int rho = 4 * g + r;
        const int i0 = (w & 1) ? (rho <= 8 ? 512 + 16 * rho + l15 : 668) : 128 * (rho >> 3) + (rho & 7) + 8 * l15;
        const int i1 = rho < 8 ? 256 + 64 * (rho >> 2) + (rho & 3) + 4 * l15 : 384 + 32 * ((rho - 8) >> 1) + (rho & 1) + 2 * l15;
        *(LAS u16*)(kt + i0 * 2) = (u16)pk2(a0[r], a0[r]);
        *(LAS u16*)(kt + i1 * 2) = (u16)pk2(a1[r], a1[r]);
      }
    }
    LBAR();
    {
      int ix[11];
#pragma unroll
      for (int e = 0; e < 11; ++e) {
        const int l = 2047 - 8 * tid - e;
        const int hi = (int)(pk2((float)l, 0.f) & 0xFFFFu) - (134 << 7) + 128;
        ix[e] = 2 * (l < 0 ? 664 : l < 128 ? l : hi);
      }
#pragma unroll
      for (int c2 = 0; c2 < 2; ++c2) {
        unsigned v[11];
#pragma unroll
        for (int e = 0; e < 11; ++e) v[e] = *(LAS u16*)(L + L_KT + c2 * KT_STR + ix[e]);
#pragma unroll
        for (int c = 0; c < 4; ++c) {
          v4u d;
#pragma unroll
          for (int j = 0; j < 4; ++j) d[j] = v[3 - c + 2 * j] | (v[4 - c + 2 * j] << 16);
          *(LAS v4u*)(L + L_KC + (c2 * 4 + c) * KC_STR + tid * 16) = d;
        }
      }
    }
    LBAR();
  }
  const unsigned dskp = *(const GAS unsigned*)(p.dsk + 2 * wg);
  const float dsk0 = bflo(dskp), dsk1 = bfhi(dskp);
  STAMP(3);
  for (unsigned sp = 0;; ++sp) {
    asm volatile("" ::: "memory");
    const v4u f = ld16<SC1>(rws, lane * 16, WS_RDY);
    if (__all(f.x == tag && f.y == tag && f.z == tag && f.w == tag)) break;
    if (sp >= cap) { cap = 0; break; }
  }
  if (wg == 0 && tid == 0) __builtin_amdgcn_raw_buffer_store_b32(tag, rws, WS_EP, 0, SC1);
  STAMP(4);

  const bool par = lane & 1;
  const bool cur0 = 4 * g + (lane & 1) <= l15, cur1 = 4 * g + (lane & 1) + 2 <= l15;
  const unsigned selA = par ? 0x01000c0cu : 0x0c0c0100u, selB = par ? 0x03020c0cu : 0x0c0c0302u, selZ = 0x0c0c0c0cu;
  const unsigned sel0 = cur0 ? selZ : selA, sel1 = cur1 ? selZ : selB, sel2 = cur0 ? selA : selZ, sel3 = cur1 ? selB : selZ;
  const int head = 4 * mem + w;
  v4u resA = {0, 0, 0, 0}, resB = {0, 0, 0, 0};

#pragma clang loop unroll(disable)
  for (int layer = 0; layer < 2; ++layer) {
#pragma clang loop unroll(disable)
    for (int h = 0; h < 2; ++h) {
      FRESH();
      const int ftok = l15 ^ ((l15 >> 3) << 1);
#pragma unroll
      for (int i = 0; i < 48; ++i) asm volatile("" : "+a"(wq[i]));
      lds_t XI = L + L_XI + h * 49152;
      const int tile0 = 64 * h + 2 * grp;
      const bool first = tile0 == 0;
      if (layer == 0) {
        v4u xv[12];
#pragma unroll
        for (int i = 0; i < 12; ++i) {
          const int T = (i >> 2) == 0 && first ? 0 : tile0 - 1 + (i >> 2);
          xv[i] = ld16<0>(rx, lane * 16, T * 16384 + (4 * (i & 3) + w) * 1024);
        }
#pragma unroll
        for (int i = 0; i < 12; ++i) {
          const int tok = 4 * (i & 3) + w, ft = tok ^ ((tok >> 3) << 1);
          *(LAS v4u*)(XI + (i >> 2) * 16384 + tok * 1024 + ((lane ^ ft) * 16)) = xv[i];
        }
      } else {
        v4u xv[12];
        for (unsigned sp = 0;; ++sp) {
          asm volatile("" ::: "memory");
          unsigned m = 0;
#pragma unroll
          for (int i = 0; i < 12; ++i) {
            const int T = (i >> 2) == 0 && first ? 0 : tile0 - 1 + (i >> 2);
            xv[i] = ld16<SC1>(rws, lane * 16, WS_X2 + T * 16384 + w * 4096 + (i & 3) * 1024);
          }
#pragma unroll
          for (int i = 0; i < 12; ++i) m = mx4(m, xv[i]);
          if (!__any(m == SENT)) break;
          if (sp >= cap) { cap = 0; break; }
        }
#pragma unroll
        for (int t = 0; t < 3; ++t)
#pragma unroll
          for (int j = 0; j < 4; ++j) {
            const int tok = 4 * (lane & 3) + j, ft = tok ^ ((tok >> 3) << 1), c = 16 * w + (lane >> 2);
            *(LAS v4u*)(XI + t * 16384 + tok * 1024 + ((c ^ ft) * 16)) = v4u{xv[4 * t][j], xv[4 * t + 1][j], xv[4 * t + 2][j], xv[4 * t + 3][j]};
          }
      }
      LBAR();
      STAMP(8 + layer * 24 + h * 2);
      v4f kH = Z4, vH = Z4, q0 = Z4, k0 = Z4, v0 = Z4, q1 = Z4, k1 = Z4, v1 = Z4;
      float aT[3], bT[3];
      v4f aR[3], bR[3];
#pragma unroll
      for (int jj = 0; jj < 4; ++jj) {
        lds_t xb = XI + l15 * 1024 + (((4 * (jj ^ w) + g) ^ ftok) * 16);
        v4f gr[3] = {Z4, Z4, Z4}, sm[3] = {Z4, Z4, Z4};
        v4f p0, p1;
        if (jj == 2) { const int ll = lane < 48 ? lane : 0; p0 = lr16f(L + L_SP + ll * 32); p1 = lr16f(L + L_SP + ll * 32 + 16); }
        if (jj == 3) {
#pragma unroll
          for (int t = 0; t < 3; ++t) {
            lds_t sf = L + L_SF + w * 384 + t * 64;
            aT[t] = *(LAS float*)(sf + l15 * 4); bT[t] = *(LAS float*)(sf + 192 + l15 * 4);
            aR[t] = lr16f(sf + g * 16); bR[t] = lr16f(sf + 192 + g * 16);
          }
        }
#pragma unroll
        for (int m = 0; m < 4; ++m) {
          const int j = 4 * m + jj;
          const v4u xh = lr16(xb + m * 256), x0 = lr16(xb + m * 256 + 16384), x1 = lr16(xb + m * 256 + 32768);
          if (jj == 0) {
            gr[0] = mfma32(xh, xh, gr[0]); sm[0] = mfma32(ONES, xh, sm[0]);
            gr[1] = mfma32(x0, x0, gr[1]); sm[1] = mfma32(ONES, x0, sm[1]);
            gr[2] = mfma32(x1, x1, gr[2]); sm[2] = mfma32(ONES, x1, sm[2]);
          }
          kH = mfma32(wq[3 * j + 1], xh, kH); vH = mfma32(xh, wq[3 * j + 2], vH);
          q0 = mfma32(wq[3 * j], x0, q0); k0 = mfma32(wq[3 * j + 1], x0, k0); v0 = mfma32(x0, wq[3 * j + 2], v0);
          q1 = mfma32(wq[3 * j], x1, q1); k1 = mfma32(wq[3 * j + 1], x1, k1); v1 = mfma32(x1, wq[3 * j + 2], v1);
        }
        if (jj == 0) {
#pragma unroll
          for (int t = 0; t < 3; ++t) {
            const float d = (lane & 2) ? ((lane & 1) ? gr[t][3] : gr[t][2]) : ((lane & 1) ? gr[t][1] : gr[t][0]);
            if ((l15 >> 2) == g) *(LAS v2f*)(L + L_SP + (t * 16 + l15) * 32 + w * 8) = v2f{sm[t][0], d};
          }
        }
        if (jj == 1) LBAR();
        if (jj == 2) {
          const float mean = (p0[0] + p0[2] + p1[0] + p1[2]) * (1.f / 512.f);
          const float var = fmaxf((p0[1] + p0[3] + p1[1] + p1[3]) * (1.f / 512.f) - mean * mean, 0.f);
          const float a = __builtin_amdgcn_rsqf(var + 1e-5f);
          if (lane < 48) { *(LAS float*)(L + L_SF + w * 384 + lane * 4) = a; *(LAS float*)(L + L_SF + w * 384 + 192 + lane * 4) = -mean * a; }
        }
      }
      resB = resA;
      {
        const int pp = 16 * w + (lane >> 2), c = 16 * cg + (pp >> 2);
#pragma unroll
        for (int j = 0; j < 4; ++j) {
          const int tok = 4 * (lane & 3) + j, ft = tok ^ ((tok >> 3) << 1);
          resA[j] = *(LAS unsigned*)(XI + (1 + sel) * 16384 + tok * 1024 + ((c ^ ft) * 16) + (pp & 3) * 4);
        }
      }
      const float QS = 0.25f * 1.44269504088896341f;
      auto fixT = [&](v4f a, v4f cs, float sa, float sb) { const v4f r = a * sa + cs * sb; return v2u{pk2(r[0], r[1]), pk2(r[2], r[3])}; };
      auto fixR = [&](v4f a, v4f sa, v4f sb) { const v4f r = a * sa + sb * csv; return v2u{pk2(r[0], r[1]), pk2(r[2], r[3])}; };
      const v2u kb[3] = {fixT(kH, csk, aT[0], bT[0]), fixT(k0, csk, aT[1], bT[1]), fixT(k1, csk, aT[2], bT[2])};
      const v2u qb[2] = {fixT(q0, csq, aT[1] * QS, bT[1] * QS), fixT(q1, csq, aT[2] * QS, bT[2] * QS)};
      const v2u vb[3] = {fixR(vH, aR[0], bR[0]), fixR(v0, aR[1], bR[1]), fixR(v1, aR[2], bR[2])};
      v2u ob[2];
#pragma unroll
      for (int t = 0; t < 2; ++t) {
        const v4f sP = mfma16(kb[t], qb[t], Z4), sC = mfma16(kb[t + 1], qb[t], Z4);
        float p0s = par ? sP[1] : sP[0], p1s = par ? sP[3] : sP[2];
        if (t == 0 && first) { p0s = -1.44e9f; p1s = -1.44e9f; }
        const float s0 = cur0 ? (par ? sC[1] : sC[0]) : p0s, s1 = cur1 ? (par ? sC[3] : sC[2]) : p1s;
        const float mxv = xmax(fmaxf(s0, s1));
        const float e0 = __builtin_amdgcn_exp2f(s0 - mxv), e1 = __builtin_amdgcn_exp2f(s1 - mxv);
        const unsigned pk = pk2(e0, e1);
        const float inv = __builtin_amdgcn_rcpf(xsum(bflo(pk) + bfhi(pk)));
        const v4u pf = {__builtin_amdgcn_perm(0u, pk, sel0), __builtin_amdgcn_perm(0u, pk, sel1), __builtin_amdgcn_perm(0u, pk, sel2), __builtin_amdgcn_perm(0u, pk, sel3)};
        const v4f o = mfma32(v4u{vb[t].x, vb[t].y, vb[t + 1].x, vb[t + 1].y}, pf, Z4) * inv;
        ob[t] = v2u{pk2(o[0], o[1]), pk2(o[2], o[3])};
      }
      {
        auto rx0 = __builtin_amdgcn_permlane32_swap(ob[0].x, ob[1].x, false, false);
        auto ry0 = __builtin_amdgcn_permlane32_swap(ob[0].y, ob[1].y, false, false);
        st16<SC1>(nosent(v4u{rx0[0], ry0[0], rx0[1], ry0[1]}), rws, (lane >> 5) * 16384 + (lane & 31) * 16, WS_O + layer * MB2 + tile0 * 16384 + head * 512);
      }
      STAMP(9 + layer * 24 + h * 2);
    }
#pragma clang loop unroll(disable)
    for (int h = 0; h < 2; ++h) {
      FRESH();
      const int tile = 64 * h + 2 * grp + sel;
      v4u of[4];
      for (unsigned sp = 0;; ++sp) {
        asm volatile("" ::: "memory");
        unsigned m = 0;
#pragma unroll
        for (int i = 0; i < 4; ++i) of[i] = ld16<SC1>(rws, lane * 16 + i * 1024, WS_O + layer * MB2 + tile * 16384 + w * 4096);
#pragma unroll
        for (int i = 0; i < 4; ++i) m = mx4(m, of[i]);
        if (!__any(m == SENT)) break;
        if (sp >= cap) { cap = 0; break; }
      }
      STAMP(12 + layer * 24 + h * 2);
      if (h == 0) LBAR();
#pragma unroll
      for (int c = 0; c < 8; ++c) {
        v4f a = Z4;
#pragma unroll
        for (int ks = 0; ks < 4; ++ks) a = mfma32(of[ks], wo[ks * 8 + c], a);
        *(LAS v4f*)(L + L_RED + w * 8192 + c * 1024 + g * 256 + ((l15 ^ (g + 4 * (c >> 1))) * 16)) = a;
      }
      LBAR();
      v4f y0, y1;
      {
        lds_t rb = L + L_RED + ((lane >> 2) & 3) * 2048 + (lane & 3) * 256 + (((4 * w + g) ^ l15) * 16);
        y0 = (lr16f(rb) + lr16f(rb + 8192)) + (lr16f(rb + 16384) + lr16f(rb + 24576));
        y1 = (lr16f(rb + 1024) + lr16f(rb + 9216)) + (lr16f(rb + 17408) + lr16f(rb + 25600));
      }
      v4u xo;
      float s[4], q[4];
#pragma unroll
      for (int j = 0; j < 4; ++j) {
        const float a = y0[j] + bflo(resB[j]), b = y1[j] + bfhi(resB[j]);
        xo[j] = pk2(a, b); s[j] = a + b; q[j] = a * a + b * b;
      }
      resB = resA;
      st16<SC1>(nosent(xo), rws, (lane >> 2) * 8192 + (lane & 3) * 16, WS_X1 + layer * MB2 + (64 * cg + 16 * w) * 8192 + tile * 64);
      {
        float sr = fold16(fold32(s[0], s[2]), fold32(s[1], s[3])), qr = fold16(fold32(q[0], q[2]), fold32(q[1], q[3]));
        sr = dppadd<0x128>(sr); qr = dppadd<0x128>(qr);
        sr = dppadd<0x124>(sr); qr = dppadd<0x124>(qr);
        if ((lane & 12) == 0) *(LAS v2f*)(L + L_S2 + (4 * (lane & 3) + g) * 32 + w * 8) = v2f{sr, qr};
      }
      LBAR();
      if (tid < 16) {
        const v4f p0 = lr16f(L + L_S2 + tid * 32), p1 = lr16f(L + L_S2 + tid * 32 + 16);
        const float mean = (p0[0] + p0[2] + p1[0] + p1[2]) * (1.f / 128.f);
        const float var = fmaxf((p0[1] + p0[3] + p1[1] + p1[3]) * (1.f / 128.f) - mean * mean, 0.f);
        const unsigned lo = pk2(mean, mean) & 0xFFFFu;
        const unsigned vb_ = asu(var) + 0x8000u;
        const unsigned d = ((vb_ < lo ? 0u : vb_ - lo) & 0xFFFF0000u) | lo;
        __builtin_amdgcn_raw_buffer_store_b32(min(d, SENT - 1), rws, tid * 4, WS_ST + layer * 32768 + cg * 8192 + tile * 64, SC1);
      }
      STAMP(13 + layer * 24 + h * 2);
    }
    v4f cD[4] = {Z4, Z4, Z4, Z4};
    v4u kf[16];
    lds_t kp;
    {
      FRESH();
      kp = L + L_KC + ((w >> 1) * 4 + (l15 & 3)) * KC_STR + 2 * (2028 - 32 * (w & 1) - (l15 & 12) + 8 * g) - 128 * 31;
#pragma unroll
      for (int a = 0; a < 16; ++a) { const v2u lo = lr8(kp + 128 * (31 - a)), hi = lr8(kp + 128 * (31 - a) + 8); kf[a] = v4u{lo.x, lo.y, hi.x, hi.y}; if (a < 10) asm volatile("" : "+a"(kf[a])); }
    }
#pragma clang loop unroll(disable)
    for (int h = 0; h < 2; ++h) {
      FRESH();
      v4u xd, st[4];
      for (unsigned sp = 0;; ++sp) {
        asm volatile("" ::: "memory");
        xd = ld16<SC1>(rws, tid * 16, WS_X1 + layer * MB2 + wg * 8192 + h * 4096);
#pragma unroll
        for (int i = 0; i < 4; ++i) st[i] = ld16<SC1>(rws, tid * 16, WS_ST + layer * 32768 + i * 8192 + h * 4096);
        unsigned m = mx4(0, xd);
#pragma unroll
        for (int i = 0; i < 4; ++i) m = mx4(m, st[i]);
        if (!__any(m == SENT)) break;
        if (sp >= cap) { cap = 0; break; }
      }
      STAMP(16 + layer * 24 + h * 3);
      float u0[4], u1[4], b0[4], b1[4];
#pragma unroll
      for (int j = 0; j < 4; ++j) {
        const float m0 = bflo(st[0][j]), m1 = bflo(st[1][j]), m2 = bflo(st[2][j]), m3 = bflo(st[3][j]);
        const float mean = ((m0 + m1) + (m2 + m3)) * 0.25f;
        const float t = ((m0 * m0 + asf(st[0][j])) + (m1 * m1 + asf(st[1][j]))) + ((m2 * m2 + asf(st[2][j])) + (m3 * m3 + asf(st[3][j])));
        const float a = __builtin_amdgcn_rsqf(fmaxf(t * 0.25f - mean * mean, 0.f) + 1e-5f);
        const float x0 = bflo(xd[j]), x1 = bfhi(xd[j]);
        u0[j] = (x0 - mean) * a; u1[j] = (x1 - mean) * a;
        b0[j] = x0 + u0[j] * dsk0; b1[j] = x1 + u1[j] * dsk1;
      }
      *(LAS v2u*)(L + L_U + 480 + h * 2048 + tid * 8) = v2u{pk2(u0[0], u0[1]), pk2(u0[2], u0[3])};
      *(LAS v2u*)(L + L_U + U_STR + 480 + h * 2048 + tid * 8) = v2u{pk2(u1[0], u1[1]), pk2(u1[2], u1[3])};
      LBAR();
      lds_t ub = L + L_U + (w >> 1) * U_STR + 480 - 32 - 64 * (6 + (w & 1)) + l15 * 32 + g * 16;
#pragma unroll
      for (int e = 0; e < 16; ++e) {
        const v4u bf = lr16(ub + h * 2048 + 128 * e);
#pragma unroll
        for (int i = 0; i < 4; ++i) { const int a = 4 * i + 3 - e; if (a >= 0) cD[i] = mfma32(kf[a], bf, cD[i]); }
      }
#pragma unroll
      for (int i = 0; i < 4; ++i) *(LAS v4f*)(L + L_RED + w * 4096 + i * 1024 + g * 256 + ((l15 ^ (4 * g)) * 16)) = cD[i];
      LBAR();
      {
        lds_t rb = L + L_RED + w * 1024 + (lane & 3) * 256 + (((lane >> 2) ^ (4 * (lane & 3))) * 16);
        const v4f y0 = lr16f(rb) + lr16f(rb + 4096), y1 = lr16f(rb + 8192) + lr16f(rb + 12288);
        v4u xo;
#pragma unroll
        for (int j = 0; j < 4; ++j) xo[j] = pk2(b0[j] + y0[j], b1[j] + y1[j]);
        if (layer == 0) {
          const int slot = (wg & ~63) + 16 * (wg & 3) + ((wg >> 2) & 15);
          st16<SC1>(nosent(xo), rws, (tid >> 2) * 16384 + (tid & 3) * 16, WS_X2 + h * (64 * 16384) + slot * 64);
        } else {
#pragma unroll
          for (int j = 0; j < 4; ++j) __builtin_amdgcn_raw_buffer_store_b32(xo[j], rout, tid * 4096 + j * 1024, h * (1024 * 1024) + wg * 4, 0);
        }
      }
      STAMP(17 + layer * 24 + h * 3);
      if (h == 0) {
#pragma unroll
        for (int i = 0; i < 4; ++i) cD[i] = Z4;
#pragma unroll
        for (int rho = 0; rho < 4; ++rho) {
          v4u ku[4];
#pragma unroll
          for (int u = 0; u < 4; ++u) { const int a = 19 - rho + 4 * u; const v2u lo = lr8(kp + 128 * (31 - a)), hi = lr8(kp + 128 * (31 - a) + 8); ku[u] = v4u{lo.x, lo.y, hi.x, hi.y}; }
#pragma unroll
          for (int ee = 0; ee < 4; ++ee) {
            const int e = rho + 4 * ee;
            const v4u bf = lr16(ub + 128 * e);
#pragma unroll
            for (int i = 0; i < 4; ++i) { const int a = 4 * i + 19 - e; cD[i] = mfma32(a >= 16 ? ku[i - ee] : kf[a], bf, cD[i]); }
          }
        }
        STAMP(18 + layer * 24);
      }
    }
  }
  STAMP(5);
}

extern "C" void kernel_launch(void* const* d_in, const int* in_sizes, int n_in, void* d_out, int out_size, void* d_ws, size_t ws_size,
                              hipStream_t stream) {
  Params p;
  p.x = (const u16*)d_in[0]; p.wqkv = (const u16*)d_in[1]; p.wo = (const u16*)d_in[2];
  p.lar = (const u16*)d_in[4]; p.aim = (const u16*)d_in[5]; p.cre = (const u16*)d_in[6]; p.cim = (const u16*)d_in[7];
  p.ldt = (const u16*)d_in[8]; p.dsk = (const u16*)d_in[9];
  p.out = (u16*)d_out; p.ws = (unsigned char*)d_ws;
  (void)hipFuncSetAttribute((const void*)fused_ab2, hipFuncAttributeMaxDynamicSharedMemorySize, L_END);
  fused_ab2<<<256, 256, L_END, stream>>>(p);
}
```

```cpp
#include <hip/hip_runtime.h>
#include <hip/hip_bf16.h>
#include <stdint.h>

struct Params {
  const void *x, *wqkv, *wo, *lar, *aim, *cre, *cim, *ldt, *dsk;
  void* out;
  void* ws;
};

static __device__ const unsigned thr_tab[15360] __attribute__((aligned(128))) = {
0,0,149568,149504,16,4160,149572,149508,32,8352,149568,149568,48,12512,149572,149572,64,16,149568,149568,80,4176,149572,149572,96,8368,149568,149568,112,12528,149572,149572,
128,32,149568,149568,144,4192,149572,149572,160,8320,149568,149568,176,12480,149572,149572,192,48,149568,149568,208,4208,149572,149572,224,8336,149568,149568,240,12496,149572,149572,
256,64,149520,149520,272,4096,149524,149524,288,8416,149520,149520,304,12448,149524,149524,320,80,149584,149520,336,4112,149588,149524,352,8432,149584,149584,368,12464,149588,149588,
384,96,149584,149584,400,4128,149588,149588,416,8384,149584,149584,432,12416,149588,149588,448,112,149584,149584,464,4144,149588,149588,480,8400,149584,149584,496,12432,149588,149588,
16384,128,149536,149536,16400,4288,149540,149540,16416,8224,149536,149536,16432,12384,149540,149540,16448,144,149536,149536,16464,4304,149540,149540,16480,8240,149536,149536,16496,12400,149540,149540,
16512,160,149600,149536,16528,4320,149604,149540,16544,8192,149600,149600,16560,12352,149604,149604,16576,176,149600,149600,16592,4336,149604,149604,16608,8208,149600,149600,16624,12368,149604,149604,
16640,192,149552,149552,16656,4224,149556,149556,16672,8288,149552,149552,16688,12320,149556,149556,16704,208,149552,149552,16720,4240,149556,149556,16736,8304,149552,149552,16752,12336,149556,149556,
16768,224,149552,149552,16784,4256,149556,149556,16800,8256,149552,149552,16816,12288,149556,149556,16832,240,149616,149552,16848,4272,149620,149556,16864,8272,149616,149616,16880,12304,149620,149620,
0,256,150272,150208,16,4416,150276,150212,32,8608,150272,150272,48,12768,150276,150276,64,272,150272,150272,80,4432,150276,150276,96,8624,150272,150272,112,12784,150276,150276,
128,288,150272,150272,144,4448,150276,150276,160,8576,150272,150272,176,12736,150276,150276,192,304,150272,150272,208,4464,150276,150276,224,8592,150272,150272,240,12752,150276,150276,
256,320,150224,150224,272,4352,150228,150228,288,8672,150224,150224,304,12704,150228,150228,320,336,150288,150224,336,4368,150292,150228,352,8688,150288,150288,368,12720,150292,150292,
384,352,150288,150288,400,4384,150292,150292,416,8640,150288,150288,432,12672,150292,150292,448,368,150288,150288,464,4400,150292,150292,480,8656,150288,150288,496,12688,150292,150292,
16384,384,150240,150240,16400,4544,150244,150244,16416,8480,150240,150240,16432,12640,150244,150244,16448,400,150240,150240,16464,4560,150244,150244,16480,8496,150240,150240,16496,12656,150244,150244,
16512,416,150304,150240,16528,4576,150308,150244,16544,8448,150304,150304,16560,12608,150308,150308,16576,432,150304,150304,16592,4592,150308,150308,16608,8464,150304,150304,16624,12624,150308,150308,
16640,448,150256,150256,16656,4480,150260,150260,16672,8544,150256,150256,16688,12576,150260,150260,16704,464,150256,150256,16720,4496,150260,150260,16736,8560,150256,150256,16752,12592,150260,150260,
16768,480,150256,150256,16784,4512,150260,150260,16800,8512,150256,150256,16816,12544,150260,150260,16832,496,150320,150256,16848,4528,150324,150260,16864,8528,150320,150320,16880,12560,150324,150324,
0,512,150976,150912,16,4672,150980,150916,32,8864,150976,150976,48,13024,150980,150980,64,528,150976,150976,80,4688,150980,150980,96,8880,150976,150976,112,13040,150980,150980,
128,544,150976,150976,144,4704,150980,150980,160,8832,150976,150976,176,12992,150980,150980,192,560,150976,150976,208,4720,150980,150980,224,8848,150976,150976,240,13008,150980,150980,
256,576,150928,150928,272,4608,150932,150932,288,8928,150928,150928,304,12960,150932,150932,320,592,150992,150928,336,4624,150996,150932,352,8944,150992,150992,368,12976,150996,150996,
384,608,150992,150992,400,4640,150996,150996,416,8896,150992,150992,432,12928,150996,150996,448,624,150992,150992,464,4656,150996,150996,480,8912,150992,150992,496,12944,150996,150996,
16384,640,150944,150944,16400,4800,150948,150948,16416,8736,150944,150944,16432,12896,150948,150948,16448,656,150944,150944,16464,4816,150948,150948,16480,8752,150944,150944,16496,12912,150948,150948,
16512,672,151008,150944,16528,4832,151012,150948,16544,8704,151008,151008,16560,12864,151012,151012,16576,688,151008,151008,16592,4848,151012,151012,16608,8720,151008,151008,16624,12880,151012,151012,
16640,704,150960,150960,16656,4736,150964,150964,16672,8800,150960,150960,16688,12832,150964,150964,16704,720,150960,150960,16720,4752,150964,150964,16736,8816,150960,150960,16752,12848,150964,150964,
16768,736,150960,150960,16784,4768,150964,150964,16800,8768,150960,150960,16816,12800,150964,150964,16832,752,151024,150960,16848,4784,151028,150964,16864,8784,151024,151024,16880,12816,151028,151028,
0,768,151680,151616,16,4928,151684,151620,32,9120,151680,151680,48,13280,151684,151684,64,784,151680,151680,80,4944,151684,151684,96,9136,151680,151680,112,13296,151684,151684,
128,800,151680,151680,144,4960,151684,151684,160,9088,151680,151680,176,13248,151684,151684,192,816,151680,151680,208,4976,151684,151684,224,9104,151680,151680,240,13264,151684,151684,
256,832,151632,151632,272,4864,151636,151636,288,9184,151632,151632,304,13216,151636,151636,320,848,151696,151632,336,4880,151700,151636,352,9200,151696,151696,368,13232,151700,151700,
384,864,151696,151696,400,4896,151700,151700,416,9152,151696,151696,432,13184,151700,151700,448,880,151696,151696,464,4912,151700,151700,480,9168,151696,151696,496,13200,151700,151700,
16384,896,151648,151648,16400,5056,151652,151652,16416,8992,151648,151648,16432,13152,151652,151652,16448,912,151648,151648,16464,5072,151652,151652,16480,9008,151648,151648,16496,13168,151652,151652,
16512,928,151712,151648,16528,5088,151716,151652,16544,8960,151712,151712,16560,13120,151716,151716,16576,944,151712,151712,16592,5104,151716,151716,16608,8976,151712,151712,16624,13136,151716,151716,
16640,960,151664,151664,16656,4992,151668,151668,16672,9056,151664,151664,16688,13088,151668,151668,16704,976,151664,151664,16720,5008,151668,151668,16736,9072,151664,151664,16752,13104,151668,151668,
16768,992,151664,151664,16784,5024,151668,151668,16800,9024,151664,151664,16816,13056,151668,151668,16832,1008,151728,151664,16848,5040,151732,151668,16864,9040,151728,151728,16880,13072,151732,151732,
147456,149504,149504,149504,147472,149508,149508,149504,147488,149512,149512,149504,147504,149516,149516,149504,152832,149520,149520,149504,152832,149524,149524,149504,152832,149528,149528,149504,152832,149532,149532,149504,
152832,149536,149536,149504,152832,149540,149540,149504,152832,149544,149544,149504,152832,149548,149548,149504,152832,149552,149552,149504,152832,149556,149556,149504,152832,149560,149560,149504,152832,149564,149564,149504,
152832,149568,149504,149520,152832,149572,149508,149520,152832,149576,149512,149520,152832,149580,149516,149520,147520,149584,149520,149520,147536,149588,149524,149520,147552,149592,149528,149520,147568,149596,149532,149520,
152832,149600,149536,149520,152832,149604,149540,149520,152832,149608,149544,149520,152832,149612,149548,149520,152832,149616,149552,149520,152832,149620,149556,149520,152832,149624,149560,149520,152832,149628,149564,149520,
152832,149632,149504,149536,152832,149636,149508,149536,152832,149640,149512,149536,152832,149644,149516,149536,152832,149648,149520,149536,152832,149652,149524,149536,152832,149656,149528,149536,152832,149660,149532,149536,
147584,149664,149536,149536,147600,149668,149540,149536,147616,149672,149544,149536,147632,149676,149548,149536,152832,149680,149552,149536,152832,149684,149556,149536,152832,149688,149560,149536,152832,149692,149564,149536,
152832,149696,149504,149552,152832,149700,149508,149552,152832,149704,149512,149552,152832,149708,149516,149552,152832,149712,149520,149552,152832,149716,149524,149552,152832,149720,149528,149552,152832,149724,149532,149552,
152832,149728,149536,149552,152832,149732,149540,149552,152832,149736,149544,149552,152832,149740,149548,149552,147648,149744,149552,149552,147664,149748,149556,149552,147680,149752,149560,149552,147696,149756,149564,149552,
147464,150208,150208,150208,147480,150212,150212,150208,147496,150216,150216,150208,147512,150220,150220,150208,152832,150224,150224,150208,152832,150228,150228,150208,152832,150232,150232,150208,152832,150236,150236,150208,
152832,150240,150240,150208,152832,150244,150244,150208,152832,150248,150248,150208,152832,150252,150252,150208,152832,150256,150256,150208,152832,150260,150260,150208,152832,150264,150264,150208,152832,150268,150268,150208,
152832,150272,150208,150224,152832,150276,150212,150224,152832,150280,150216,150224,152832,150284,150220,150224,147528,150288,150224,150224,147544,150292,150228,150224,147560,150296,150232,150224,147576,150300,150236,150224,
152832,150304,150240,150224,152832,150308,150244,150224,152832,150312,150248,150224,152832,150316,150252,150224,152832,150320,150256,150224,152832,150324,150260,150224,152832,150328,150264,150224,152832,150332,150268,150224,
152832,150336,150208,150240,152832,150340,150212,150240,152832,150344,150216,150240,152832,150348,150220,150240,152832,150352,150224,150240,152832,150356,150228,150240,152832,150360,150232,150240,152832,150364,150236,150240,
147592,150368,150240,150240,147608,150372,150244,150240,147624,150376,150248,150240,147640,150380,150252,150240,152832,150384,150256,150240,152832,150388,150260,150240,152832,150392,150264,150240,152832,150396,150268,150240,
152832,150400,150208,150256,152832,150404,150212,150256,152832,150408,150216,150256,152832,150412,150220,150256,152832,150416,150224,150256,152832,150420,150228,150256,152832,150424,150232,150256,152832,150428,150236,150256,
152832,150432,150240,150256,152832,150436,150244,150256,152832,150440,150248,150256,152832,150444,150252,150256,147656,150448,150256,150256,147672,150452,150260,150256,147688,150456,150264,150256,147704,150460,150268,150256,
148480,150912,150912,150912,148496,150916,150916,150912,148512,150920,150920,150912,148528,150924,150924,150912,152832,150928,150928,150912,152832,150932,150932,150912,152832,150936,150936,150912,152832,150940,150940,150912,
152832,150944,150944,150912,152832,150948,150948,150912,152832,150952,150952,150912,152832,150956,150956,150912,152832,150960,150960,150912,152832,150964,150964,150912,152832,150968,150968,150912,152832,150972,150972,150912,
152832,150976,150912,150928,152832,150980,150916,150928,152832,150984,150920,150928,152832,150988,150924,150928,148544,150992,150928,150928,148560,150996,150932,150928,148576,151000,150936,150928,148592,151004,150940,150928,
152832,151008,150944,150928,152832,151012,150948,150928,152832,151016,150952,150928,152832,151020,150956,150928,152832,151024,150960,150928,152832,151028,150964,150928,152832,151032,150968,150928,152832,151036,150972,150928,
152832,151040,150912,150944,152832,151044,150916,150944,152832,151048,150920,150944,152832,151052,150924,150944,152832,151056,150928,150944,152832,151060,150932,150944,152832,151064,150936,150944,152832,151068,150940,150944,
148608,151072,150944,150944,148624,151076,150948,150944,148640,151080,150952,150944,148656,151084,150956,150944,152832,151088,150960,150944,152832,151092,150964,150944,152832,151096,150968,150944,152832,151100,150972,150944,
152832,151104,150912,150960,152832,151108,150916,150960,152832,151112,150920,150960,152832,151116,150924,150960,152832,151120,150928,150960,152832,151124,150932,150960,152832,151128,150936,150960,152832,151132,150940,150960,
152832,151136,150944,150960,152832,151140,150948,150960,152832,151144,150952,150960,152832,151148,150956,150960,148672,151152,150960,150960,148688,151156,150964,150960,148704,151160,150968,150960,148720,151164,150972,150960,
148488,151616,151616,151616,148504,151620,151620,151616,148520,151624,151624,151616,148536,151628,151628,151616,152832,151632,151632,151616,152832,151636,151636,151616,152832,151640,151640,151616,152832,151644,151644,151616,
152832,151648,151648,151616,152832,151652,151652,151616,152832,151656,151656,151616,152832,151660,151660,151616,152832,151664,151664,151616,152832,151668,151668,151616,152832,151672,151672,151616,152832,151676,151676,151616,
152832,151680,151616,151632,152832,151684,151620,151632,152832,151688,151624,151632,152832,151692,151628,151632,148552,151696,151632,151632,148568,151700,151636,151632,148584,151704,151640,151632,148600,151708,151644,151632,
152832,151712,151648,151632,152832,151716,151652,151632,152832,151720,151656,151632,152832,151724,151660,151632,152832,151728,151664,151632,152832,151732,151668,151632,152832,151736,151672,151632,152832,151740,151676,151632,
152832,151744,151616,151648,152832,151748,151620,151648,152832,151752,151624,151648,152832,151756,151628,151648,152832,151760,151632,151648,152832,151764,151636,151648,152832,151768,151640,151648,152832,151772,151644,151648,
148616,151776,151648,151648,148632,151780,151652,151648,148648,151784,151656,151648,148664,151788,151660,151648,152832,151792,151664,151648,152832,151796,151668,151648,152832,151800,151672,151648,152832,151804,151676,151648,
152832,151808,151616,151664,152832,151812,151620,151664,152832,151816,151624,151664,152832,151820,151628,151664,152832,151824,151632,151664,152832,151828,151636,151664,152832,151832,151640,151664,152832,151836,151644,151664,
152832,151840,151648,151664,152832,151844,151652,151664,152832,151848,151656,151664,152832,151852,151660,151664,148680,151856,151664,151664,148696,151860,151668,151664,148712,151864,151672,151664,148728,151868,151676,151664,
202116108,202113794,202113280,202116108,202116108,50465804,16780300,202116108,202116108,202116108,202113280,202113794,202116108,202116108,16780300,50465804,202116108,202116108,202113280,202113794,202116108,202116108,16780300,50465804,202116108,202116108,202113280,202113794,202116108,202116108,16780300,50465804,
202116108,202116108,202113280,202113794,202116108,202116108,16780300,50465804,202116108,202116108,202113280,202113794,202116108,202116108,16780300,50465804,202116108,202116108,202113280,202113794,202116108,202116108,16780300,50465804,202116108,202116108,202113280,202113794,202116108,202116108,16780300,50465804,
202113280,202113794,202116108,202116108,16780300,50465804,202116108,202116108,202113280,202113794,202116108,202116108,16780300,50465804,202116108,202116108,202116108,202113794,202113280,202116108,202116108,50465804,16780300,202116108,202116108,202116108,202113280,202113794,202116108,202116108,16780300,50465804,
202116108,202116108,202113280,202113794,202116108,202116108,16780300,50465804,202116108,202116108,202113280,202113794,202116108,202116108,16780300,50465804,202116108,202116108,202113280,202113794,202116108,202116108,16780300,50465804,202116108,202116108,202113280,202113794,202116108,202116108,16780300,50465804,
202113280,202113794,202116108,202116108,16780300,50465804,202116108,202116108,202113280,202113794,202116108,202116108,16780300,50465804,202116108,202116108,202113280,202113794,202116108,202116108,16780300,50465804,202116108,202116108,202113280,202113794,202116108,202116108,16780300,50465804,202116108,202116108,
202116108,202113794,202113280,202116108,202116108,50465804,16780300,202116108,202116108,202116108,202113280,202113794,202116108,202116108,16780300,50465804,202116108,202116108,202113280,202113794,202116108,202116108,16780300,50465804,202116108,202116108,202113280,202113794,202116108,202116108,16780300,50465804,
202113280,202113794,202116108,202116108,16780300,50465804,202116108,202116108,202113280,202113794,202116108,202116108,16780300,50465804,202116108,202116108,202113280,202113794,202116108,202116108,16780300,50465804,202116108,202116108,202113280,202113794,202116108,202116108,16780300,50465804,202116108,202116108,
202113280,202113794,202116108,202116108,16780300,50465804,202116108,202116108,202113280,202113794,202116108,202116108,16780300,50465804,202116108,202116108,202116108,202113794,202113280,202116108,202116108,50465804,16780300,202116108,202116108,202116108,202113280,202113794,202116108,202116108,16780300,50465804,
202116108,202113794,202113280,202116108,202116108,50465804,16780300,202116108,202116108,202116108,202113280,202113794,202116108,202116108,16780300,50465804,202116108,202116108,202113280,202113794,202116108,202116108,16780300,50465804,202116108,202116108,202113280,202113794,202116108,202116108,16780300,50465804,
202116108,202116108,202113280,202113794,202116108,202116108,16780300,50465804,202116108,202116108,202113280,202113794,202116108,202116108,16780300,50465804,202116108,202116108,202113280,202113794,202116108,202116108,16780300,50465804,202116108,202116108,202113280,202113794,202116108,202116108,16780300,50465804,
202113280,202113794,202116108,202116108,16780300,50465804,202116108,202116108,202113280,202113794,202116108,202116108,16780300,50465804,202116108,202116108,202116108,202113794,202113280,202116108,202116108,50465804,16780300,202116108,202116108,202116108,202113280,202113794,202116108,202116108,16780300,50465804,
202116108,202116108,202113280,202113794,202116108,202116108,16780300,50465804,202116108,202116108,202113280,202113794,202116108,202116108,16780300,50465804,202116108,202116108,202113280,202113794,202116108,202116108,16780300,50465804,202116108,202116108,202113280,202113794,202116108,202116108,16780300,50465804,
202113280,202113794,202116108,202116108,16780300,50465804,202116108,202116108,202113280,202113794,202116108,202116108,16780300,50465804,202116108,202116108,202113280,202113794,202116108,202116108,16780300,50465804,202116108,202116108,202113280,202113794,202116108,202116108,16780300,50465804,202116108,202116108,
202116108,202113794,202113280,202116108,202116108,50465804,16780300,202116108,202116108,202116108,202113280,202113794,202116108,202116108,16780300,50465804,202116108,202116108,202113280,202113794,202116108,202116108,16780300,50465804,202116108,202116108,202113280,202113794,202116108,202116108,16780300,50465804,
202113280,202113794,202116108,202116108,16780300,50465804,202116108,202116108,202113280,202113794,202116108,202116108,16780300,50465804,202116108,202116108,202113280,202113794,202116108,202116108,16780300,50465804,202116108,202116108,202113280,202113794,202116108,202116108,16780300,50465804,202116108,202116108,
202113280,202113794,202116108,202116108,16780300,50465804,202116108,202116108,202113280,202113794,202116108,202116108,16780300,50465804,202116108,202116108,202116108,202113794,202113280,202116108,202116108,50465804,16780300,202116108,202116108,202116108,202113280,202113794,202116108,202116108,16780300,50465804,
202116108,202113794,202113280,202116108,202116108,50465804,16780300,202116108,202116108,202116108,202113280,202113794,202116108,202116108,16780300,50465804,202116108,202116108,202113280,202113794,202116108,202116108,16780300,50465804,202116108,202116108,202113280,202113794,202116108,202116108,16780300,50465804,
202116108,202116108,202113280,202113794,202116108,202116108,16780300,50465804,202116108,202116108,202113280,202113794,202116108,202116108,16780300,50465804,202116108,202116108,202113280,202113794,202116108,202116108,16780300,50465804,202116108,202116108,202113280,202113794,202116108,202116108,16780300,50465804,
202113280,202113794,202116108,202116108,16780300,50465804,202116108,202116108,202113280,202113794,202116108,202116108,16780300,50465804,202116108,202116108,202116108,202113794,202113280,202116108,202116108,50465804,16780300,202116108,202116108,202116108,202113280,202113794,202116108,202116108,16780300,50465804,
202116108,202116108,202113280,202113794,202116108,202116108,16780300,50465804,202116108,202116108,202113280,202113794,202116108,202116108,16780300,50465804,202116108,202116108,202113280,202113794,202116108,202116108,16780300,50465804,202116108,202116108,202113280,202113794,202116108,202116108,16780300,50465804,
202113280,202113794,202116108,202116108,16780300,50465804,202116108,202116108,202113280,202113794,202116108,202116108,16780300,50465804,202116108,202116108,202113280,202113794,202116108,202116108,16780300,50465804,202116108,202116108,202113280,202113794,202116108,202116108,16780300,50465804,202116108,202116108,
202116108,202113794,202113280,202116108,202116108,50465804,16780300,202116108,202116108,202116108,202113280,202113794,202116108,202116108,16780300,50465804,202116108,202116108,202113280,202113794,202116108,202116108,16780300,50465804,202116108,202116108,202113280,202113794,202116108,202116108,16780300,50465804,
202113280,202113794,202116108,202116108,16780300,50465804,202116108,202116108,202113280,202113794,202116108,202116108,16780300,50465804,202116108,202116108,202113280,202113794,202116108,202116108,16780300,50465804,202116108,202116108,202113280,202113794,202116108,202116108,16780300,50465804,202116108,202116108,
202113280,202113794,202116108,202116108,16780300,50465804,202116108,202116108,202113280,202113794,202116108,202116108,16780300,50465804,202116108,202116108,202116108,202113794,202113280,202116108,202116108,50465804,16780300,202116108,202116108,202116108,202113280,202113794,202116108,202116108,16780300,50465804,
202116108,202113794,202113280,202116108,202116108,50465804,16780300,202116108,202116108,202116108,202113280,202113794,202116108,202116108,16780300,50465804,202116108,202116108,202113280,202113794,202116108,202116108,16780300,50465804,202116108,202116108,202113280,202113794,202116108,202116108,16780300,50465804,
202116108,202116108,202113280,202113794,202116108,202116108,16780300,50465804,202116108,202116108,202113280,202113794,202116108,202116108,16780300,50465804,202116108,202116108,202113280,202113794,202116108,202116108,16780300,50465804,202116108,202116108,202113280,202113794,202116108,202116108,16780300,50465804,
202113280,202113794,202116108,202116108,16780300,50465804,202116108,202116108,202113280,202113794,202116108,202116108,16780300,50465804,202116108,202116108,202116108,202113794,202113280,202116108,202116108,50465804,16780300,202116108,202116108,202116108,202113280,202113794,202116108,202116108,16780300,50465804,
202116108,202116108,202113280,202113794,202116108,202116108,16780300,50465804,202116108,202116108,202113280,202113794,202116108,202116108,16780300,50465804,202116108,202116108,202113280,202113794,202116108,202116108,16780300,50465804,202116108,202116108,202113280,202113794,202116108,202116108,16780300,50465804,
202113280,202113794,202116108,202116108,16780300,50465804,202116108,202116108,202113280,202113794,202116108,202116108,16780300,50465804,202116108,202116108,202113280,202113794,202116108,202116108,16780300,50465804,202116108,202116108,202113280,202113794,202116108,202116108,16780300,50465804,202116108,202116108,
202116108,202113794,202113280,202116108,202116108,50465804,16780300,202116108,202116108,202116108,202113280,202113794,202116108,202116108,16780300,50465804,202116108,202116108,202113280,202113794,202116108,202116108,16780300,50465804,202116108,202116108,202113280,202113794,202116108,202116108,16780300,50465804,
202113280,202113794,202116108,202116108,16780300,50465804,202116108,202116108,202113280,202113794,202116108,202116108,16780300,50465804,202116108,202116108,202113280,202113794,202116108,202116108,16780300,50465804,202116108,202116108,202113280,202113794,202116108,202116108,16780300,50465804,202116108,202116108,
202113280,202113794,202116108,202116108,16780300,50465804,202116108,202116108,202113280,202113794,202116108,202116108,16780300,50465804,202116108,202116108,202116108,202113794,202113280,202116108,202116108,50465804,16780300,202116108,202116108,202116108,202113280,202113794,202116108,202116108,16780300,50465804,
0,49152,0,98304,1040,49160,16,98308,2080,49168,32,98312,3120,49176,48,98316,4160,49280,64,98320,5200,49288,80,98324,6240,49296,96,98328,7280,49304,112,98332,
8352,49440,128,98336,9392,49448,144,98340,10368,49456,160,98344,11408,49464,176,98348,12512,49568,192,98352,13552,49576,208,98356,14528,49584,224,98360,15568,49592,240,98364,
16,53312,4096,98368,1024,53320,4112,98372,2096,53328,4128,98376,3104,53336,4144,98380,4176,53440,4160,98384,5184,53448,4176,98388,6256,53456,4192,98392,7264,53464,4208,98396,
8368,53600,4224,98400,9376,53608,4240,98404,10384,53616,4256,98408,11392,53624,4272,98412,12528,53728,4288,98416,13536,53736,4304,98420,14544,53744,4320,98424,15552,53752,4336,98428,
32,57344,16384,98432,1072,57352,16400,98436,2048,57360,16416,98440,3088,57368,16432,98444,4192,57472,16448,98448,5232,57480,16464,98452,6208,57488,16480,98456,7248,57496,16496,98460,
8320,57632,16512,98464,9360,57640,16528,98468,10400,57648,16544,98472,11440,57656,16560,98476,12480,57760,16576,98480,13520,57768,16592,98484,14560,57776,16608,98488,15600,57784,16624,98492,
48,61504,20480,98496,1056,61512,20496,98500,2064,61520,20512,98504,3072,61528,20528,98508,4208,61632,20544,98512,5216,61640,20560,98516,6224,61648,20576,98520,7232,61656,20592,98524,
8336,61792,20608,98528,9344,61800,20624,98532,10416,61808,20640,98536,11424,61816,20656,98540,12496,61920,20672,98544,13504,61928,20688,98548,14576,61936,20704,98552,15584,61944,20720,98556,
64,50208,131072,98560,1104,50216,131088,98564,2144,50224,131104,98568,3184,50232,131120,98572,4096,50336,131136,98576,5136,50344,131152,98580,6176,50352,131168,98584,7216,50360,131184,98588,
8416,50432,131200,98592,9456,50440,131216,98596,10432,50448,131232,98600,11472,50456,131248,98604,12448,50560,131264,98608,13488,50568,131280,98612,14464,50576,131296,98616,15504,50584,131312,98620,
80,54368,135168,98624,1088,54376,135184,98628,2160,54384,135200,98632,3168,54392,135216,98636,4112,54496,135232,98640,5120,54504,135248,98644,6192,54512,135264,98648,7200,54520,135280,98652,
8432,54592,135296,98656,9440,54600,135312,98660,10448,54608,135328,98664,11456,54616,135344,98668,12464,54720,135360,98672,13472,54728,135376,98676,14480,54736,135392,98680,15488,54744,135408,98684,
96,58400,147456,98688,1136,58408,147472,98692,2112,58416,147488,98696,3152,58424,147504,98700,4128,58528,147520,98704,5168,58536,147536,98708,6144,58544,147552,98712,7184,58552,147568,98716,
8384,58624,147584,98720,9424,58632,147600,98724,10464,58640,147616,98728,11504,58648,147632,98732,12416,58752,147648,98736,13456,58760,147664,98740,14496,58768,147680,98744,15536,58776,147696,98748,
112,62560,151552,98752,1120,62568,151568,98756,2128,62576,151584,98760,3136,62584,151600,98764,4144,62688,151616,98768,5152,62696,151632,98772,6160,62704,151648,98776,7168,62712,151664,98780,
8400,62784,151680,98784,9408,62792,151696,98788,10480,62800,151712,98792,11488,62808,151728,98796,12432,62912,151744,98800,13440,62920,151760,98804,14512,62928,151776,98808,15520,62936,151792,98812,
128,51264,262144,128400,1168,51272,262160,128404,2208,51280,262176,128408,3248,51288,262192,128412,4288,51392,262208,128416,5328,51400,262224,128420,6368,51408,262240,128424,7408,51416,262256,128428,
8224,51552,262272,128432,9264,51560,262288,128436,10240,51568,262304,128440,11280,51576,262320,128444,12384,51680,262336,128448,13424,51688,262352,128452,14400,51696,262368,128456,15440,51704,262384,128460,
144,55296,266240,128464,1152,55304,266256,128468,2224,55312,266272,128472,3232,55320,266288,128476,4304,55424,266304,128480,5312,55432,266320,128484,6384,55440,266336,128488,7392,55448,266352,128492,
8240,55584,266368,128496,9248,55592,266384,128500,10256,55600,266400,128504,11264,55608,266416,128508,12400,55712,266432,128512,13408,55720,266448,128516,14416,55728,266464,128520,15424,55736,266480,128524,
160,59456,278528,128528,1200,59464,278544,128532,2176,59472,278560,128536,3216,59480,278576,128540,4320,59584,278592,128544,5360,59592,278608,128548,6336,59600,278624,128552,7376,59608,278640,128556,
8192,59744,278656,128560,9232,59752,278672,128564,10272,59760,278688,128568,11312,59768,278704,128572,12352,59872,278720,128576,13392,59880,278736,128580,14432,59888,278752,128584,15472,59896,278768,128588,
176,63488,282624,128592,1184,63496,282640,128596,2192,63504,282656,128600,3200,63512,282672,128604,4336,63616,282688,128608,5344,63624,282704,128612,6352,63632,282720,128616,7360,63640,282736,128620,
8208,63776,282752,128624,9216,63784,282768,128628,10288,63792,282784,128632,11296,63800,282800,128636,12368,63904,282816,128640,13376,63912,282832,128644,14448,63920,282848,128648,15456,63928,282864,128652,
192,52320,393216,128656,1232,52328,393232,128660,2272,52336,393248,128664,3312,52344,393264,128668,4224,52448,393280,128672,5264,52456,393296,128676,6304,52464,393312,128680,7344,52472,393328,128684,
8288,52544,393344,128688,9328,52552,393360,128692,10304,52560,393376,128696,11344,52568,393392,128700,12320,52672,393408,128704,13360,52680,393424,128708,14336,52688,393440,128712,15376,52696,393456,128716,
208,56352,397312,128720,1216,56360,397328,128724,2288,56368,397344,128728,3296,56376,397360,128732,4240,56480,397376,128736,5248,56488,397392,128740,6320,56496,397408,128744,7328,56504,397424,128748,
8304,56576,397440,128752,9312,56584,397456,128756,10320,56592,397472,128760,11328,56600,397488,128764,12336,56704,397504,128768,13344,56712,397520,128772,14352,56720,397536,128776,15360,56728,397552,128780,
224,60512,409600,128784,1264,60520,409616,128788,2240,60528,409632,128792,3280,60536,409648,128796,4256,60640,409664,128800,5296,60648,409680,128804,6272,60656,409696,128808,7312,60664,409712,128812,
8256,60736,409728,128816,9296,60744,409744,128820,10336,60752,409760,128824,11376,60760,409776,128828,12288,60864,409792,128832,13328,60872,409808,128836,14368,60880,409824,128840,15408,60888,409840,128844,
240,64544,413696,128848,1248,64552,413712,128852,2256,64560,413728,128856,3264,64568,413744,128860,4272,64672,413760,128864,5280,64680,413776,128868,6288,64688,413792,128872,7296,64696,413808,128876,
8272,64768,413824,128880,9280,64776,413840,128884,10352,64784,413856,128888,11360,64792,413872,128892,12304,64896,413888,128896,13312,64904,413904,128900,14384,64912,413920,128904,15392,64920,413936,128908,
0,0,0,0,16,16,16,128,32,32,32,256,48,48,48,384,8192,16384,1073741824,512,8208,16400,1073741824,640,8224,16416,1073741824,768,8240,16432,1073741824,896,
16384,32768,1073741824,0,16400,32784,1073741824,128,16416,32800,1073741824,256,16432,32816,1073741824,384,24576,49152,1073741824,512,24592,49168,1073741824,640,24608,49184,1073741824,768,24624,49200,1073741824,896,
32768,65536,1073741824,0,32784,65552,1073741824,128,32800,65568,1073741824,256,32816,65584,1073741824,384,40960,81920,1073741824,512,40976,81936,1073741824,640,40992,81952,1073741824,768,41008,81968,1073741824,896,
49152,98304,1073741824,0,49168,98320,1073741824,128,49184,98336,1073741824,256,49200,98352,1073741824,384,57344,114688,1073741824,512,57360,114704,1073741824,640,57376,114720,1073741824,768,57392,114736,1073741824,896,
65536,131072,1073741824,0,65552,131088,1073741824,128,65568,131104,1073741824,256,65584,131120,1073741824,384,73728,147456,1073741824,512,73744,147472,1073741824,640,73760,147488,1073741824,768,73776,147504,1073741824,896,
81920,163840,1073741824,0,81936,163856,1073741824,128,81952,163872,1073741824,256,81968,163888,1073741824,384,90112,180224,1073741824,512,90128,180240,1073741824,640,90144,180256,1073741824,768,90160,180272,1073741824,896,
98304,196608,1073741824,0,98320,196624,1073741824,128,98336,196640,1073741824,256,98352,196656,1073741824,384,106496,212992,1073741824,512,106512,213008,1073741824,640,106528,213024,1073741824,768,106544,213040,1073741824,896,
114688,229376,1073741824,0,114704,229392,1073741824,128,114720,229408,1073741824,256,114736,229424,1073741824,384,122880,245760,1073741824,512,122896,245776,1073741824,640,122912,245792,1073741824,768,122928,245808,1073741824,896,
0,262144,4096,0,16,262160,4112,128,32,262176,4128,256,48,262192,4144,384,8192,278528,1073741824,512,8208,278544,1073741824,640,8224,278560,1073741824,768,8240,278576,1073741824,896,
16384,294912,1073741824,0,16400,294928,1073741824,128,16416,294944,1073741824,256,16432,294960,1073741824,384,24576,311296,1073741824,512,24592,311312,1073741824,640,24608,311328,1073741824,768,24624,311344,1073741824,896,
32768,327680,1073741824,0,32784,327696,1073741824,128,32800,327712,1073741824,256,32816,327728,1073741824,384,40960,344064,1073741824,512,40976,344080,1073741824,640,40992,344096,1073741824,768,41008,344112,1073741824,896,
49152,360448,1073741824,0,49168,360464,1073741824,128,49184,360480,1073741824,256,49200,360496,1073741824,384,57344,376832,1073741824,512,57360,376848,1073741824,640,57376,376864,1073741824,768,57392,376880,1073741824,896,
65536,393216,1073741824,0,65552,393232,1073741824,128,65568,393248,1073741824,256,65584,393264,1073741824,384,73728,409600,1073741824,512,73744,409616,1073741824,640,73760,409632,1073741824,768,73776,409648,1073741824,896,
81920,425984,1073741824,0,81936,426000,1073741824,128,81952,426016,1073741824,256,81968,426032,1073741824,384,90112,442368,1073741824,512,90128,442384,1073741824,640,90144,442400,1073741824,768,90160,442416,1073741824,896,
98304,458752,1073741824,0,98320,458768,1073741824,128,98336,458784,1073741824,256,98352,458800,1073741824,384,106496,475136,1073741824,512,106512,475152,1073741824,640,106528,475168,1073741824,768,106544,475184,1073741824,896,
114688,491520,1073741824,0,114704,491536,1073741824,128,114720,491552,1073741824,256,114736,491568,1073741824,384,122880,507904,1073741824,512,122896,507920,1073741824,640,122912,507936,1073741824,768,122928,507952,1073741824,896,
0,524288,32768,0,16,524304,32784,128,32,524320,32800,256,48,524336,32816,384,8192,540672,1073741824,512,8208,540688,1073741824,640,8224,540704,1073741824,768,8240,540720,1073741824,896,
16384,557056,1073741824,0,16400,557072,1073741824,128,16416,557088,1073741824,256,16432,557104,1073741824,384,24576,573440,1073741824,512,24592,573456,1073741824,640,24608,573472,1073741824,768,24624,573488,1073741824,896,
32768,589824,1073741824,0,32784,589840,1073741824,128,32800,589856,1073741824,256,32816,589872,1073741824,384,40960,606208,1073741824,512,40976,606224,1073741824,640,40992,606240,1073741824,768,41008,606256,1073741824,896,
49152,622592,1073741824,0,49168,622608,1073741824,128,49184,622624,1073741824,256,49200,622640,1073741824,384,57344,638976,1073741824,512,57360,638992,1073741824,640,57376,639008,1073741824,768,57392,639024,1073741824,896,
65536,655360,1073741824,0,65552,655376,1073741824,128,65568,655392,1073741824,256,65584,655408,1073741824,384,73728,671744,1073741824,512,73744,671760,1073741824,640,73760,671776,1073741824,768,73776,671792,1073741824,896,
81920,688128,1073741824,0,81936,688144,1073741824,128,81952,688160,1073741824,256,81968,688176,1073741824,384,90112,704512,1073741824,512,90128,704528,1073741824,640,90144,704544,1073741824,768,90160,704560,1073741824,896,
98304,720896,1073741824,0,98320,720912,1073741824,128,98336,720928,1073741824,256,98352,720944,1073741824,384,106496,737280,1073741824,512,106512,737296,1073741824,640,106528,737312,1073741824,768,106544,737328,1073741824,896,
114688,753664,1073741824,0,114704,753680,1073741824,128,114720,753696,1073741824,256,114736,753712,1073741824,384,122880,770048,1073741824,512,122896,770064,1073741824,640,122912,770080,1073741824,768,122928,770096,1073741824,896,
0,786432,36864,0,16,786448,36880,128,32,786464,36896,256,48,786480,36912,384,8192,802816,1073741824,512,8208,802832,1073741824,640,8224,802848,1073741824,768,8240,802864,1073741824,896,
16384,819200,1073741824,0,16400,819216,1073741824,128,16416,819232,1073741824,256,16432,819248,1073741824,384,24576,835584,1073741824,512,24592,835600,1073741824,640,24608,835616,1073741824,768,24624,835632,1073741824,896,
32768,851968,1073741824,0,32784,851984,1073741824,128,32800,852000,1073741824,256,32816,852016,1073741824,384,40960,868352,1073741824,512,40976,868368,1073741824,640,40992,868384,1073741824,768,41008,868400,1073741824,896,
49152,884736,1073741824,0,49168,884752,1073741824,128,49184,884768,1073741824,256,49200,884784,1073741824,384,57344,901120,1073741824,512,57360,901136,1073741824,640,57376,901152,1073741824,768,57392,901168,1073741824,896,
65536,917504,1073741824,0,65552,917520,1073741824,128,65568,917536,1073741824,256,65584,917552,1073741824,384,73728,933888,1073741824,512,73744,933904,1073741824,640,73760,933920,1073741824,768,73776,933936,1073741824,896,
81920,950272,1073741824,0,81936,950288,1073741824,128,81952,950304,1073741824,256,81968,950320,1073741824,384,90112,966656,1073741824,512,90128,966672,1073741824,640,90144,966688,1073741824,768,90160,966704,1073741824,896,
98304,983040,1073741824,0,98320,983056,1073741824,128,98336,983072,1073741824,256,98352,983088,1073741824,384,106496,999424,1073741824,512,106512,999440,1073741824,640,106528,999456,1073741824,768,106544,999472,1073741824,896,
114688,1015808,1073741824,0,114704,1015824,1073741824,128,114720,1015840,1073741824,256,114736,1015856,1073741824,384,122880,1032192,1073741824,512,122896,1032208,1073741824,640,122912,1032224,1073741824,768,122928,1032240,1073741824,896,
0,0,152320,152320,16,272,152448,152448,32,544,152576,152576,48,816,152704,152704,64,2112,152320,152352,80,2384,152448,152480,96,2656,152576,152608,112,2928,152704,152736,
128,4224,152320,152384,144,4496,152448,152512,160,4768,152576,152640,176,5040,152704,152768,192,6336,152320,152416,208,6608,152448,152544,224,6880,152576,152672,240,7152,152704,152800,
272,16,152352,152320,256,256,152480,152448,304,560,152608,152576,288,800,152736,152704,336,2128,152352,152352,320,2368,152480,152480,368,2672,152608,152608,352,2912,152736,152736,
400,4240,152352,152384,384,4480,152480,152512,432,4784,152608,152640,416,5024,152736,152768,464,6352,152352,152416,448,6592,152480,152544,496,6896,152608,152672,480,7136,152736,152800,
544,32,152384,152320,560,304,152512,152448,512,512,152640,152576,528,784,152768,152704,608,2144,152384,152352,624,2416,152512,152480,576,2624,152640,152608,592,2896,152768,152736,
672,4256,152384,152384,688,4528,152512,152512,640,4736,152640,152640,656,5008,152768,152768,736,6368,152384,152416,752,6640,152512,152544,704,6848,152640,152672,720,7120,152768,152800,
816,48,152416,152320,800,288,152544,152448,784,528,152672,152576,768,768,152800,152704,880,2160,152416,152352,864,2400,152544,152480,848,2640,152672,152608,832,2880,152800,152736,
944,4272,152416,152384,928,4512,152544,152512,912,4752,152672,152640,896,4992,152800,152768,1008,6384,152416,152416,992,6624,152544,152544,976,6864,152672,152672,960,7104,152800,152800,
8192,64,152328,152320,8208,336,152456,152448,8224,608,152584,152576,8240,880,152712,152704,8256,2048,152328,152352,8272,2320,152456,152480,8288,2592,152584,152608,8304,2864,152712,152736,
8320,4288,152328,152384,8336,4560,152456,152512,8352,4832,152584,152640,8368,5104,152712,152768,8384,6272,152328,152416,8400,6544,152456,152544,8416,6816,152584,152672,8432,7088,152712,152800,
8464,80,152360,152320,8448,320,152488,152448,8496,624,152616,152576,8480,864,152744,152704,8528,2064,152360,152352,8512,2304,152488,152480,8560,2608,152616,152608,8544,2848,152744,152736,
8592,4304,152360,152384,8576,4544,152488,152512,8624,4848,152616,152640,8608,5088,152744,152768,8656,6288,152360,152416,8640,6528,152488,152544,8688,6832,152616,152672,8672,7072,152744,152800,
8736,96,152392,152320,8752,368,152520,152448,8704,576,152648,152576,8720,848,152776,152704,8800,2080,152392,152352,8816,2352,152520,152480,8768,2560,152648,152608,8784,2832,152776,152736,
8864,4320,152392,152384,8880,4592,152520,152512,8832,4800,152648,152640,8848,5072,152776,152768,8928,6304,152392,152416,8944,6576,152520,152544,8896,6784,152648,152672,8912,7056,152776,152800,
9008,112,152424,152320,8992,352,152552,152448,8976,592,152680,152576,8960,832,152808,152704,9072,2096,152424,152352,9056,2336,152552,152480,9040,2576,152680,152608,9024,2816,152808,152736,
9136,4336,152424,152384,9120,4576,152552,152512,9104,4816,152680,152640,9088,5056,152808,152768,9200,6320,152424,152416,9184,6560,152552,152544,9168,6800,152680,152672,9152,7040,152808,152800,
49152,128,152336,152320,49168,400,152464,152448,49184,672,152592,152576,49200,944,152720,152704,49216,2240,152336,152352,49232,2512,152464,152480,49248,2784,152592,152608,49264,3056,152720,152736,
49280,4096,152336,152384,49296,4368,152464,152512,49312,4640,152592,152640,49328,4912,152720,152768,49344,6208,152336,152416,49360,6480,152464,152544,49376,6752,152592,152672,49392,7024,152720,152800,
49424,144,152368,152320,49408,384,152496,152448,49456,688,152624,152576,49440,928,152752,152704,49488,2256,152368,152352,49472,2496,152496,152480,49520,2800,152624,152608,49504,3040,152752,152736,
49552,4112,152368,152384,49536,4352,152496,152512,49584,4656,152624,152640,49568,4896,152752,152768,49616,6224,152368,152416,49600,6464,152496,152544,49648,6768,152624,152672,49632,7008,152752,152800,
49696,160,152400,152320,49712,432,152528,152448,49664,640,152656,152576,49680,912,152784,152704,49760,2272,152400,152352,49776,2544,152528,152480,49728,2752,152656,152608,49744,3024,152784,152736,
49824,4128,152400,152384,49840,4400,152528,152512,49792,4608,152656,152640,49808,4880,152784,152768,49888,6240,152400,152416,49904,6512,152528,152544,49856,6720,152656,152672,49872,6992,152784,152800,
49968,176,152432,152320,49952,416,152560,152448,49936,656,152688,152576,49920,896,152816,152704,50032,2288,152432,152352,50016,2528,152560,152480,50000,2768,152688,152608,49984,3008,152816,152736,
50096,4144,152432,152384,50080,4384,152560,152512,50064,4624,152688,152640,50048,4864,152816,152768,50160,6256,152432,152416,50144,6496,152560,152544,50128,6736,152688,152672,50112,6976,152816,152800,
57344,192,152344,152320,57360,464,152472,152448,57376,736,152600,152576,57392,1008,152728,152704,57408,2176,152344,152352,57424,2448,152472,152480,57440,2720,152600,152608,57456,2992,152728,152736,
57472,4160,152344,152384,57488,4432,152472,152512,57504,4704,152600,152640,57520,4976,152728,152768,57536,6144,152344,152416,57552,6416,152472,152544,57568,6688,152600,152672,57584,6960,152728,152800,
57616,208,152376,152320,57600,448,152504,152448,57648,752,152632,152576,57632,992,152760,152704,57680,2192,152376,152352,57664,2432,152504,152480,57712,2736,152632,152608,57696,2976,152760,152736,
57744,4176,152376,152384,57728,4416,152504,152512,57776,4720,152632,152640,57760,4960,152760,152768,57808,6160,152376,152416,57792,6400,152504,152544,57840,6704,152632,152672,57824,6944,152760,152800,
57888,224,152408,152320,57904,496,152536,152448,57856,704,152664,152576,57872,976,152792,152704,57952,2208,152408,152352,57968,2480,152536,152480,57920,2688,152664,152608,57936,2960,152792,152736,
58016,4192,152408,152384,58032,4464,152536,152512,57984,4672,152664,152640,58000,4944,152792,152768,58080,6176,152408,152416,58096,6448,152536,152544,58048,6656,152664,152672,58064,6928,152792,152800,
58160,240,152440,152320,58144,480,152568,152448,58128,720,152696,152576,58112,960,152824,152704,58224,2224,152440,152352,58208,2464,152568,152480,58192,2704,152696,152608,58176,2944,152824,152736,
58288,4208,152440,152384,58272,4448,152568,152512,58256,4688,152696,152640,58240,4928,152824,152768,58352,6192,152440,152416,58336,6432,152568,152544,58320,6672,152696,152672,58304,6912,152824,152800,
0,16384,0,0,16,20544,16,128,32,24736,32,256,48,28896,48,384,1073741824,16388,8192,512,1073741824,20548,8208,640,1073741824,24740,8224,768,1073741824,28900,8240,896,
1073741824,16392,16384,0,1073741824,20552,16400,128,1073741824,24744,16416,256,1073741824,28904,16432,384,1073741824,16396,24576,512,1073741824,20556,24592,640,1073741824,24748,24608,768,1073741824,28908,24624,896,
1073741824,16400,32768,0,1073741824,20560,32784,128,1073741824,24752,32800,256,1073741824,28912,32816,384,1073741824,16404,40960,512,1073741824,20564,40976,640,1073741824,24756,40992,768,1073741824,28916,41008,896,
1073741824,16408,49152,0,1073741824,20568,49168,128,1073741824,24760,49184,256,1073741824,28920,49200,384,1073741824,16412,57344,512,1073741824,20572,57360,640,1073741824,24764,57376,768,1073741824,28924,57392,896,
1073741824,16416,65536,0,1073741824,20576,65552,128,1073741824,24704,65568,256,1073741824,28864,65584,384,1073741824,16420,73728,512,1073741824,20580,73744,640,1073741824,24708,73760,768,1073741824,28868,73776,896,
1073741824,16424,81920,0,1073741824,20584,81936,128,1073741824,24712,81952,256,1073741824,28872,81968,384,1073741824,16428,90112,512,1073741824,20588,90128,640,1073741824,24716,90144,768,1073741824,28876,90160,896,
1073741824,16432,98304,0,1073741824,20592,98320,128,1073741824,24720,98336,256,1073741824,28880,98352,384,1073741824,16436,106496,512,1073741824,20596,106512,640,1073741824,24724,106528,768,1073741824,28884,106544,896,
1073741824,16440,114688,0,1073741824,20600,114704,128,1073741824,24728,114720,256,1073741824,28888,114736,384,1073741824,16444,122880,512,1073741824,20604,122896,640,1073741824,24732,122912,768,1073741824,28892,122928,896,
1073741824,16448,0,0,1073741824,20480,16,128,1073741824,24800,32,256,1073741824,28832,48,384,1073741824,16452,8192,512,1073741824,20484,8208,640,1073741824,24804,8224,768,1073741824,28836,8240,896,
1073741824,16456,16384,0,1073741824,20488,16400,128,1073741824,24808,16416,256,1073741824,28840,16432,384,1073741824,16460,24576,512,1073741824,20492,24592,640,1073741824,24812,24608,768,1073741824,28844,24624,896,
1073741824,16464,32768,0,1073741824,20496,32784,128,1073741824,24816,32800,256,1073741824,28848,32816,384,1073741824,16468,40960,512,1073741824,20500,40976,640,1073741824,24820,40992,768,1073741824,28852,41008,896,
1073741824,16472,49152,0,1073741824,20504,49168,128,1073741824,24824,49184,256,1073741824,28856,49200,384,1073741824,16476,57344,512,1073741824,20508,57360,640,1073741824,24828,57376,768,1073741824,28860,57392,896,
1073741824,16480,65536,0,1073741824,20512,65552,128,1073741824,24768,65568,256,1073741824,28800,65584,384,1073741824,16484,73728,512,1073741824,20516,73744,640,1073741824,24772,73760,768,1073741824,28804,73776,896,
1073741824,16488,81920,0,1073741824,20520,81936,128,1073741824,24776,81952,256,1073741824,28808,81968,384,1073741824,16492,90112,512,1073741824,20524,90128,640,1073741824,24780,90144,768,1073741824,28812,90160,896,
1073741824,16496,98304,0,1073741824,20528,98320,128,1073741824,24784,98336,256,1073741824,28816,98352,384,1073741824,16500,106496,512,1073741824,20532,106512,640,1073741824,24788,106528,768,1073741824,28820,106544,896,
1073741824,16504,114688,0,1073741824,20536,114704,128,1073741824,24792,114720,256,1073741824,28824,114736,384,1073741824,16508,122880,512,1073741824,20540,122896,640,1073741824,24796,122912,768,1073741824,28828,122928,896,
1073741824,16512,0,0,1073741824,20672,16,128,1073741824,24608,32,256,1073741824,28768,48,384,1073741824,16516,8192,512,1073741824,20676,8208,640,1073741824,24612,8224,768,1073741824,28772,8240,896,
1073741824,16520,16384,0,1073741824,20680,16400,128,1073741824,24616,16416,256,1073741824,28776,16432,384,1073741824,16524,24576,512,1073741824,20684,24592,640,1073741824,24620,24608,768,1073741824,28780,24624,896,
1073741824,16528,32768,0,1073741824,20688,32784,128,1073741824,24624,32800,256,1073741824,28784,32816,384,1073741824,16532,40960,512,1073741824,20692,40976,640,1073741824,24628,40992,768,1073741824,28788,41008,896,
1073741824,16536,49152,0,1073741824,20696,49168,128,1073741824,24632,49184,256,1073741824,28792,49200,384,1073741824,16540,57344,512,1073741824,20700,57360,640,1073741824,24636,57376,768,1073741824,28796,57392,896,
1073741824,16544,65536,0,1073741824,20704,65552,128,1073741824,24576,65568,256,1073741824,28736,65584,384,1073741824,16548,73728,512,1073741824,20708,73744,640,1073741824,24580,73760,768,1073741824,28740,73776,896,
1073741824,16552,81920,0,1073741824,20712,81936,128,1073741824,24584,81952,256,1073741824,28744,81968,384,1073741824,16556,90112,512,1073741824,20716,90128,640,1073741824,24588,90144,768,1073741824,28748,90160,896,
1073741824,16560,98304,0,1073741824,20720,98320,128,1073741824,24592,98336,256,1073741824,28752,98352,384,1073741824,16564,106496,512,1073741824,20724,106512,640,1073741824,24596,106528,768,1073741824,28756,106544,896,
1073741824,16568,114688,0,1073741824,20728,114704,128,1073741824,24600,114720,256,1073741824,28760,114736,384,1073741824,16572,122880,512,1073741824,20732,122896,640,1073741824,24604,122912,768,1073741824,28764,122928,896,
1073741824,16576,0,0,1073741824,20608,16,128,1073741824,24672,32,256,1073741824,28704,48,384,1073741824,16580,8192,512,1073741824,20612,8208,640,1073741824,24676,8224,768,1073741824,28708,8240,896,
1073741824,16584,16384,0,1073741824,20616,16400,128,1073741824,24680,16416,256,1073741824,28712,16432,384,1073741824,16588,24576,512,1073741824,20620,24592,640,1073741824,24684,24608,768,1073741824,28716,24624,896,
1073741824,16592,32768,0,1073741824,20624,32784,128,1073741824,24688,32800,256,1073741824,28720,32816,384,1073741824,16596,40960,512,1073741824,20628,40976,640,1073741824,24692,40992,768,1073741824,28724,41008,896,
1073741824,16600,49152,0,1073741824,20632,49168,128,1073741824,24696,49184,256,1073741824,28728,49200,384,1073741824,16604,57344,512,1073741824,20636,57360,640,1073741824,24700,57376,768,1073741824,28732,57392,896,
1073741824,16608,65536,0,1073741824,20640,65552,128,1073741824,24640,65568,256,1073741824,28672,65584,384,1073741824,16612,73728,512,1073741824,20644,73744,640,1073741824,24644,73760,768,1073741824,28676,73776,896,
1073741824,16616,81920,0,1073741824,20648,81936,128,1073741824,24648,81952,256,1073741824,28680,81968,384,1073741824,16620,90112,512,1073741824,20652,90128,640,1073741824,24652,90144,768,1073741824,28684,90160,896,
1073741824,16624,98304,0,1073741824,20656,98320,128,1073741824,24656,98336,256,1073741824,28688,98352,384,1073741824,16628,106496,512,1073741824,20660,106512,640,1073741824,24660,106528,768,1073741824,28692,106544,896,
1073741824,16632,114688,0,1073741824,20664,114704,128,1073741824,24664,114720,256,1073741824,28696,114736,384,1073741824,16636,122880,512,1073741824,20668,122896,640,1073741824,24668,122912,768,1073741824,28700,122928,896,
132064,131648,49152,49152,132072,131680,49168,49472,132080,131712,49184,49792,132088,131744,49200,50112,132096,131776,49216,49168,132104,131808,49232,49488,132112,131840,49248,49808,132120,131872,49264,50128,
132128,131904,49280,49184,132136,131936,49296,49504,132144,131968,49312,49824,132152,132000,49328,50144,132160,132032,49344,49200,132168,132064,49360,49520,132176,132096,49376,49840,132184,132128,49392,50160,
132192,131664,49472,49216,132200,131696,49488,49408,132208,131728,49504,49856,132216,131760,49520,50048,132224,131792,49408,49232,132232,131824,49424,49424,132240,131856,49440,49872,132248,131888,49456,50064,
132256,131920,49600,49248,132264,131952,49616,49440,132272,131984,49632,49888,132280,132016,49648,50080,132288,132048,49536,49264,132296,132080,49552,49456,132304,132112,49568,49904,132312,132144,49584,50096,
132320,131680,49792,49280,132328,131712,49808,49600,132336,131744,49824,49664,132344,131776,49840,49984,132352,131808,49856,49296,132360,131840,49872,49616,132368,131872,49888,49680,132376,131904,49904,50000,
132384,131936,49664,49312,132392,131968,49680,49632,132400,132000,49696,49696,132408,132032,49712,50016,132416,132064,49728,49328,132424,132096,49744,49648,132432,132128,49760,49712,132440,132160,49776,50032,
132448,131696,50112,49344,132456,131728,50128,49536,132464,131760,50144,49728,132472,131792,50160,49920,132480,131824,50048,49360,132488,131856,50064,49552,132496,131888,50080,49744,132504,131920,50096,49936,
132512,131952,49984,49376,132520,131984,50000,49568,132528,132016,50016,49760,132536,132048,50032,49952,132544,132080,49920,49392,132552,132112,49936,49584,132560,132144,49952,49776,132568,132176,49968,49968,
132576,131584,53248,50176,132584,131616,53264,50496,132592,131648,53280,50816,132600,131680,53296,51136,132608,131712,53312,50192,132616,131744,53328,50512,132624,131776,53344,50832,132632,131808,53360,51152,
132640,131840,53376,50208,132648,131872,53392,50528,132656,131904,53408,50848,132664,131936,53424,51168,132672,131968,53440,50224,132680,132000,53456,50544,132688,132032,53472,50864,132696,132064,53488,51184,
132704,131600,53568,50240,132712,131632,53584,50432,132720,131664,53600,50880,132728,131696,53616,51072,132736,131728,53504,50256,132744,131760,53520,50448,132752,131792,53536,50896,132760,131824,53552,51088,
132768,131856,53696,50272,132776,131888,53712,50464,132784,131920,53728,50912,132792,131952,53744,51104,132800,131984,53632,50288,132808,132016,53648,50480,132816,132048,53664,50928,132824,132080,53680,51120,
132832,131616,53888,50304,132840,131648,53904,50624,132848,131680,53920,50688,132856,131712,53936,51008,132864,131744,53952,50320,132872,131776,53968,50640,132880,131808,53984,50704,132888,131840,54000,51024,
132896,131872,53760,50336,132904,131904,53776,50656,132912,131936,53792,50720,132920,131968,53808,51040,132928,132000,53824,50352,132936,132032,53840,50672,132944,132064,53856,50736,132952,132096,53872,51056,
132960,131632,54208,50368,132968,131664,54224,50560,132976,131696,54240,50752,132984,131728,54256,50944,132992,131760,54144,50384,133000,131792,54160,50576,133008,131824,54176,50768,133016,131856,54192,50960,
133024,131888,54080,50400,133032,131920,54096,50592,133040,131952,54112,50784,133048,131984,54128,50976,133056,132016,54016,50416,133064,132048,54032,50608,133072,132080,54048,50800,133080,132112,54064,50992,
133088,136256,57344,51200,133096,136288,57360,51520,133104,136320,57376,51840,133112,136352,57392,52160,133120,136384,57408,51216,133128,136416,57424,51536,133136,136448,57440,51856,133144,136480,57456,52176,
133152,136512,57472,51232,133160,136544,57488,51552,133168,136576,57504,51872,133176,136608,57520,52192,133184,136640,57536,51248,133192,136672,57552,51568,133200,136704,57568,51888,133208,136736,57584,52208,
133216,136272,57664,51264,133224,136304,57680,51456,133232,136336,57696,51904,133240,136368,57712,52096,133248,136400,57600,51280,133256,136432,57616,51472,133264,136464,57632,51920,133272,136496,57648,52112,
133280,136528,57792,51296,133288,136560,57808,51488,133296,136592,57824,51936,133304,136624,57840,52128,133312,136656,57728,51312,133320,136688,57744,51504,133328,136720,57760,51952,133336,136752,57776,52144,
133344,136288,57984,51328,133352,136320,58000,51648,133360,136352,58016,51712,133368,136384,58032,52032,133376,136416,58048,51344,133384,136448,58064,51664,133392,136480,58080,51728,133400,136512,58096,52048,
133408,136544,57856,51360,133416,136576,57872,51680,133424,136608,57888,51744,133432,136640,57904,52064,133440,136672,57920,51376,133448,136704,57936,51696,133456,136736,57952,51760,133464,136768,57968,52080,
133472,136304,58304,51392,133480,136336,58320,51584,133488,136368,58336,51776,133496,136400,58352,51968,133504,136432,58240,51408,133512,136464,58256,51600,133520,136496,58272,51792,133528,136528,58288,51984,
133536,136560,58176,51424,133544,136592,58192,51616,133552,136624,58208,51808,133560,136656,58224,52000,133568,136688,58112,51440,133576,136720,58128,51632,133584,136752,58144,51824,133592,136784,58160,52016,
133600,136192,61440,52224,133608,136224,61456,52544,133616,136256,61472,52864,133624,136288,61488,53184,133632,136320,61504,52240,133640,136352,61520,52560,133648,136384,61536,52880,133656,136416,61552,53200,
133664,136448,61568,52256,133672,136480,61584,52576,133680,136512,61600,52896,133688,136544,61616,53216,133696,136576,61632,52272,133704,136608,61648,52592,133712,136640,61664,52912,133720,136672,61680,53232,
133728,136208,61760,52288,133736,136240,61776,52480,133744,136272,61792,52928,133752,136304,61808,53120,133760,136336,61696,52304,133768,136368,61712,52496,133776,136400,61728,52944,133784,136432,61744,53136,
133792,136464,61888,52320,133800,136496,61904,52512,133808,136528,61920,52960,133816,136560,61936,53152,133824,136592,61824,52336,133832,136624,61840,52528,133840,136656,61856,52976,133848,136688,61872,53168,
133856,136224,62080,52352,133864,136256,62096,52672,133872,136288,62112,52736,133880,136320,62128,53056,133888,136352,62144,52368,133896,136384,62160,52688,133904,136416,62176,52752,133912,136448,62192,53072,
133920,136480,61952,52384,133928,136512,61968,52704,133936,136544,61984,52768,133944,136576,62000,53088,133952,136608,62016,52400,133960,136640,62032,52720,133968,136672,62048,52784,133976,136704,62064,53104,
133984,136240,62400,52416,133992,136272,62416,52608,134000,136304,62432,52800,134008,136336,62448,52992,134016,136368,62336,52432,134024,136400,62352,52624,134032,136432,62368,52816,134040,136464,62384,53008,
134048,136496,62272,52448,134056,136528,62288,52640,134064,136560,62304,52832,134072,136592,62320,53024,134080,136624,62208,52464,134088,136656,62224,52656,134096,136688,62240,52848,134104,136720,62256,53040,
98392,0,0,0,102552,16,4096,16,106712,32,8192,32,110872,48,12288,48,98384,16384,16384,8192,102544,16400,20480,8208,106704,16416,24576,8224,110864,16432,28672,8240,
98376,32768,32768,16384,102536,32784,36864,16400,106696,32800,40960,16416,110856,32816,45056,16432,98368,49152,49152,24576,102528,49168,53248,24592,106688,49184,57344,24608,110848,49200,61440,24624,
98408,65536,65536,32768,102568,65552,69632,32784,106728,65568,73728,32800,110888,65584,77824,32816,98400,81920,81920,40960,102560,81936,86016,40976,106720,81952,90112,40992,110880,81968,94208,41008,
98392,98304,98304,49152,102552,98320,102400,49168,106712,98336,106496,49184,110872,98352,110592,49200,98384,114688,114688,57344,102544,114704,118784,57360,106704,114720,122880,57376,110864,114736,126976,57392,
98424,131072,131072,65536,102584,131088,135168,65552,106744,131104,139264,65568,110904,131120,143360,65584,98416,147456,147456,73728,102576,147472,151552,73744,106736,147488,155648,73760,110896,147504,159744,73776,
98408,163840,163840,81920,102568,163856,167936,81936,106728,163872,172032,81952,110888,163888,176128,81968,98400,180224,180224,90112,102560,180240,184320,90128,106720,180256,188416,90144,110880,180272,192512,90160,
98440,196608,196608,98304,102600,196624,200704,98320,106760,196640,204800,98336,110920,196656,208896,98352,98432,212992,212992,106496,102592,213008,217088,106512,106752,213024,221184,106528,110912,213040,225280,106544,
98424,229376,229376,114688,102584,229392,233472,114704,106744,229408,237568,114720,110904,229424,241664,114736,98416,245760,245760,122880,102576,245776,249856,122896,106736,245792,253952,122912,110896,245808,258048,122928,
98328,262144,262144,0,102488,262160,266240,16,106648,262176,270336,32,110808,262192,274432,48,98320,278528,278528,8192,102480,278544,282624,8208,106640,278560,286720,8224,110800,278576,290816,8240,
98312,294912,294912,16384,102472,294928,299008,16400,106632,294944,303104,16416,110792,294960,307200,16432,98304,311296,311296,24576,102464,311312,315392,24592,106624,311328,319488,24608,110784,311344,323584,24624,
98344,327680,327680,32768,102504,327696,331776,32784,106664,327712,335872,32800,110824,327728,339968,32816,98336,344064,344064,40960,102496,344080,348160,40976,106656,344096,352256,40992,110816,344112,356352,41008,
98328,360448,360448,49152,102488,360464,364544,49168,106648,360480,368640,49184,110808,360496,372736,49200,98320,376832,376832,57344,102480,376848,380928,57360,106640,376864,385024,57376,110800,376880,389120,57392,
98360,393216,393216,65536,102520,393232,397312,65552,106680,393248,401408,65568,110840,393264,405504,65584,98352,409600,409600,73728,102512,409616,413696,73744,106672,409632,417792,73760,110832,409648,421888,73776,
98344,425984,425984,81920,102504,426000,430080,81936,106664,426016,434176,81952,110824,426032,438272,81968,98336,442368,442368,90112,102496,442384,446464,90128,106656,442400,450560,90144,110816,442416,454656,90160,
98376,458752,458752,98304,102536,458768,462848,98320,106696,458784,466944,98336,110856,458800,471040,98352,98368,475136,475136,106496,102528,475152,479232,106512,106688,475168,483328,106528,110848,475184,487424,106544,
98360,491520,491520,114688,102520,491536,495616,114704,106680,491552,499712,114720,110840,491568,503808,114736,98352,507904,507904,122880,102512,507920,512000,122896,106672,507936,516096,122912,110832,507952,520192,122928,
115032,524288,524288,0,119192,524304,528384,16,123352,524320,532480,32,127512,524336,536576,48,115024,540672,540672,8192,119184,540688,544768,8208,123344,540704,548864,8224,127504,540720,552960,8240,
115016,557056,557056,16384,119176,557072,561152,16400,123336,557088,565248,16416,127496,557104,569344,16432,115008,573440,573440,24576,119168,573456,577536,24592,123328,573472,581632,24608,127488,573488,585728,24624,
115048,589824,589824,32768,119208,589840,593920,32784,123368,589856,598016,32800,127528,589872,602112,32816,115040,606208,606208,40960,119200,606224,610304,40976,123360,606240,614400,40992,127520,606256,618496,41008,
115032,622592,622592,49152,119192,622608,626688,49168,123352,622624,630784,49184,127512,622640,634880,49200,115024,638976,638976,57344,119184,638992,643072,57360,123344,639008,647168,57376,127504,639024,651264,57392,
115064,655360,655360,65536,119224,655376,659456,65552,123384,655392,663552,65568,127544,655408,667648,65584,115056,671744,671744,73728,119216,671760,675840,73744,123376,671776,679936,73760,127536,671792,684032,73776,
115048,688128,688128,81920,119208,688144,692224,81936,123368,688160,696320,81952,127528,688176,700416,81968,115040,704512,704512,90112,119200,704528,708608,90128,123360,704544,712704,90144,127520,704560,716800,90160,
115080,720896,720896,98304,119240,720912,724992,98320,123400,720928,729088,98336,127560,720944,733184,98352,115072,737280,737280,106496,119232,737296,741376,106512,123392,737312,745472,106528,127552,737328,749568,106544,
115064,753664,753664,114688,119224,753680,757760,114704,123384,753696,761856,114720,127544,753712,765952,114736,115056,770048,770048,122880,119216,770064,774144,122896,123376,770080,778240,122912,127536,770096,782336,122928,
114968,786432,786432,0,119128,786448,790528,16,123288,786464,794624,32,127448,786480,798720,48,114960,802816,802816,8192,119120,802832,806912,8208,123280,802848,811008,8224,127440,802864,815104,8240,
114952,819200,819200,16384,119112,819216,823296,16400,123272,819232,827392,16416,127432,819248,831488,16432,114944,835584,835584,24576,119104,835600,839680,24592,123264,835616,843776,24608,127424,835632,847872,24624,
114984,851968,851968,32768,119144,851984,856064,32784,123304,852000,860160,32800,127464,852016,864256,32816,114976,868352,868352,40960,119136,868368,872448,40976,123296,868384,876544,40992,127456,868400,880640,41008,
114968,884736,884736,49152,119128,884752,888832,49168,123288,884768,892928,49184,127448,884784,897024,49200,114960,901120,901120,57344,119120,901136,905216,57360,123280,901152,909312,57376,127440,901168,913408,57392,
115000,917504,917504,65536,119160,917520,921600,65552,123320,917536,925696,65568,127480,917552,929792,65584,114992,933888,933888,73728,119152,933904,937984,73744,123312,933920,942080,73760,127472,933936,946176,73776,
114984,950272,950272,81920,119144,950288,954368,81936,123304,950304,958464,81952,127464,950320,962560,81968,114976,966656,966656,90112,119136,966672,970752,90128,123296,966688,974848,90144,127456,966704,978944,90160,
115016,983040,983040,98304,119176,983056,987136,98320,123336,983072,991232,98336,127496,983088,995328,98352,115008,999424,999424,106496,119168,999440,1003520,106512,123328,999456,1007616,106528,127488,999472,1011712,106544,
115000,1015808,1015808,114688,119160,1015824,1019904,114704,123320,1015840,1024000,114720,127480,1015856,1028096,114736,114992,1032192,1032192,122880,119152,1032208,1036288,122896,123312,1032224,1040384,122912,127472,1032240,1044480,122928,
0,0,0,0,16,4096,16,16,32,8192,32,32,48,12288,48,48,64,16,8192,16384,80,4112,8208,16400,96,8208,8224,16416,112,12304,8240,16432,
128,32,16384,32768,144,4128,16400,32784,160,8224,16416,32800,176,12320,16432,32816,192,48,24576,49152,208,4144,24592,49168,224,8240,24608,49184,240,12336,24624,49200,
256,64,32768,65536,272,4160,32784,65552,288,8256,32800,65568,304,12352,32816,65584,320,80,40960,81920,336,4176,40976,81936,352,8272,40992,81952,368,12368,41008,81968,
384,96,49152,98304,400,4192,49168,98320,416,8288,49184,98336,432,12384,49200,98352,448,112,57344,114688,464,4208,57360,114704,480,8304,57376,114720,496,12400,57392,114736,
512,128,65536,131072,528,4224,65552,131088,544,8320,65568,131104,560,12416,65584,131120,576,144,73728,147456,592,4240,73744,147472,608,8336,73760,147488,624,12432,73776,147504,
640,160,81920,163840,656,4256,81936,163856,672,8352,81952,163872,688,12448,81968,163888,704,176,90112,180224,720,4272,90128,180240,736,8368,90144,180256,752,12464,90160,180272,
768,192,98304,196608,784,4288,98320,196624,800,8384,98336,196640,816,12480,98352,196656,832,208,106496,212992,848,4304,106512,213008,864,8400,106528,213024,880,12496,106544,213040,
896,224,114688,229376,912,4320,114704,229392,928,8416,114720,229408,944,12512,114736,229424,960,240,122880,245760,976,4336,122896,245776,992,8432,122912,245792,1008,12528,122928,245808,
1073741824,1073741824,0,262144,1073741824,1073741824,16,262160,1073741824,1073741824,32,262176,1073741824,1073741824,48,262192,1073741824,1073741824,8192,278528,1073741824,1073741824,8208,278544,1073741824,1073741824,8224,278560,1073741824,1073741824,8240,278576,
1073741824,1073741824,16384,294912,1073741824,1073741824,16400,294928,1073741824,1073741824,16416,294944,1073741824,1073741824,16432,294960,1073741824,1073741824,24576,311296,1073741824,1073741824,24592,311312,1073741824,1073741824,24608,311328,1073741824,1073741824,24624,311344,
1073741824,1073741824,32768,327680,1073741824,1073741824,32784,327696,1073741824,1073741824,32800,327712,1073741824,1073741824,32816,327728,1073741824,1073741824,40960,344064,1073741824,1073741824,40976,344080,1073741824,1073741824,40992,344096,1073741824,1073741824,41008,344112,
1073741824,1073741824,49152,360448,1073741824,1073741824,49168,360464,1073741824,1073741824,49184,360480,1073741824,1073741824,49200,360496,1073741824,1073741824,57344,376832,1073741824,1073741824,57360,376848,1073741824,1073741824,57376,376864,1073741824,1073741824,57392,376880,
1073741824,1073741824,65536,393216,1073741824,1073741824,65552,393232,1073741824,1073741824,65568,393248,1073741824,1073741824,65584,393264,1073741824,1073741824,73728,409600,1073741824,1073741824,73744,409616,1073741824,1073741824,73760,409632,1073741824,1073741824,73776,409648,
1073741824,1073741824,81920,425984,1073741824,1073741824,81936,426000,1073741824,1073741824,81952,426016,1073741824,1073741824,81968,426032,1073741824,1073741824,90112,442368,1073741824,1073741824,90128,442384,1073741824,1073741824,90144,442400,1073741824,1073741824,90160,442416,
1073741824,1073741824,98304,458752,1073741824,1073741824,98320,458768,1073741824,1073741824,98336,458784,1073741824,1073741824,98352,458800,1073741824,1073741824,106496,475136,1073741824,1073741824,106512,475152,1073741824,1073741824,106528,475168,1073741824,1073741824,106544,475184,
1073741824,1073741824,114688,491520,1073741824,1073741824,114704,491536,1073741824,1073741824,114720,491552,1073741824,1073741824,114736,491568,1073741824,1073741824,122880,507904,1073741824,1073741824,122896,507920,1073741824,1073741824,122912,507936,1073741824,1073741824,122928,507952,
1073741824,1073741824,0,524288,1073741824,1073741824,16,524304,1073741824,1073741824,32,524320,1073741824,1073741824,48,524336,1073741824,1073741824,8192,540672,1073741824,1073741824,8208,540688,1073741824,1073741824,8224,540704,1073741824,1073741824,8240,540720,
1073741824,1073741824,16384,557056,1073741824,1073741824,16400,557072,1073741824,1073741824,16416,557088,1073741824,1073741824,16432,557104,1073741824,1073741824,24576,573440,1073741824,1073741824,24592,573456,1073741824,1073741824,24608,573472,1073741824,1073741824,24624,573488,
1073741824,1073741824,32768,589824,1073741824,1073741824,32784,589840,1073741824,1073741824,32800,589856,1073741824,1073741824,32816,589872,1073741824,1073741824,40960,606208,1073741824,1073741824,40976,606224,1073741824,1073741824,40992,606240,1073741824,1073741824,41008,606256,
1073741824,1073741824,49152,622592,1073741824,1073741824,49168,622608,1073741824,1073741824,49184,622624,1073741824,1073741824,49200,622640,1073741824,1073741824,57344,638976,1073741824,1073741824,57360,638992,1073741824,1073741824,57376,639008,1073741824,1073741824,57392,639024,
1073741824,1073741824,65536,655360,1073741824,1073741824,65552,655376,1073741824,1073741824,65568,655392,1073741824,1073741824,65584,655408,1073741824,1073741824,73728,671744,1073741824,1073741824,73744,671760,1073741824,1073741824,73760,671776,1073741824,1073741824,73776,671792,
1073741824,1073741824,81920,688128,1073741824,1073741824,81936,688144,1073741824,1073741824,81952,688160,1073741824,1073741824,81968,688176,1073741824,1073741824,90112,704512,1073741824,1073741824,90128,704528,1073741824,1073741824,90144,704544,1073741824,1073741824,90160,704560,
1073741824,1073741824,98304,720896,1073741824,1073741824,98320,720912,1073741824,1073741824,98336,720928,1073741824,1073741824,98352,720944,1073741824,1073741824,106496,737280,1073741824,1073741824,106512,737296,1073741824,1073741824,106528,737312,1073741824,1073741824,106544,737328,
1073741824,1073741824,114688,753664,1073741824,1073741824,114704,753680,1073741824,1073741824,114720,753696,1073741824,1073741824,114736,753712,1073741824,1073741824,122880,770048,1073741824,1073741824,122896,770064,1073741824,1073741824,122912,770080,1073741824,1073741824,122928,770096,
1073741824,1073741824,0,786432,1073741824,1073741824,16,786448,1073741824,1073741824,32,786464,1073741824,1073741824,48,786480,1073741824,1073741824,8192,802816,1073741824,1073741824,8208,802832,1073741824,1073741824,8224,802848,1073741824,1073741824,8240,802864,
1073741824,1073741824,16384,819200,1073741824,1073741824,16400,819216,1073741824,1073741824,16416,819232,1073741824,1073741824,16432,819248,1073741824,1073741824,24576,835584,1073741824,1073741824,24592,835600,1073741824,1073741824,24608,835616,1073741824,1073741824,24624,835632,
1073741824,1073741824,32768,851968,1073741824,1073741824,32784,851984,1073741824,1073741824,32800,852000,1073741824,1073741824,32816,852016,1073741824,1073741824,40960,868352,1073741824,1073741824,40976,868368,1073741824,1073741824,40992,868384,1073741824,1073741824,41008,868400,
1073741824,1073741824,49152,884736,1073741824,1073741824,49168,884752,1073741824,1073741824,49184,884768,1073741824,1073741824,49200,884784,1073741824,1073741824,57344,901120,1073741824,1073741824,57360,901136,1073741824,1073741824,57376,901152,1073741824,1073741824,57392,901168,
1073741824,1073741824,65536,917504,1073741824,1073741824,65552,917520,1073741824,1073741824,65568,917536,1073741824,1073741824,65584,917552,1073741824,1073741824,73728,933888,1073741824,1073741824,73744,933904,1073741824,1073741824,73760,933920,1073741824,1073741824,73776,933936,
1073741824,1073741824,81920,950272,1073741824,1073741824,81936,950288,1073741824,1073741824,81952,950304,1073741824,1073741824,81968,950320,1073741824,1073741824,90112,966656,1073741824,1073741824,90128,966672,1073741824,1073741824,90144,966688,1073741824,1073741824,90160,966704,
1073741824,1073741824,98304,983040,1073741824,1073741824,98320,983056,1073741824,1073741824,98336,983072,1073741824,1073741824,98352,983088,1073741824,1073741824,106496,999424,1073741824,1073741824,106512,999440,1073741824,1073741824,106528,999456,1073741824,1073741824,106544,999472,
1073741824,1073741824,114688,1015808,1073741824,1073741824,114704,1015824,1073741824,1073741824,114720,1015840,1073741824,1073741824,114736,1015856,1073741824,1073741824,122880,1032192,1073741824,1073741824,122896,1032208,1073741824,1073741824,122912,1032224,1073741824,1073741824,122928,1032240,
98304,98304,159376,158736,98832,98832,159416,158776,99360,99360,159456,158816,99888,99888,159496,158856,100416,100416,159536,158896,100944,100944,159576,158936,101472,101472,159616,158976,102000,102000,159656,159016,
102528,102528,159696,159056,103056,103056,159736,159096,103584,103584,159776,159136,104112,104112,159816,159176,104640,104640,159856,159216,105168,105168,159896,159256,105696,105696,159936,159296,106224,106224,159976,159336,
98560,98560,159384,158744,99088,99088,159424,158784,99616,99616,159464,158824,100144,100144,159504,158864,100672,100672,159544,158904,101200,101200,159584,158944,101728,101728,159624,158984,102256,102256,159664,159024,
102784,102784,159704,159064,103312,103312,159744,159104,103840,103840,159784,159144,104368,104368,159824,159184,104896,104896,159864,159224,105424,105424,159904,159264,105952,105952,159944,159304,106480,106480,159984,159344,
98432,98432,159392,158752,98960,98960,159432,158792,99488,99488,159472,158832,100016,100016,159512,158872,100544,100544,159552,158912,101072,101072,159592,158952,101600,101600,159632,158992,102128,102128,159672,159032,
102656,102656,159712,159072,103184,103184,159752,159112,103712,103712,159792,159152,104240,104240,159832,159192,104768,104768,159872,159232,105296,105296,159912,159272,105824,105824,159952,159312,106352,106352,159992,159352,
98688,98688,159400,158760,99216,99216,159440,158800,99744,99744,159480,158840,100272,100272,159520,158880,100800,100800,159560,158920,101328,101328,159600,158960,101856,101856,159640,159000,102384,102384,159680,159040,
102912,102912,159720,159080,103440,103440,159760,159120,103968,103968,159800,159160,104496,104496,159840,159200,105024,105024,159880,159240,105552,105552,159920,159280,106080,106080,159960,159320,106608,106608,160000,159360,
98304,123648,159376,160016,98832,124176,159416,160056,99360,124704,159456,160096,99888,125232,159496,160136,100416,125760,159536,160176,100944,126288,159576,160216,101472,126816,159616,160256,102000,127344,159656,160296,
102528,127872,159696,160336,103056,128400,159736,160376,103584,128928,159776,160416,104112,129456,159816,160456,104640,129984,159856,160496,105168,130512,159896,160536,105696,131040,159936,160576,106224,131568,159976,160616,
98560,123904,159384,160024,99088,124432,159424,160064,99616,124960,159464,160104,100144,125488,159504,160144,100672,126016,159544,160184,101200,126544,159584,160224,101728,127072,159624,160264,102256,127600,159664,160304,
102784,128128,159704,160344,103312,128656,159744,160384,103840,129184,159784,160424,104368,129712,159824,160464,104896,130240,159864,160504,105424,130768,159904,160544,105952,131296,159944,160584,106480,131824,159984,160624,
98432,123776,159392,160032,98960,124304,159432,160072,99488,124832,159472,160112,100016,125360,159512,160152,100544,125888,159552,160192,101072,126416,159592,160232,101600,126944,159632,160272,102128,127472,159672,160312,
102656,128000,159712,160352,103184,128528,159752,160392,103712,129056,159792,160432,104240,129584,159832,160472,104768,130112,159872,160512,105296,130640,159912,160552,105824,131168,159952,160592,106352,131696,159992,160632,
98688,124032,159400,160040,99216,124560,159440,160080,99744,125088,159480,160120,100272,125616,159520,160160,100800,126144,159560,160200,101328,126672,159600,160240,101856,127200,159640,160280,102384,127728,159680,160320,
102912,128256,159720,160360,103440,128784,159760,160400,103968,129312,159800,160440,104496,129840,159840,160480,105024,130368,159880,160520,105552,130896,159920,160560,106080,131424,159960,160600,106608,131952,160000,160640,
128400,128400,161312,160672,128928,128928,161352,160712,129456,129456,161392,160752,129984,129984,161432,160792,130512,130512,161472,160832,131040,131040,161512,160872,131568,131568,161552,160912,132096,132096,161592,160952,
132624,132624,161632,160992,133152,133152,161672,161032,133680,133680,161712,161072,134208,134208,161752,161112,134736,134736,161792,161152,135264,135264,161832,161192,135792,135792,161872,161232,136320,136320,161912,161272,
128656,128656,161320,160680,129184,129184,161360,160720,129712,129712,161400,160760,130240,130240,161440,160800,130768,130768,161480,160840,131296,131296,161520,160880,131824,131824,161560,160920,132352,132352,161600,160960,
132880,132880,161640,161000,133408,133408,161680,161040,133936,133936,161720,161080,134464,134464,161760,161120,134992,134992,161800,161160,135520,135520,161840,161200,136048,136048,161880,161240,136576,136576,161920,161280,
128528,128528,161328,160688,129056,129056,161368,160728,129584,129584,161408,160768,130112,130112,161448,160808,130640,130640,161488,160848,131168,131168,161528,160888,131696,131696,161568,160928,132224,132224,161608,160968,
132752,132752,161648,161008,133280,133280,161688,161048,133808,133808,161728,161088,134336,134336,161768,161128,134864,134864,161808,161168,135392,135392,161848,161208,135920,135920,161888,161248,136448,136448,161928,161288,
128784,128784,161336,160696,129312,129312,161376,160736,129840,129840,161416,160776,130368,130368,161456,160816,130896,130896,161496,160856,131424,131424,161536,160896,131952,131952,161576,160936,132480,132480,161616,160976,
133008,133008,161656,161016,133536,133536,161696,161056,134064,134064,161736,161096,134592,134592,161776,161136,135120,135120,161816,161176,135648,135648,161856,161216,136176,136176,161896,161256,136704,136704,161936,161296,
128400,153744,161312,161952,128928,154272,161352,161992,129456,154800,161392,162032,129984,155328,161432,162072,130512,155856,161472,162112,131040,156384,161512,162152,131568,156912,161552,162192,132096,157440,161592,162232,
132624,157968,161632,162272,133152,158496,161672,162312,133680,159024,161712,162352,134208,159552,161752,162392,134736,160080,161792,162432,135264,160608,161832,162472,135792,161136,161872,162512,136320,161664,161912,162552,
128656,154000,161320,161960,129184,154528,161360,162000,129712,155056,161400,162040,130240,155584,161440,162080,130768,156112,161480,162120,131296,156640,161520,162160,131824,157168,161560,162200,132352,157696,161600,162240,
132880,158224,161640,162280,133408,158752,161680,162320,133936,159280,161720,162360,134464,159808,161760,162400,134992,160336,161800,162440,135520,160864,161840,162480,136048,161392,161880,162520,136576,161920,161920,162560,
128528,153872,161328,161968,129056,154400,161368,162008,129584,154928,161408,162048,130112,155456,161448,162088,130640,155984,161488,162128,131168,156512,161528,162168,131696,157040,161568,162208,132224,157568,161608,162248,
132752,158096,161648,162288,133280,158624,161688,162328,133808,159152,161728,162368,134336,159680,161768,162408,134864,160208,161808,162448,135392,160736,161848,162488,135920,161264,161888,162528,136448,161792,161928,162568,
128784,154128,161336,161976,129312,154656,161376,162016,129840,155184,161416,162056,130368,155712,161456,162096,130896,156240,161496,162136,131424,156768,161536,162176,131952,157296,161576,162216,132480,157824,161616,162256,
133008,158352,161656,162296,133536,158880,161696,162336,134064,159408,161736,162376,134592,159936,161776,162416,135120,160464,161816,162456,135648,160992,161856,162496,136176,161520,161896,162536,136704,162048,161936,162576,
160032,160032,160032,160032,160630,160630,160630,160590,160590,160590,160590,160590,160550,160550,160550,160510,160510,160510,160510,160510,160470,160470,160470,160430,160430,160430,160430,160430,160390,160390,160390,160350,
160350,160350,160350,160350,160310,160310,160310,160270,160270,160270,160270,160270,160230,160230,160230,160190,160190,160190,160190,160190,160150,160150,160150,160110,160110,160110,160110,160110,160070,160070,160070,160030,
160030,160030,160030,160030,160628,160628,160628,160588,160588,160588,160588,160588,160548,160548,160548,160508,160508,160508,160508,160508,160468,160468,160468,160428,160428,160428,160428,160428,160388,160388,160388,160348,
160348,160348,160348,160348,160308,160308,160308,160268,160268,160268,160268,160268,160228,160228,160228,160188,160188,160188,160188,160188,160148,160148,160148,160108,160108,160108,160108,160108,160068,160068,160068,160028,
160028,160028,160028,160028,160626,160626,160626,160586,160586,160586,160586,160586,160546,160546,160546,160506,160506,160506,160506,160506,160466,160466,160466,160426,160426,160426,160426,160426,160386,160386,160386,160346,
160346,160346,160346,160346,160306,160306,160306,160266,160266,160266,160266,160266,160226,160226,160226,160186,160186,160186,160186,160186,160146,160146,160146,160106,160106,160106,160106,160106,160066,160066,160066,160026,
160026,160026,160026,160026,160624,160624,160624,160584,160584,160584,160584,160584,160544,160544,160544,160504,160504,160504,160504,160504,160464,160464,160464,160424,160424,160424,160424,160424,160384,160384,160384,160344,
160344,160344,160344,160344,160304,160304,160304,160264,160264,160264,160264,160264,160224,160224,160224,160184,160184,160184,160184,160184,160144,160144,160144,160104,160104,160104,160104,160104,160064,160064,160064,160024,
160024,160024,160024,160024,160622,160622,160622,160582,160582,160582,160582,160582,160542,160542,160542,160502,160502,160502,160502,160502,160462,160462,160462,160422,160422,160422,160422,160422,160382,160382,160382,160342,
160342,160342,160342,160342,160302,160302,160302,160262,160262,160262,160262,160262,160222,160222,160222,160182,160182,160182,160182,160182,160142,160142,160142,160102,160102,160102,160102,160102,160062,160062,160062,160022,
160022,160022,160022,160022,160620,160620,160620,160580,160580,160580,160580,160580,160540,160540,160540,160500,160500,160500,160500,160500,160460,160460,160460,160420,160420,160420,160420,160420,160380,160380,160380,160340,
160340,160340,160340,160340,160300,160300,160300,160260,160260,160260,160260,160260,160220,160220,160220,160180,160180,160180,160180,160180,160140,160140,160140,160100,160100,160100,160100,160100,160060,160060,160060,160020,
160020,160020,160020,160020,160618,160618,160618,160578,160578,160578,160578,160578,160538,160538,160538,160498,160498,160498,160498,160498,160458,160458,160458,160418,160418,160418,160418,160418,160378,160378,160378,160338,
160338,160338,160338,160338,160298,160298,160298,160258,160258,160258,160258,160258,160218,160218,160218,160178,160178,160178,160178,160178,160138,160138,160138,160098,160098,160098,160098,160098,160058,160058,160058,160018,
160018,160018,160018,160018,160616,160616,160616,160576,160576,160576,160576,160576,160536,160536,160536,160496,160496,160496,160496,160496,160456,160456,160456,160416,160416,160416,160416,160416,160376,160376,160376,160336,
160336,160336,160336,160336,160296,160296,160296,160256,160256,160256,160256,160256,160216,160216,160216,160176,160176,160176,160176,160176,160136,160136,160136,160096,160096,160096,160096,160096,160056,160056,160056,160016,
160016,160016,160006,160006,160004,160004,159966,159966,159964,159964,159926,159926,159924,159924,159886,159886,159884,159884,159846,159846,159844,159844,159806,159806,159804,159804,159766,159766,159764,159764,159726,159726,
159724,159724,159686,159686,159684,159684,159646,159646,159644,159644,159606,159606,159604,159604,159566,159566,159564,159564,159526,159526,159524,159524,159486,159486,159484,159484,159446,159446,159444,159444,159406,159406,
159404,159404,160002,160002,160000,160000,159962,159962,159960,159960,159922,159922,159920,159920,159882,159882,159880,159880,159842,159842,159840,159840,159802,159802,159800,159800,159762,159762,159760,159760,159722,159722,
159720,159720,159682,159682,159680,159680,159642,159642,159640,159640,159602,159602,159600,159600,159562,159562,159560,159560,159522,159522,159520,159520,159482,159482,159480,159480,159442,159442,159440,159440,159402,159402,
159400,159400,159998,159998,159996,159996,159958,159958,159956,159956,159918,159918,159916,159916,159878,159878,159876,159876,159838,159838,159836,159836,159798,159798,159796,159796,159758,159758,159756,159756,159718,159718,
159716,159716,159678,159678,159676,159676,159638,159638,159636,159636,159598,159598,159596,159596,159558,159558,159556,159556,159518,159518,159516,159516,159478,159478,159476,159476,159438,159438,159436,159436,159398,159398,
159396,159396,159994,159994,159992,159992,159954,159954,159952,159952,159914,159914,159912,159912,159874,159874,159872,159872,159834,159834,159832,159832,159794,159794,159792,159792,159754,159754,159752,159752,159714,159714,
159712,159712,159674,159674,159672,159672,159634,159634,159632,159632,159594,159594,159592,159592,159554,159554,159552,159552,159514,159514,159512,159512,159474,159474,159472,159472,159434,159434,159432,159432,159394,159394,
159392,159990,159988,159988,159984,159950,159948,159948,159944,159910,159908,159908,159904,159870,159868,159868,159864,159830,159828,159828,159824,159790,159788,159788,159784,159750,159748,159748,159744,159710,159708,159708,
159704,159670,159668,159668,159664,159630,159628,159628,159624,159590,159588,159588,159584,159550,159548,159548,159544,159510,159508,159508,159504,159470,159468,159468,159464,159430,159428,159428,159424,159390,159388,159388,
159384,159982,159980,159980,159976,159942,159940,159940,159936,159902,159900,159900,159896,159862,159860,159860,159856,159822,159820,159820,159816,159782,159780,159780,159776,159742,159740,159740,159736,159702,159700,159700,
159696,159662,159660,159660,159656,159622,159620,159620,159616,159582,159580,159580,159576,159542,159540,159540,159536,159502,159500,159500,159496,159462,159460,159460,159456,159422,159420,159420,159416,159382,159380,159380,
159366,159364,159362,159360,159326,159324,159322,159320,159286,159284,159282,159280,159246,159244,159242,159240,159206,159204,159202,159200,159166,159164,159162,159160,159126,159124,159122,159120,159086,159084,159082,159080,
159046,159044,159042,159040,159006,159004,159002,159000,158966,158964,158962,158960,158926,158924,158922,158920,158886,158884,158882,158880,158846,158844,158842,158840,158806,158804,158802,158800,158766,158764,158762,158760,
159350,159348,159346,159344,159310,159308,159306,159304,159270,159268,159266,159264,159230,159228,159226,159224,159190,159188,159186,159184,159150,159148,159146,159144,159110,159108,159106,159104,159070,159068,159066,159064,
159030,159028,159026,159024,158990,158988,158986,158984,158950,158948,158946,158944,158910,158908,158906,158904,158870,158868,158866,158864,158830,158828,158826,158824,158790,158788,158786,158784,158750,158748,158746,158744,
160630,160630,160630,160630,160590,160590,160590,160590,160550,160550,160550,160550,160510,160510,160510,160510,160470,160470,160470,160470,160430,160430,160430,160430,160390,160390,160390,160390,160350,160350,160350,160350,
160310,160310,160310,160310,160270,160270,160270,160270,160230,160230,160230,160230,160190,160190,160190,160190,160150,160150,160150,160150,160110,160110,160110,160110,160070,160070,160070,160070,160030,160030,160030,160030,
160628,160628,160628,160628,160588,160588,160588,160588,160548,160548,160548,160548,160508,160508,160508,160508,160468,160468,160468,160468,160428,160428,160428,160428,160388,160388,160388,160388,160348,160348,160348,160348,
160308,160308,160308,160308,160268,160268,160268,160268,160228,160228,160228,160228,160188,160188,160188,160188,160148,160148,160148,160148,160108,160108,160108,160108,160068,160068,160068,160068,160028,160028,160028,160028,
160626,160626,160626,160626,160586,160586,160586,160586,160546,160546,160546,160546,160506,160506,160506,160506,160466,160466,160466,160466,160426,160426,160426,160426,160386,160386,160386,160386,160346,160346,160346,160346,
160306,160306,160306,160306,160266,160266,160266,160266,160226,160226,160226,160226,160186,160186,160186,160186,160146,160146,160146,160146,160106,160106,160106,160106,160066,160066,160066,160066,160026,160026,160026,160026,
160624,160624,160624,160624,160584,160584,160584,160584,160544,160544,160544,160544,160504,160504,160504,160504,160464,160464,160464,160464,160424,160424,160424,160424,160384,160384,160384,160384,160344,160344,160344,160344,
160304,160304,160304,160304,160264,160264,160264,160264,160224,160224,160224,160224,160184,160184,160184,160184,160144,160144,160144,160144,160104,160104,160104,160104,160064,160064,160064,160064,160024,160024,160024,160024,
160622,160622,160622,160622,160582,160582,160582,160582,160542,160542,160542,160542,160502,160502,160502,160502,160462,160462,160462,160462,160422,160422,160422,160422,160382,160382,160382,160382,160342,160342,160342,160342,
160302,160302,160302,160302,160262,160262,160262,160262,160222,160222,160222,160222,160182,160182,160182,160182,160142,160142,160142,160142,160102,160102,160102,160102,160062,160062,160062,160062,160022,160022,160022,160022,
160620,160620,160620,160620,160580,160580,160580,160580,160540,160540,160540,160540,160500,160500,160500,160500,160460,160460,160460,160460,160420,160420,160420,160420,160380,160380,160380,160380,160340,160340,160340,160340,
160300,160300,160300,160300,160260,160260,160260,160260,160220,160220,160220,160220,160180,160180,160180,160180,160140,160140,160140,160140,160100,160100,160100,160100,160060,160060,160060,160060,160020,160020,160020,160020,
160618,160618,160618,160618,160578,160578,160578,160578,160538,160538,160538,160538,160498,160498,160498,160498,160458,160458,160458,160458,160418,160418,160418,160418,160378,160378,160378,160378,160338,160338,160338,160338,
160298,160298,160298,160298,160258,160258,160258,160258,160218,160218,160218,160218,160178,160178,160178,160178,160138,160138,160138,160138,160098,160098,160098,160098,160058,160058,160058,160058,160018,160018,160018,160018,
160616,160616,160616,160616,160576,160576,160576,160576,160536,160536,160536,160536,160496,160496,160496,160496,160456,160456,160456,160456,160416,160416,160416,160416,160376,160376,160376,160376,160336,160336,160336,160336,
160296,160296,160296,160296,160256,160256,160256,160256,160216,160216,160216,160216,160176,160176,160176,160176,160136,160136,160136,160136,160096,160096,160096,160096,160056,160056,160056,160056,160016,160016,160016,160016,
160006,160004,160004,160004,159966,159964,159964,159964,159926,159924,159924,159924,159886,159884,159884,159884,159846,159844,159844,159844,159806,159804,159804,159804,159766,159764,159764,159764,159726,159724,159724,159724,
159686,159684,159684,159684,159646,159644,159644,159644,159606,159604,159604,159604,159566,159564,159564,159564,159526,159524,159524,159524,159486,159484,159484,159484,159446,159444,159444,159444,159406,159404,159404,159404,
160002,160000,160000,160000,159962,159960,159960,159960,159922,159920,159920,159920,159882,159880,159880,159880,159842,159840,159840,159840,159802,159800,159800,159800,159762,159760,159760,159760,159722,159720,159720,159720,
159682,159680,159680,159680,159642,159640,159640,159640,159602,159600,159600,159600,159562,159560,159560,159560,159522,159520,159520,159520,159482,159480,159480,159480,159442,159440,159440,159440,159402,159400,159400,159400,
159998,159996,159996,159996,159958,159956,159956,159956,159918,159916,159916,159916,159878,159876,159876,159876,159838,159836,159836,159836,159798,159796,159796,159796,159758,159756,159756,159756,159718,159716,159716,159716,
159678,159676,159676,159676,159638,159636,159636,159636,159598,159596,159596,159596,159558,159556,159556,159556,159518,159516,159516,159516,159478,159476,159476,159476,159438,159436,159436,159436,159398,159396,159396,159396,
159994,159992,159992,159992,159954,159952,159952,159952,159914,159912,159912,159912,159874,159872,159872,159872,159834,159832,159832,159832,159794,159792,159792,159792,159754,159752,159752,159752,159714,159712,159712,159712,
159674,159672,159672,159672,159634,159632,159632,159632,159594,159592,159592,159592,159554,159552,159552,159552,159514,159512,159512,159512,159474,159472,159472,159472,159434,159432,159432,159432,159394,159392,159392,159392,
159988,159986,159984,159984,159948,159946,159944,159944,159908,159906,159904,159904,159868,159866,159864,159864,159828,159826,159824,159824,159788,159786,159784,159784,159748,159746,159744,159744,159708,159706,159704,159704,
159668,159666,159664,159664,159628,159626,159624,159624,159588,159586,159584,159584,159548,159546,159544,159544,159508,159506,159504,159504,159468,159466,159464,159464,159428,159426,159424,159424,159388,159386,159384,159384,
159980,159978,159976,159976,159940,159938,159936,159936,159900,159898,159896,159896,159860,159858,159856,159856,159820,159818,159816,159816,159780,159778,159776,159776,159740,159738,159736,159736,159700,159698,159696,159696,
159660,159658,159656,159656,159620,159618,159616,159616,159580,159578,159576,159576,159540,159538,159536,159536,159500,159498,159496,159496,159460,159458,159456,159456,159420,159418,159416,159416,159380,159378,159376,159376,
159358,159356,159354,159352,159318,159316,159314,159312,159278,159276,159274,159272,159238,159236,159234,159232,159198,159196,159194,159192,159158,159156,159154,159152,159118,159116,159114,159112,159078,159076,159074,159072,
159038,159036,159034,159032,158998,158996,158994,158992,158958,158956,158954,158952,158918,158916,158914,158912,158878,158876,158874,158872,158838,158836,158834,158832,158798,158796,158794,158792,158758,158756,158754,158752,
159342,159340,159338,159336,159302,159300,159298,159296,159262,159260,159258,159256,159222,159220,159218,159216,159182,159180,159178,159176,159142,159140,159138,159136,159102,159100,159098,159096,159062,159060,159058,159056,
159022,159020,159018,159016,158982,158980,158978,158976,158942,158940,158938,158936,158902,158900,158898,158896,158862,158860,158858,158856,158822,158820,158818,158816,158782,158780,158778,158776,158742,158740,158738,158736,
160630,160630,160630,98304,160590,160590,160590,98320,160550,160550,160550,98336,160510,160510,160510,98352,160470,160470,160470,98368,160430,160430,160430,98384,160390,160390,160390,98400,160350,160350,160350,98416,
160310,160310,160310,98432,160270,160270,160270,98448,160230,160230,160230,98464,160190,160190,160190,98480,160150,160150,160150,98496,160110,160110,160110,98512,160070,160070,160070,98528,160030,160030,160030,98544,
160628,160628,160628,98560,160588,160588,160588,98576,160548,160548,160548,98592,160508,160508,160508,98608,160468,160468,160468,98624,160428,160428,160428,98640,160388,160388,160388,98656,160348,160348,160348,98672,
160308,160308,160308,98688,160268,160268,160268,98704,160228,160228,160228,98720,160188,160188,160188,98736,160148,160148,160148,98752,160108,160108,160108,98768,160068,160068,160068,98784,160028,160028,160028,98800,
160626,160626,160626,98816,160586,160586,160586,98832,160546,160546,160546,98848,160506,160506,160506,98864,160466,160466,160466,98880,160426,160426,160426,98896,160386,160386,160386,98912,160346,160346,160346,98928,
160306,160306,160306,98944,160266,160266,160266,98960,160226,160226,160226,98976,160186,160186,160186,98992,160146,160146,160146,99008,160106,160106,160106,99024,160066,160066,160066,99040,160026,160026,160026,99056,
160624,160624,160624,99072,160584,160584,160584,99088,160544,160544,160544,99104,160504,160504,160504,99120,160464,160464,160464,99136,160424,160424,160424,99152,160384,160384,160384,99168,160344,160344,160344,99184,
160304,160304,160304,99200,160264,160264,160264,99216,160224,160224,160224,99232,160184,160184,160184,99248,160144,160144,160144,99264,160104,160104,160104,99280,160064,160064,160064,99296,160024,160024,160024,99312,
160622,160622,160622,99328,160582,160582,160582,99344,160542,160542,160542,99360,160502,160502,160502,99376,160462,160462,160462,99392,160422,160422,160422,99408,160382,160382,160382,99424,160342,160342,160342,99440,
160302,160302,160302,99456,160262,160262,160262,99472,160222,160222,160222,99488,160182,160182,160182,99504,160142,160142,160142,99520,160102,160102,160102,99536,160062,160062,160062,99552,160022,160022,160022,99568,
160620,160620,160620,99584,160580,160580,160580,99600,160540,160540,160540,99616,160500,160500,160500,99632,160460,160460,160460,99648,160420,160420,160420,99664,160380,160380,160380,99680,160340,160340,160340,99696,
160300,160300,160300,99712,160260,160260,160260,99728,160220,160220,160220,99744,160180,160180,160180,99760,160140,160140,160140,99776,160100,160100,160100,99792,160060,160060,160060,99808,160020,160020,160020,99824,
160618,160618,160618,99840,160578,160578,160578,99856,160538,160538,160538,99872,160498,160498,160498,99888,160458,160458,160458,99904,160418,160418,160418,99920,160378,160378,160378,99936,160338,160338,160338,99952,
160298,160298,160298,99968,160258,160258,160258,99984,160218,160218,160218,100000,160178,160178,160178,100016,160138,160138,160138,100032,160098,160098,160098,100048,160058,160058,160058,100064,160018,160018,160018,100080,
160616,160616,160616,100096,160576,160576,160576,100112,160536,160536,160536,100128,160496,160496,160496,100144,160456,160456,160456,100160,160416,160416,160416,100176,160376,160376,160376,100192,160336,160336,160336,100208,
160296,160296,160296,100224,160256,160256,160256,100240,160216,160216,160216,100256,160176,160176,160176,100272,160136,160136,160136,100288,160096,160096,160096,100304,160056,160056,160056,100320,160016,160016,160006,100336,
160004,160004,159966,100352,159964,159964,159926,100368,159924,159924,159886,100384,159884,159884,159846,100400,159844,159844,159806,100416,159804,159804,159766,100432,159764,159764,159726,100448,159724,159724,159686,100464,
159684,159684,159646,100480,159644,159644,159606,100496,159604,159604,159566,100512,159564,159564,159526,100528,159524,159524,159486,100544,159484,159484,159446,100560,159444,159444,159406,100576,159404,159404,160002,100592,
160000,160000,159962,100608,159960,159960,159922,100624,159920,159920,159882,100640,159880,159880,159842,100656,159840,159840,159802,100672,159800,159800,159762,100688,159760,159760,159722,100704,159720,159720,159682,100720,
159680,159680,159642,100736,159640,159640,159602,100752,159600,159600,159562,100768,159560,159560,159522,100784,159520,159520,159482,100800,159480,159480,159442,100816,159440,159440,159402,100832,159400,159400,159998,100848,
159996,159996,159958,100864,159956,159956,159918,100880,159916,159916,159878,100896,159876,159876,159838,100912,159836,159836,159798,100928,159796,159796,159758,100944,159756,159756,159718,100960,159716,159716,159678,100976,
159676,159676,159638,100992,159636,159636,159598,101008,159596,159596,159558,101024,159556,159556,159518,101040,159516,159516,159478,101056,159476,159476,159438,101072,159436,159436,159398,101088,159396,159396,159994,101104,
159992,159992,159954,101120,159952,159952,159914,101136,159912,159912,159874,101152,159872,159872,159834,101168,159832,159832,159794,101184,159792,159792,159754,101200,159752,159752,159714,101216,159712,159712,159674,101232,
159672,159672,159634,101248,159632,159632,159594,101264,159592,159592,159554,101280,159552,159552,159514,101296,159512,159512,159474,101312,159472,159472,159434,101328,159432,159432,159394,101344,159392,159990,159988,101360,
159984,159950,159948,101376,159944,159910,159908,101392,159904,159870,159868,101408,159864,159830,159828,101424,159824,159790,159788,101440,159784,159750,159748,101456,159744,159710,159708,101472,159704,159670,159668,101488,
159664,159630,159628,101504,159624,159590,159588,101520,159584,159550,159548,101536,159544,159510,159508,101552,159504,159470,159468,101568,159464,159430,159428,101584,159424,159390,159388,101600,159384,159982,159980,101616,
159976,159942,159940,101632,159936,159902,159900,101648,159896,159862,159860,101664,159856,159822,159820,101680,159816,159782,159780,101696,159776,159742,159740,101712,159736,159702,159700,101728,159696,159662,159660,101744,
159656,159622,159620,101760,159616,159582,159580,101776,159576,159542,159540,101792,159536,159502,159500,101808,159496,159462,159460,101824,159456,159422,159420,101840,159416,159382,159380,101856,159366,159364,159362,101872,
159326,159324,159322,101888,159286,159284,159282,101904,159246,159244,159242,101920,159206,159204,159202,101936,159166,159164,159162,101952,159126,159124,159122,101968,159086,159084,159082,101984,159046,159044,159042,102000,
159006,159004,159002,102016,158966,158964,158962,102032,158926,158924,158922,102048,158886,158884,158882,102064,158846,158844,158842,102080,158806,158804,158802,102096,158766,158764,158762,102112,159350,159348,159346,102128,
159310,159308,159306,102144,159270,159268,159266,102160,159230,159228,159226,102176,159190,159188,159186,102192,159150,159148,159146,102208,159110,159108,159106,102224,159070,159068,159066,102240,159030,159028,159026,102256,
158990,158988,158986,102272,158950,158948,158946,102288,158910,158908,158906,102304,158870,158868,158866,102320,158830,158828,158826,102336,158790,158788,158786,102352,158750,158748,158746,102368,158720,158720,158720,102384,
102400,131584,158720,0,102404,131588,160656,0,102408,131592,158720,0,102412,131596,160656,0,102416,131600,158720,0,102420,131604,160656,0,102424,131608,158720,0,102428,131612,160656,0,
106560,131616,158720,0,106564,131620,160656,0,106568,131624,158720,0,106572,131628,160656,0,106576,131632,158720,0,106580,131636,160656,0,106584,131640,158720,0,106588,131644,160656,0,
110720,131648,158720,0,110724,131652,160656,0,110728,131656,158720,0,110732,131660,160656,0,110736,131664,158720,0,110740,131668,160656,0,110744,131672,158720,0,110748,131676,160656,0,
114880,131680,158720,0,114884,131684,160656,0,114888,131688,158720,0,114892,131692,160656,0,114896,131696,158720,0,114900,131700,160656,0,114904,131704,158720,0,114908,131708,160656,0,
119040,131712,158720,0,119044,131716,160656,0,119048,131720,158720,0,119052,131724,160656,0,119056,131728,158720,0,119060,131732,160656,0,119064,131736,158720,0,119068,131740,160656,0,
123200,131744,158720,0,123204,131748,160656,0,123208,131752,158720,0,123212,131756,160656,0,123216,131760,158720,0,123220,131764,160656,0,123224,131768,158720,0,123228,131772,160656,0,
127360,131776,158720,0,127364,131780,160656,0,127368,131784,158720,0,127372,131788,160656,0,127376,131792,158720,0,127380,131796,160656,0,127384,131800,158720,0,127388,131804,160656,0,
131520,131808,158720,0,131524,131812,160656,0,131528,131816,158720,0,131532,131820,160656,0,131536,131824,158720,0,131540,131828,160656,0,131544,131832,158720,0,131548,131836,160656,0,
102400,131840,158720,0,102404,131844,160656,0,102408,131848,158720,0,102412,131852,160656,0,102416,131856,158720,0,102420,131860,160656,0,102424,131864,158720,0,102428,131868,160656,0,
106560,131872,158720,0,106564,131876,160656,0,106568,131880,158720,0,106572,131884,160656,0,106576,131888,158720,0,106580,131892,160656,0,106584,131896,158720,0,106588,131900,160656,0,
110720,131904,158720,0,110724,131908,160656,0,110728,131912,158720,0,110732,131916,160656,0,110736,131920,158720,0,110740,131924,160656,0,110744,131928,158720,0,110748,131932,160656,0,
114880,131936,158720,0,114884,131940,160656,0,114888,131944,158720,0,114892,131948,160656,0,114896,131952,158720,0,114900,131956,160656,0,114904,131960,158720,0,114908,131964,160656,0,
119040,131968,158720,0,119044,131972,160656,0,119048,131976,158720,0,119052,131980,160656,0,119056,131984,158720,0,119060,131988,160656,0,119064,131992,158720,0,119068,131996,160656,0,
123200,132000,158720,0,123204,132004,160656,0,123208,132008,158720,0,123212,132012,160656,0,123216,132016,158720,0,123220,132020,160656,0,123224,132024,158720,0,123228,132028,160656,0,
127360,132032,158720,0,127364,132036,160656,0,127368,132040,158720,0,127372,132044,160656,0,127376,132048,158720,0,127380,132052,160656,0,127384,132056,158720,0,127388,132060,160656,0,
131520,132064,158720,0,131524,132068,160656,0,131528,132072,158720,0,131532,132076,160656,0,131536,132080,158720,0,131540,132084,160656,0,131544,132088,158720,0,131548,132092,160656,0,
102400,136192,158720,0,102404,136196,160656,0,102408,136200,158720,0,102412,136204,160656,0,102416,136208,158720,0,102420,136212,160656,0,102424,136216,158720,0,102428,136220,160656,0,
106560,136224,158720,0,106564,136228,160656,0,106568,136232,158720,0,106572,136236,160656,0,106576,136240,158720,0,106580,136244,160656,0,106584,136248,158720,0,106588,136252,160656,0,
110720,136256,158720,0,110724,136260,160656,0,110728,136264,158720,0,110732,136268,160656,0,110736,136272,158720,0,110740,136276,160656,0,110744,136280,158720,0,110748,136284,160656,0,
114880,136288,158720,0,114884,136292,160656,0,114888,136296,158720,0,114892,136300,160656,0,114896,136304,158720,0,114900,136308,160656,0,114904,136312,158720,0,114908,136316,160656,0,
119040,136320,158720,0,119044,136324,160656,0,119048,136328,158720,0,119052,136332,160656,0,119056,136336,158720,0,119060,136340,160656,0,119064,136344,158720,0,119068,136348,160656,0,
123200,136352,158720,0,123204,136356,160656,0,123208,136360,158720,0,123212,136364,160656,0,123216,136368,158720,0,123220,136372,160656,0,123224,136376,158720,0,123228,136380,160656,0,
127360,136384,158720,0,127364,136388,160656,0,127368,136392,158720,0,127372,136396,160656,0,127376,136400,158720,0,127380,136404,160656,0,127384,136408,158720,0,127388,136412,160656,0,
131520,136416,158720,0,131524,136420,160656,0,131528,136424,158720,0,131532,136428,160656,0,131536,136432,158720,0,131540,136436,160656,0,131544,136440,158720,0,131548,136444,160656,0,
102400,136448,158720,0,102404,136452,160656,0,102408,136456,158720,0,102412,136460,160656,0,102416,136464,158720,0,102420,136468,160656,0,102424,136472,158720,0,102428,136476,160656,0,
106560,136480,158720,0,106564,136484,160656,0,106568,136488,158720,0,106572,136492,160656,0,106576,136496,158720,0,106580,136500,160656,0,106584,136504,158720,0,106588,136508,160656,0,
110720,136512,158720,0,110724,136516,160656,0,110728,136520,158720,0,110732,136524,160656,0,110736,136528,158720,0,110740,136532,160656,0,110744,136536,158720,0,110748,136540,160656,0,
114880,136544,158720,0,114884,136548,160656,0,114888,136552,158720,0,114892,136556,160656,0,114896,136560,158720,0,114900,136564,160656,0,114904,136568,158720,0,114908,136572,160656,0,
119040,136576,158720,0,119044,136580,160656,0,119048,136584,158720,0,119052,136588,160656,0,119056,136592,158720,0,119060,136596,160656,0,119064,136600,158720,0,119068,136604,160656,0,
123200,136608,158720,0,123204,136612,160656,0,123208,136616,158720,0,123212,136620,160656,0,123216,136624,158720,0,123220,136628,160656,0,123224,136632,158720,0,123228,136636,160656,0,
127360,136640,158720,0,127364,136644,160656,0,127368,136648,158720,0,127372,136652,160656,0,127376,136656,158720,0,127380,136660,160656,0,127384,136664,158720,0,127388,136668,160656,0,
131520,136672,158720,0,131524,136676,160656,0,131528,136680,158720,0,131532,136684,160656,0,131536,136688,158720,0,131540,136692,160656,0,131544,136696,158720,0,131548,136700,160656,0
};

__global__ void __launch_bounds__(256, 1) fused_ab2(Params p) {
  asm volatile(
    "s_mov_b64 s[76:77], %0\n"
    "s_mov_b32 s93, %1\n"
    "s_mov_b64 s[96:97], %2\n"
    "v_mov_b32 v0, %3\n"
    ".L_begin:\n"
    "s_getpc_b64 s[24:25]\n"
    "s_load_dwordx16 s[28:43], s[76:77], 0\n"
    "s_load_dwordx4 s[44:47], s[76:77], 64\n"
    "s_load_dwordx2 s[48:49], s[76:77], 80\n"
    "v_mov_b32 v57, v0\n"
    "v_readfirstlane_b32 s53, v0\n"
    "v_lshlrev_b32 v0, 4, v0\n"
    "s_and_b32 s97, s97, 0xffff\n"
    "s_mov_b32 s98, 61440\n"
    "s_mov_b32 s99, 0x20000\n"
    "s_lshr_b32 s89, s93, 3\n"
    "s_and_b32 s91, s89, 15\n"
    "s_lshl_b32 s91, s91, 12\n"
    "s_mov_b32 s26, .L_end-.L_begin\n"
    "s_mov_b32 s27, 0x20000\n"
    "s_cmp_lt_u32 s89, 16\n"
    "s_cselect_b64 s[24:25], s[24:25], s[96:97]\n"
    "s_cselect_b32 s26, s26, s98\n"
    "s_and_b32 s25, s25, 0xffff\n"
    "buffer_load_dwordx4 v[228:231], v0, s[24:27], s91 offen\n"
    "s_mov_b32 s92, 12288\n"
    "buffer_load_dwordx4 v[240:243], v0, s[96:99], s92 offen\n"
    "s_mov_b32 s92, 40960\n"
    "buffer_load_dwordx4 v[252:255], v0, s[96:99], s92 offen\n"
    "buffer_load_dwordx4 v[4:7], v0, s[96:99], 0 offen\n"
    "s_mov_b32 s92, 16384\n"
    "buffer_load_dwordx4 v[244:247], v0, s[96:99], s92 offen\n"
    "s_and_b32 s90, s93, 7\n"
    "s_lshl_b32 s50, s90, 5\n"
    "s_add_u32 s50, s50, s89\n"
    "v_and_b32 v1, 0x3f0, v0\n"
    "s_lshr_b32 s53, s53, 6\n"
    "s_and_b32 s52, s50, 7\n"
    "s_lshr_b32 s51, s50, 3\n"
    "s_lshr_b32 s54, s52, 2\n"
    "s_and_b32 s55, s52, 3\n"
    "s_lshl_b32 s63, s53, 12\n"
    "s_mov_b32 s6, 0xb3a000\n"
    "s_mov_b32 s7, 0x20000\n"
    "s_mov_b32 s10, 0x200000\n"
    "s_mov_b32 s11, 0x20000\n"
    "s_mov_b32 s18, 0x180000\n"
    "s_mov_b32 s19, 0x20000\n"
    "s_mov_b32 s14, 0x200000\n"
    "s_mov_b32 s15, 0x20000\n"
    "s_mov_b32 s22, 0x80000\n"
    "s_mov_b32 s23, 0x20000\n"
    "s_lshl_b32 s90, s50, 9\n"
    "v_lshl_add_u32 v61, v57, 1, s90\n"
    "s_lshl_b32 s91, s53, 2\n"
    "s_and_b32 s92, s53, 2\n"
    "s_xor_b32 s91, s91, s92\n"
    "s_lshl_b32 s91, s91, 4\n"
    "v_xor_b32 v232, s91, v1\n"
    "v_lshrrev_b32 v66, 3, v57\n"
    "v_mul_u32_u24 v66, 0xc00, v66\n"
    "v_bfe_u32 v67, v57, 4, 1\n"
    "v_lshlrev_b32 v67, 5, v67\n"
    "v_and_b32 v68, 64, v57\n"
    "v_or_b32 v67, v67, v68\n"
    "v_and_b32 v68, 0x70, v1\n"
    "v_xor_b32 v67, v67, v68\n"
    "s_lshl_b32 s91, s52, 7\n"
    "v_add3_u32 v236, v66, v67, s91\n"
    "v_add_u32 v237, 97280, v236\n"
    "v_add_u32 v238, 194560, v236\n"
    "v_add_u32 v239, 291840, v236\n"
    "s_lshl_b32 s58, s51, 15\n"
    "s_add_u32 s58, s58, s63\n"
    "s_add_u32 s80, s58, 0x0\n"
    "s_add_u32 s81, s80, 0x4000\n"
    "s_sub_u32 s79, s80, 0x4000\n"
    "s_cmp_eq_u32 s51, 0\n"
    "s_cselect_b32 s79, s80, s79\n"
    "s_lshl_b32 s89, s50, 2\n"
    "v_mov_b32 v72, 0xa11000\n"
    "v_lshlrev_b32 v224, 3, v1\n"
    "s_cmp_eq_u32 s53, 0\n"
    "s_cselect_b32 s90, 6144, 0\n"
    "s_cmp_eq_u32 s53, 1\n"
    "s_cselect_b32 s90, 4096, s90\n"
    "s_cmp_eq_u32 s53, 2\n"
    "s_cselect_b32 s90, 2048, s90\n"
    "s_cmp_eq_u32 s53, 3\n"
    "s_cselect_b32 s90, 4096, s90\n"
    "s_cselect_b32 s91, 256, 0\n"
    "s_add_u32 s91, s91, s50\n"
    "s_mul_i32 s91, s90, s91\n"
    "s_add_u32 s26, s90, s91\n"
    "s_cmp_eq_u32 s53, 2\n"
    "s_waitcnt lgkmcnt(0)\n"
    "global_load_ushort v212, v61, s[34:35]\n"
    "global_load_ushort v213, v61, s[36:37]\n"
    "global_load_ushort v214, v61, s[38:39]\n"
    "global_load_ushort v215, v61, s[40:41]\n"
    "s_cselect_b64 s[24:25], s[32:33], s[30:31]\n"
    "s_bitcmp1_b32 s53, 0\n"
    "s_cselect_b64 s[24:25], s[28:29], s[24:25]\n"
    "s_and_b32 s25, s25, 0xffff\n"
    "s_load_dword s60, s[42:43], s89\n"
    "s_load_dword s61, s[44:45], s89\n"
    "s_mov_b32 s20, s32\n"
    "s_and_b32 s21, s33, 0xffff\n"
    "s_lshr_b32 s90, s30, 7\n"
    "s_add_u32 s90, s90, s52\n"
    "s_and_b32 s90, s90, 7\n"
    "s_cmp_eq_u32 s90, 3\n"
    "s_cselect_b32 s90, s51, 0\n"
    "s_lshr_b32 s33, s93, 1\n"
    "s_xor_b32 s33, s33, s90\n"
    "s_and_b32 s33, s33, 3\n"
    "s_lshl_b32 s33, s33, 8\n"
    "s_and_b32 s93, s51, 3\n"
    "v_xor_b32 v232, s33, v232\n"
    "s_mov_b32 s8, s28\n"
    "s_and_b32 s9, s29, 0xffff\n"
    "s_add_u32 m0, s63, 0\n"
    "v_xor_b32 v233, 16, v232\n"
    "buffer_load_dwordx4 v232, s[8:11], s79 offen lds\n"
    "v_xor_b32 v234, 32, v232\n"
    "v_xor_b32 v235, 48, v232\n"
    "buffer_load_dwordx4 v233, s[8:11], s79 offen offset:1024 lds\n"
    "v_min_u32 v73, 1, v1\n"
    "s_mov_b32 s4, s48\n"
    "s_and_b32 s5, s49, 0xffff\n"
    "v_lshl_or_b32 v72, v73, 30, v72\n"
    "s_xor_b32 s28, s33, 0\n"
    "s_mul_i32 s28, s28, 1536\n"
    "s_mov_b32 s16, s30\n"
    "s_and_b32 s17, s31, 0xffff\n"
    "s_mov_b32 s77, 4096\n"
    "s_add_u32 s76, s28, 0x800\n"
    "v_mov_b32 v40, 0x3f80\n"
    "s_lshl1_add_u32 s40, s51, s54\n"
    "s_lshl_b32 s41, s40, 6\n"
    "s_lshl_b32 s85, s55, 13\n"
    "buffer_load_dwordx4 v234, s[8:11], s79 offen offset:2048 lds\n"
    "s_add_u32 s85, s85, s41\n"
    "s_lshl2_add_u32 s39, s52, s53\n"
    "s_lshl_b32 s83, s51, 15\n"
    "s_lshl_b32 s39, s39, 9\n"
    "s_add_u32 s83, s83, s39\n"
    "s_lshl2_add_u32 s84, s55, s53\n"
    "s_lshl_b32 s84, s84, 17\n"
    "s_add_u32 s84, s84, s41\n"
    "s_add_u32 s90, s84, 0x400000\n"
    "s_xor_b32 s29, s33, 256\n"
    "s_mul_i32 s29, s29, 1536\n"
    "s_add_u32 s92, s29, 0x400\n"
    "s_mov_b32 s39, 8192\n"
    "v_mov_b32 v120, 0x3f803f80\n"
    "buffer_load_dwordx4 v235, s[8:11], s79 offen offset:3072 lds\n"
    "s_add_u32 m0, s63, 16384\n"
    "v_mov_b32 v121, 0x3f803f80\n"
    "v_mov_b32 v122, 0x3f803f80\n"
    "v_mov_b32 v123, 0x3f803f80\n"
    "s_add_u32 s38, s29, 0x800\n"
    "s_mov_b32 s64, 0xaaaaaaaa\n"
    "s_mov_b32 s65, 0xaaaaaaaa\n"
    "s_mov_b32 s70, 0xcccccccc\n"
    "s_mov_b32 s71, 0xcccccccc\n"
    "s_xor_b32 s31, s33, 512\n"
    "s_lshl4_add_u32 s40, s40, s93\n"
    "s_mul_i32 s31, s31, 1536\n"
    "s_lshl_b32 s82, s40, 10\n"
    "s_add_u32 s40, s31, 0x400\n"
    "buffer_load_dwordx4 v232, s[8:11], s80 offen lds\n"
    "s_add_u32 s41, s31, 0x800\n"
    "s_and_b32 s87, s50, 0xc0\n"
    "s_xor_b32 s34, s33, 768\n"
    "s_mul_i32 s34, s34, 1536\n"
    "s_lshl3_add_u32 s78, s51, s52\n"
    "s_lshl_b32 s78, s78, 7\n"
    "v_add_u32 v104, 147456, v1\n"
    "s_mov_b32 s66, 0xfff0ffff\n"
    "s_mov_b32 s67, 0xf000ff00\n"
    "s_mov_b32 s68, 0xffc0fffc\n"
    "s_mov_b32 s69, 0xc000fc00\n"
    "v_mov_b32 v251, 0xceaba950\n"
    "v_lshlrev_b32 v110, 3, v1\n"
    "s_lshl_b32 s37, s51, 10\n"
    "buffer_load_dwordx4 v233, s[8:11], s80 offen offset:1024 lds\n"
    "v_and_b32 v110, 0x380, v110\n"
    "s_add_u32 s37, s37, 0xb32000\n"
    "s_mov_b32 s57, 40000\n"
    "s_mov_b32 s12, s46\n"
    "s_add_u32 s82, s82, s63\n"
    "s_add_u32 s46, s82, 0x0\n"
    "s_and_b32 s13, s47, 0xffff\n"
    "s_xor_b32 s47, s46, 1024\n"
    "s_xor_b32 s48, s46, 2048\n"
    "s_xor_b32 s49, s46, 3072\n"
    "s_mov_b32 s35, 0x05040100\n"
    "s_mov_b32 s36, 0x07060302\n"
    "s_lshl_b32 s86, s50, 13\n"
    "s_mov_b32 s30, 0xa02000\n"
    "buffer_load_dwordx4 v234, s[8:11], s80 offen offset:2048 lds\n"
    "buffer_load_dwordx4 v235, s[8:11], s80 offen offset:3072 lds\n"
    "s_add_u32 m0, s63, 32768\n"
    "s_add_u32 s80, s58, 0x100000\n"
    "s_sub_u32 s79, s80, 0x4000\n"
    "buffer_load_dwordx4 v232, s[8:11], s81 offen lds\n"
    "buffer_load_dwordx4 v233, s[8:11], s81 offen offset:1024 lds\n"
    "s_waitcnt lgkmcnt(0)\n"
    "s_add_u32 s45, s28, 0x400\n"
    "s_add_u32 s88, s85, 0xa00000\n"
    "s_add_u32 s89, s83, 0x100000\n"
    "s_and_b32 s42, s50, 3\n"
    "buffer_load_dwordx4 v234, s[8:11], s81 offen offset:2048 lds\n"
    "s_lshl4_add_u32 s87, s42, s87\n"
    "s_bfe_u32 s42, s50, 0x40002\n"
    "s_add_u32 s87, s87, s42\n"
    "s_add_u32 s42, s34, 0x400\n"
    "s_lshl_b32 s43, s55, 8\n"
    "s_xor_b32 s59, s43, s33\n"
    "s_lshl_b32 s44, s54, 14\n"
    "s_add_u32 s59, s59, s44\n"
    "s_add_u32 s44, s34, 0x800\n"
    "s_lshl_b32 s87, s87, 6\n"
    "s_cmp_eq_u32 s51, 0\n"
    "s_cselect_b64 s[72:73], s[66:67], -1\n"
    "s_cselect_b64 s[74:75], s[68:69], -1\n"
    "s_and_b32 s62, s61, 0xffff0000\n"
    "buffer_load_dwordx4 v235, s[8:11], s81 offen offset:3072 lds\n"
    "s_add_u32 m0, s63, 49152\n"
    "s_add_u32 s81, s80, 0x4000\n"
    "s_lshl_b32 s61, s61, 16\n"
    "s_waitcnt vmcnt(20)\n"
    "v_mov_b32 v228, -1\n"
    "buffer_load_dword v248, v224, s[24:27], s91 offen\n"
    "s_and_b32 s91, s53, 2\n"
    "s_lshl_b32 s91, s91, 3\n"
    "s_lshr_b32 s60, s60, s91\n"
    "s_lshl_b32 s60, s60, 16\n"
    "v_mov_b32 v210, s60\n"
    "v_mul_f32 v210, 0x3fb8aa3b, v210\n"
    "v_exp_f32 v210, v210\n"
    "v_mov_b32 v229, -1\n"
    "v_cvt_pk_bf16_f32 v210, v210, v210\n"
    "v_lshlrev_b32 v210, 16, v210\n"
    "v_mov_b32 v230, -1\n"
    "s_waitcnt vmcnt(20)\n"
    "v_add_u32 v129, 528, v243\n"
    "v_add_u32 v130, 1056, v243\n"
    "v_add_u32 v131, 1584, v243\n"
    "v_mov_b32 v231, -1\n"
    "v_add_u32 v132, 2112, v243\n"
    "v_add_u32 v133, 2640, v243\n"
    "v_add_u32 v134, 3168, v243\n"
    "v_add_u32 v135, 3696, v243\n"
    "v_add_u32 v136, 4224, v243\n"
    "v_add_u32 v137, 4752, v243\n"
    "v_add_u32 v138, 5280, v243\n"
    "v_add_u32 v139, 5808, v243\n"
    "v_add_u32 v140, 6336, v243\n"
    "v_add_u32 v141, 6864, v243\n"
    "v_add_u32 v142, 7392, v243\n"
    "v_add_u32 v143, 7920, v243\n"
    "v_mov_b32 v3, v240\n"
    "v_xor_b32 v152, 0, v241\n"
    "v_xor_b32 v153, 1024, v241\n"
    "v_xor_b32 v154, 2048, v241\n"
    "v_xor_b32 v155, 3072, v241\n"
    "v_add_u32 v156, 49152, v152\n"
    "v_add_u32 v157, 49152, v153\n"
    "v_add_u32 v158, 49152, v154\n"
    "v_add_u32 v159, 49152, v155\n"
    "v_xor_b32 v89, 64, v3\n"
    "v_xor_b32 v90, 128, v3\n"
    "v_xor_b32 v91, 192, v3\n"
    "v_add_u32 v111, s43, v242\n"
    "s_add_u32 s43, s78, 0xb32000\n"
    "v_add_u32 v88, 49152, v3\n"
    "s_xor_b32 s91, s93, 2\n"
    "s_lshl_b32 s91, s91, 15\n"
    "s_waitcnt vmcnt(18)\n"
    "v_xor_b32 v5, s33, v5\n"
    "s_waitcnt vmcnt(16)\n"
    "v_lshlrev_b32 v212, 16, v212\n"
    "v_mul_f32 v212, 0x3fb8aa3b, v212\n"
    "v_exp_f32 v212, v212\n"
    "s_waitcnt vmcnt(15)\n"
    "v_lshlrev_b32 v213, 16, v213\n"
    "v_cvt_pk_bf16_f32 v212, v212, v212\n"
    "v_lshlrev_b32 v212, 16, v212\n"
    "v_mul_f32 v213, v213, v210\n"
    "v_mul_f32 v212, v212, v210\n"
    "v_mul_f32 v213, 0x3e22f983, v213\n"
    "v_mul_f32 v212, 0xbfb8aa3b, v212\n"
    "v_fract_f32 v171, v213\n"
    "v_exp_f32 v170, v212\n"
    "v_cos_f32 v160, v171\n"
    "v_sin_f32 v161, v171\n"
    "v_add_f32 v210, v210, v210\n"
    "v_mul_f32 v160, v160, v170\n"
    "v_mul_f32 v161, v161, v170\n"
    "s_waitcnt vmcnt(14)\n"
    "v_lshlrev_b32 v214, 16, v214\n"
    "s_waitcnt vmcnt(13)\n"
    "v_lshlrev_b32 v215, 16, v215\n"
    "v_mul_f32 v214, v214, v210\n"
    "v_mul_f32 v215, v215, v210\n"
    "v_mul_f32 v175, 0xc1000000, v213\n"
    "v_pk_mul_f32 v[182:183], v[160:161], v[160:161] op_sel:[0,0] op_sel_hi:[0,1]\n"
    "v_mul_f32 v174, 0x41000000, v212\n"
    "v_fract_f32 v175, v175\n"
    "v_cvt_pk_bf16_f32 v8, v214, v215\n"
    "v_exp_f32 v174, v174\n"
    "v_cos_f32 v162, v175\n"
    "v_sin_f32 v163, v175\n"
    "v_pk_fma_f32 v[166:167], v[160:161], v[160:161], v[182:183] op_sel:[1,1,0] op_sel_hi:[1,0,1] neg_lo:[0,1,0]\n"
    "v_pk_mul_f32 v[188:189], v[214:215], v[160:161] op_sel:[0,0] op_sel_hi:[0,1]\n"
    "v_mul_f32 v179, 0x43000000, v213\n"
    "v_mul_f32 v178, 0x43000000, v212\n"
    "v_fract_f32 v179, v179\n"
    "v_pk_mul_f32 v[184:185], v[166:167], v[166:167] op_sel:[0,0] op_sel_hi:[0,1]\n"
    "v_pk_fma_f32 v[198:199], v[214:215], v[160:161], v[188:189] op_sel:[1,1,0] op_sel_hi:[1,0,1] neg_lo:[0,1,0]\n"
    "v_mul_f32 v162, v162, v174\n"
    "v_mul_f32 v163, v163, v174\n"
    "ds_write2st64_b32 v243, v8, v40 offset0:0 offset1:66\n"
    "v_exp_f32 v178, v178\n"
    "v_cos_f32 v164, v179\n"
    "v_sin_f32 v165, v179\n"
    "v_pk_fma_f32 v[168:169], v[166:167], v[166:167], v[184:185] op_sel:[1,1,0] op_sel_hi:[1,0,1] neg_lo:[0,1,0]\n"
    "v_cvt_pk_bf16_f32 v9, v198, v199\n"
    "v_cvt_pk_bf16_f32 v41, v162, v163\n"
    "v_pk_mul_f32 v[182:183], v[162:163], v[162:163] op_sel:[0,0] op_sel_hi:[0,1]\n"
    "v_pk_mul_f32 v[184:185], v[198:199], v[160:161] op_sel:[0,0] op_sel_hi:[0,1]\n"
    "v_mul_f32 v164, v164, v178\n"
    "v_mul_f32 v165, v165, v178\n"
    "v_pk_mul_f32 v[186:187], v[214:215], v[164:165] op_sel:[0,0] op_sel_hi:[0,1]\n"
    "v_pk_fma_f32 v[204:205], v[162:163], v[162:163], v[182:183] op_sel:[1,1,0] op_sel_hi:[1,0,1] neg_lo:[0,1,0]\n"
    "v_pk_fma_f32 v[196:197], v[198:199], v[160:161], v[184:185] op_sel:[1,1,0] op_sel_hi:[1,0,1] neg_lo:[0,1,0]\n"
    "ds_write2st64_b32 v129, v9, v41 offset0:0 offset1:66\n"
    "v_pk_fma_f32 v[190:191], v[214:215], v[164:165], v[186:187] op_sel:[1,1,0] op_sel_hi:[1,0,1] neg_lo:[0,1,0]\n"
    "v_cvt_pk_bf16_f32 v10, v196, v197\n"
    "v_cvt_pk_bf16_f32 v42, v204, v205\n"
    "v_pk_mul_f32 v[186:187], v[204:205], v[162:163] op_sel:[0,0] op_sel_hi:[0,1]\n"
    "v_pk_mul_f32 v[188:189], v[196:197], v[160:161] op_sel:[0,0] op_sel_hi:[0,1]\n"
    "v_pk_fma_f32 v[206:207], v[204:205], v[162:163], v[186:187] op_sel:[1,1,0] op_sel_hi:[1,0,1] neg_lo:[0,1,0]\n"
    "v_pk_fma_f32 v[198:199], v[196:197], v[160:161], v[188:189] op_sel:[1,1,0] op_sel_hi:[1,0,1] neg_lo:[0,1,0]\n"
    "ds_write2st64_b32 v130, v10, v42 offset0:0 offset1:66\n"
    "v_cvt_pk_bf16_f32 v11, v198, v199\n"
    "v_cvt_pk_bf16_f32 v43, v206, v207\n"
    "v_pk_mul_f32 v[182:183], v[206:207], v[162:163] op_sel:[0,0] op_sel_hi:[0,1]\n"
    "v_pk_mul_f32 v[184:185], v[198:199], v[160:161] op_sel:[0,0] op_sel_hi:[0,1]\n"
    "v_pk_fma_f32 v[204:205], v[206:207], v[162:163], v[182:183] op_sel:[1,1,0] op_sel_hi:[1,0,1] neg_lo:[0,1,0]\n"
    "v_pk_fma_f32 v[196:197], v[198:199], v[160:161], v[184:185] op_sel:[1,1,0] op_sel_hi:[1,0,1] neg_lo:[0,1,0]\n"
    "ds_write2st64_b32 v131, v11, v43 offset0:0 offset1:66\n"
    "v_cvt_pk_bf16_f32 v12, v196, v197\n"
    "v_cvt_pk_bf16_f32 v44, v204, v205\n"
    "v_pk_mul_f32 v[186:187], v[204:205], v[162:163] op_sel:[0,0] op_sel_hi:[0,1]\n"
    "v_pk_mul_f32 v[188:189], v[196:197], v[160:161] op_sel:[0,0] op_sel_hi:[0,1]\n"
    "v_pk_fma_f32 v[206:207], v[204:205], v[162:163], v[186:187] op_sel:[1,1,0] op_sel_hi:[1,0,1] neg_lo:[0,1,0]\n"
    "v_pk_fma_f32 v[198:199], v[196:197], v[160:161], v[188:189] op_sel:[1,1,0] op_sel_hi:[1,0,1] neg_lo:[0,1,0]\n"
    "ds_write2st64_b32 v132, v12, v44 offset0:0 offset1:66\n"
    "v_cvt_pk_bf16_f32 v13, v198, v199\n"
    "v_cvt_pk_bf16_f32 v45, v206, v207\n"
    "v_pk_mul_f32 v[182:183], v[206:207], v[162:163] op_sel:[0,0] op_sel_hi:[0,1]\n"
    "v_pk_mul_f32 v[184:185], v[198:199], v[160:161] op_sel:[0,0] op_sel_hi:[0,1]\n"
    "v_pk_fma_f32 v[204:205], v[206:207], v[162:163], v[182:183] op_sel:[1,1,0] op_sel_hi:[1,0,1] neg_lo:[0,1,0]\n"
    "v_pk_fma_f32 v[196:197], v[198:199], v[160:161], v[184:185] op_sel:[1,1,0] op_sel_hi:[1,0,1] neg_lo:[0,1,0]\n"
    "ds_write2st64_b32 v133, v13, v45 offset0:0 offset1:66\n"
    "v_cvt_pk_bf16_f32 v14, v196, v197\n"
    "v_cvt_pk_bf16_f32 v46, v204, v205\n"
    "v_pk_mul_f32 v[186:187], v[204:205], v[162:163] op_sel:[0,0] op_sel_hi:[0,1]\n"
    "v_pk_mul_f32 v[188:189], v[196:197], v[160:161] op_sel:[0,0] op_sel_hi:[0,1]\n"
    "v_pk_fma_f32 v[206:207], v[204:205], v[162:163], v[186:187] op_sel:[1,1,0] op_sel_hi:[1,0,1] neg_lo:[0,1,0]\n"
    "v_pk_fma_f32 v[198:199], v[196:197], v[160:161], v[188:189] op_sel:[1,1,0] op_sel_hi:[1,0,1] neg_lo:[0,1,0]\n"
    "ds_write2st64_b32 v134, v14, v46 offset0:0 offset1:66\n"
    "v_cvt_pk_bf16_f32 v15, v198, v199\n"
    "v_cvt_pk_bf16_f32 v47, v206, v207\n"
    "v_pk_mul_f32 v[182:183], v[206:207], v[162:163] op_sel:[0,0] op_sel_hi:[0,1]\n"
    "ds_write2st64_b32 v135, v15, v47 offset0:0 offset1:66\n"
    "v_pk_fma_f32 v[204:205], v[206:207], v[162:163], v[182:183] op_sel:[1,1,0] op_sel_hi:[1,0,1] neg_lo:[0,1,0]\n"
    "v_cvt_pk_bf16_f32 v16, v190, v191\n"
    "v_cvt_pk_bf16_f32 v48, v204, v205\n"
    "v_pk_mul_f32 v[184:185], v[204:205], v[162:163] op_sel:[0,0] op_sel_hi:[0,1]\n"
    "v_pk_mul_f32 v[188:189], v[190:191], v[160:161] op_sel:[0,0] op_sel_hi:[0,1]\n"
    "v_pk_fma_f32 v[206:207], v[204:205], v[162:163], v[184:185] op_sel:[1,1,0] op_sel_hi:[1,0,1] neg_lo:[0,1,0]\n"
    "v_pk_fma_f32 v[202:203], v[190:191], v[160:161], v[188:189] op_sel:[1,1,0] op_sel_hi:[1,0,1] neg_lo:[0,1,0]\n"
    "ds_write2st64_b32 v136, v16, v48 offset0:0 offset1:66\n"
    "buffer_load_dword v72, v72, s[4:7], 0 offen sc1\n"
    "buffer_load_dwordx4 v236, s[16:19], s45 offen lds\n"
    "v_cvt_pk_bf16_f32 v17, v202, v203\n"
    "v_cvt_pk_bf16_f32 v49, v206, v207\n"
    "v_pk_mul_f32 v[182:183], v[206:207], v[162:163] op_sel:[0,0] op_sel_hi:[0,1]\n"
    "v_pk_mul_f32 v[184:185], v[202:203], v[160:161] op_sel:[0,0] op_sel_hi:[0,1]\n"
    "v_pk_mul_f32 v[186:187], v[190:191], v[164:165] op_sel:[0,0] op_sel_hi:[0,1]\n"
    "v_pk_fma_f32 v[204:205], v[206:207], v[162:163], v[182:183] op_sel:[1,1,0] op_sel_hi:[1,0,1] neg_lo:[0,1,0]\n"
    "v_pk_fma_f32 v[200:201], v[202:203], v[160:161], v[184:185] op_sel:[1,1,0] op_sel_hi:[1,0,1] neg_lo:[0,1,0]\n"
    "v_pk_fma_f32 v[192:193], v[190:191], v[164:165], v[186:187] op_sel:[1,1,0] op_sel_hi:[1,0,1] neg_lo:[0,1,0]\n"
    "v_cvt_pk_bf16_f32 v18, v200, v201\n"
    "v_cvt_pk_bf16_f32 v50, v204, v205\n"
    "buffer_load_dwordx4 v237, s[16:19], s45 offen offset:1024 lds\n"
    "ds_write2st64_b32 v137, v17, v49 offset0:0 offset1:66\n"
    "v_pk_mul_f32 v[186:187], v[204:205], v[162:163] op_sel:[0,0] op_sel_hi:[0,1]\n"
    "v_pk_mul_f32 v[188:189], v[200:201], v[160:161] op_sel:[0,0] op_sel_hi:[0,1]\n"
    "v_pk_fma_f32 v[206:207], v[204:205], v[162:163], v[186:187] op_sel:[1,1,0] op_sel_hi:[1,0,1] neg_lo:[0,1,0]\n"
    "v_pk_fma_f32 v[202:203], v[200:201], v[160:161], v[188:189] op_sel:[1,1,0] op_sel_hi:[1,0,1] neg_lo:[0,1,0]\n"
    "v_pk_mul_f32 v[182:183], v[206:207], v[162:163] op_sel:[0,0] op_sel_hi:[0,1]\n"
    "v_pk_mul_f32 v[184:185], v[202:203], v[160:161] op_sel:[0,0] op_sel_hi:[0,1]\n"
    "ds_write2st64_b32 v138, v18, v50 offset0:0 offset1:66\n"
    "v_cvt_pk_bf16_f32 v19, v202, v203\n"
    "v_cvt_pk_bf16_f32 v51, v206, v207\n"
    "v_pk_fma_f32 v[204:205], v[206:207], v[162:163], v[182:183] op_sel:[1,1,0] op_sel_hi:[1,0,1] neg_lo:[0,1,0]\n"
    "v_pk_fma_f32 v[200:201], v[202:203], v[160:161], v[184:185] op_sel:[1,1,0] op_sel_hi:[1,0,1] neg_lo:[0,1,0]\n"
    "v_pk_mul_f32 v[186:187], v[204:205], v[162:163] op_sel:[0,0] op_sel_hi:[0,1]\n"
    "buffer_load_dwordx4 v238, s[16:19], s45 offen offset:2048 lds\n"
    "ds_write2st64_b32 v139, v19, v51 offset0:0 offset1:66\n"
    "v_pk_mul_f32 v[188:189], v[200:201], v[160:161] op_sel:[0,0] op_sel_hi:[0,1]\n"
    "v_cvt_pk_bf16_f32 v20, v200, v201\n"
    "v_cvt_pk_bf16_f32 v52, v204, v205\n"
    "v_pk_fma_f32 v[206:207], v[204:205], v[162:163], v[186:187] op_sel:[1,1,0] op_sel_hi:[1,0,1] neg_lo:[0,1,0]\n"
    "v_pk_fma_f32 v[202:203], v[200:201], v[160:161], v[188:189] op_sel:[1,1,0] op_sel_hi:[1,0,1] neg_lo:[0,1,0]\n"
    "v_pk_mul_f32 v[182:183], v[206:207], v[162:163] op_sel:[0,0] op_sel_hi:[0,1]\n"
    "v_pk_mul_f32 v[184:185], v[202:203], v[160:161] op_sel:[0,0] op_sel_hi:[0,1]\n"
    "v_pk_fma_f32 v[204:205], v[206:207], v[162:163], v[182:183] op_sel:[1,1,0] op_sel_hi:[1,0,1] neg_lo:[0,1,0]\n"
    "v_pk_fma_f32 v[200:201], v[202:203], v[160:161], v[184:185] op_sel:[1,1,0] op_sel_hi:[1,0,1] neg_lo:[0,1,0]\n"
    "ds_write2st64_b32 v140, v20, v52 offset0:0 offset1:66\n"
    "v_cvt_pk_bf16_f32 v21, v202, v203\n"
    "v_cvt_pk_bf16_f32 v53, v206, v207\n"
    "buffer_load_dwordx4 v239, s[16:19], s45 offen offset:3072 lds\n"
    "s_add_u32 m0, s63, 65536\n"
    "v_pk_mul_f32 v[184:185], v[192:193], v[166:167] op_sel:[0,0] op_sel_hi:[0,1]\n"
    "s_xor_b32 s45, s93, 0\n"
    "ds_write2st64_b32 v141, v21, v53 offset0:0 offset1:66\n"
    "v_pk_mul_f32 v[186:187], v[204:205], v[162:163] op_sel:[0,0] op_sel_hi:[0,1]\n"
    "v_pk_mul_f32 v[182:183], v[192:193], v[164:165] op_sel:[0,0] op_sel_hi:[0,1]\n"
    "v_pk_fma_f32 v[198:199], v[192:193], v[166:167], v[184:185] op_sel:[1,1,0] op_sel_hi:[1,0,1] neg_lo:[0,1,0]\n"
    "v_pk_fma_f32 v[206:207], v[204:205], v[162:163], v[186:187] op_sel:[1,1,0] op_sel_hi:[1,0,1] neg_lo:[0,1,0]\n"
    "v_pk_mul_f32 v[188:189], v[200:201], v[160:161] op_sel:[0,0] op_sel_hi:[0,1]\n"
    "v_pk_fma_f32 v[194:195], v[192:193], v[164:165], v[182:183] op_sel:[1,1,0] op_sel_hi:[1,0,1] neg_lo:[0,1,0]\n"
    "v_pk_mul_f32 v[186:187], v[198:199], v[166:167] op_sel:[0,0] op_sel_hi:[0,1]\n"
    "v_cvt_pk_bf16_f32 v22, v200, v201\n"
    "v_cvt_pk_bf16_f32 v54, v204, v205\n"
    "v_pk_fma_f32 v[202:203], v[200:201], v[160:161], v[188:189] op_sel:[1,1,0] op_sel_hi:[1,0,1] neg_lo:[0,1,0]\n"
    "v_pk_fma_f32 v[196:197], v[198:199], v[166:167], v[186:187] op_sel:[1,1,0] op_sel_hi:[1,0,1] neg_lo:[0,1,0]\n"
    "v_pk_mul_f32 v[184:185], v[194:195], v[166:167] op_sel:[0,0] op_sel_hi:[0,1]\n"
    "v_cvt_pk_bf16_f32 v23, v202, v203\n"
    "buffer_load_dwordx4 v236, s[16:19], s28 offen lds\n"
    "ds_write2st64_b32 v142, v22, v54 offset0:0 offset1:66\n"
    "v_pk_mul_f32 v[182:183], v[194:195], v[164:165] op_sel:[0,0] op_sel_hi:[0,1]\n"
    "v_pk_mul_f32 v[188:189], v[196:197], v[166:167] op_sel:[0,0] op_sel_hi:[0,1]\n"
    "v_pk_fma_f32 v[202:203], v[194:195], v[166:167], v[184:185] op_sel:[1,1,0] op_sel_hi:[1,0,1] neg_lo:[0,1,0]\n"
    "v_cvt_pk_bf16_f32 v25, v198, v199\n"
    "v_pk_fma_f32 v[190:191], v[194:195], v[164:165], v[182:183] op_sel:[1,1,0] op_sel_hi:[1,0,1] neg_lo:[0,1,0]\n"
    "v_cvt_pk_bf16_f32 v55, v206, v207\n"
    "v_pk_fma_f32 v[198:199], v[196:197], v[166:167], v[188:189] op_sel:[1,1,0] op_sel_hi:[1,0,1] neg_lo:[0,1,0]\n"
    "v_pk_mul_f32 v[186:187], v[202:203], v[166:167] op_sel:[0,0] op_sel_hi:[0,1]\n"
    "s_lshl_b32 s45, s45, 15\n"
    "ds_write2st64_b32 v143, v23, v55 offset0:0 offset1:66\n"
    "v_pk_mul_f32 v[184:185], v[190:191], v[168:169] op_sel:[0,0] op_sel_hi:[0,1]\n"
    "v_cvt_pk_bf16_f32 v27, v198, v199\n"
    "v_pk_fma_f32 v[200:201], v[202:203], v[166:167], v[186:187] op_sel:[1,1,0] op_sel_hi:[1,0,1] neg_lo:[0,1,0]\n"
    "buffer_load_dwordx4 v237, s[16:19], s28 offen offset:1024 lds\n"
    "v_pk_mul_f32 v[182:183], v[190:191], v[164:165] op_sel:[0,0] op_sel_hi:[0,1]\n"
    "v_pk_fma_f32 v[198:199], v[190:191], v[168:169], v[184:185] op_sel:[1,1,0] op_sel_hi:[1,0,1] neg_lo:[0,1,0]\n"
    "v_cvt_pk_bf16_f32 v24, v192, v193\n"
    "v_pk_mul_f32 v[188:189], v[200:201], v[166:167] op_sel:[0,0] op_sel_hi:[0,1]\n"
    "v_cvt_pk_bf16_f32 v29, v202, v203\n"
    "v_pk_fma_f32 v[192:193], v[190:191], v[164:165], v[182:183] op_sel:[1,1,0] op_sel_hi:[1,0,1] neg_lo:[0,1,0]\n"
    "v_cvt_pk_bf16_f32 v33, v198, v199\n"
    "v_pk_fma_f32 v[202:203], v[200:201], v[166:167], v[188:189] op_sel:[1,1,0] op_sel_hi:[1,0,1] neg_lo:[0,1,0]\n"
    "v_pk_mul_f32 v[188:189], v[192:193], v[168:169] op_sel:[0,0] op_sel_hi:[0,1]\n"
    "v_cvt_pk_bf16_f32 v31, v202, v203\n"
    "v_cvt_pk_bf16_f32 v34, v192, v193\n"
    "ds_write_b32 v243, v33 offset:13200\n"
    "ds_write_b32 v243, v34 offset:13728\n"
    "v_pk_mul_f32 v[186:187], v[192:193], v[164:165] op_sel:[0,0] op_sel_hi:[0,1]\n"
    "v_pk_fma_f32 v[202:203], v[192:193], v[168:169], v[188:189] op_sel:[1,1,0] op_sel_hi:[1,0,1] neg_lo:[0,1,0]\n"
    "buffer_load_dwordx4 v238, s[16:19], s28 offen offset:2048 lds\n"
    "v_cvt_pk_bf16_f32 v28, v194, v195\n"
    "v_pk_fma_f32 v[194:195], v[192:193], v[164:165], v[186:187] op_sel:[1,1,0] op_sel_hi:[1,0,1] neg_lo:[0,1,0]\n"
    "v_cvt_pk_bf16_f32 v35, v202, v203\n"
    "v_pk_mul_f32 v[184:185], v[194:195], v[168:169] op_sel:[0,0] op_sel_hi:[0,1]\n"
    "v_cvt_pk_bf16_f32 v36, v194, v195\n"
    "ds_write_b32 v243, v35 offset:14256\n"
    "ds_write_b32 v243, v36 offset:14784\n"
    "v_pk_mul_f32 v[182:183], v[194:195], v[164:165] op_sel:[0,0] op_sel_hi:[0,1]\n"
    "v_pk_fma_f32 v[198:199], v[194:195], v[168:169], v[184:185] op_sel:[1,1,0] op_sel_hi:[1,0,1] neg_lo:[0,1,0]\n"
    "v_cvt_pk_bf16_f32 v32, v190, v191\n"
    "v_pk_fma_f32 v[190:191], v[194:195], v[164:165], v[182:183] op_sel:[1,1,0] op_sel_hi:[1,0,1] neg_lo:[0,1,0]\n"
    "v_cvt_pk_bf16_f32 v37, v198, v199\n"
    "v_pk_mul_f32 v[188:189], v[190:191], v[168:169] op_sel:[0,0] op_sel_hi:[0,1]\n"
    "v_cvt_pk_bf16_f32 v38, v190, v191\n"
    "v_pk_fma_f32 v[202:203], v[190:191], v[168:169], v[188:189] op_sel:[1,1,0] op_sel_hi:[1,0,1] neg_lo:[0,1,0]\n"
    "buffer_load_dwordx4 v239, s[16:19], s28 offen offset:3072 lds\n"
    "s_add_u32 m0, s63, 81920\n"
    "ds_write_b32 v243, v37 offset:15312\n"
    "ds_write_b32 v243, v38 offset:15840\n"
    "v_pk_mul_f32 v[186:187], v[190:191], v[164:165] op_sel:[0,0] op_sel_hi:[0,1]\n"
    "v_cvt_pk_bf16_f32 v39, v202, v203\n"
    "v_pk_fma_f32 v[192:193], v[190:191], v[164:165], v[186:187] op_sel:[1,1,0] op_sel_hi:[1,0,1] neg_lo:[0,1,0]\n"
    "s_add_u32 s28, s86, 0x400000\n"
    "ds_write_b32 v243, v39 offset:16368\n"
    "v_cvt_pk_bf16_f32 v74, v192, v193\n"
    "v_pk_mul_f32 v[182:183], v[192:193], v[164:165] op_sel:[0,0] op_sel_hi:[0,1]\n"
    "ds_write2st64_b32 v243, v24, v74 offset0:33 offset1:99\n"
    "buffer_load_dwordx4 v236, s[16:19], s76 offen lds\n"
    "v_pk_fma_f32 v[194:195], v[192:193], v[164:165], v[182:183] op_sel:[1,1,0] op_sel_hi:[1,0,1] neg_lo:[0,1,0]\n"
    "v_cvt_pk_bf16_f32 v26, v196, v197\n"
    "v_cvt_pk_bf16_f32 v75, v194, v195\n"
    "v_pk_mul_f32 v[184:185], v[194:195], v[164:165] op_sel:[0,0] op_sel_hi:[0,1]\n"
    "ds_write2st64_b32 v129, v25, v75 offset0:33 offset1:99\n"
    "v_pk_fma_f32 v[190:191], v[194:195], v[164:165], v[184:185] op_sel:[1,1,0] op_sel_hi:[1,0,1] neg_lo:[0,1,0]\n"
    "v_cvt_pk_bf16_f32 v30, v200, v201\n"
    "v_cvt_pk_bf16_f32 v76, v190, v191\n"
    "v_pk_mul_f32 v[186:187], v[190:191], v[164:165] op_sel:[0,0] op_sel_hi:[0,1]\n"
    "ds_write2st64_b32 v130, v26, v76 offset0:33 offset1:99\n"
    "v_pk_fma_f32 v[192:193], v[190:191], v[164:165], v[186:187] op_sel:[1,1,0] op_sel_hi:[1,0,1] neg_lo:[0,1,0]\n"
    "v_min_u32 v16, 1, v0\n"
    "buffer_load_dwordx4 v237, s[16:19], s76 offen offset:1024 lds\n"
    "v_cvt_pk_bf16_f32 v77, v192, v193\n"
    "v_pk_mul_f32 v[188:189], v[192:193], v[164:165] op_sel:[0,0] op_sel_hi:[0,1]\n"
    "ds_write2st64_b32 v131, v27, v77 offset0:33 offset1:99\n"
    "v_pk_fma_f32 v[194:195], v[192:193], v[164:165], v[188:189] op_sel:[1,1,0] op_sel_hi:[1,0,1] neg_lo:[0,1,0]\n"
    "v_lshlrev_b32 v16, 30, v16\n"
    "v_cvt_pk_bf16_f32 v78, v194, v195\n"
    "v_pk_mul_f32 v[182:183], v[194:195], v[164:165] op_sel:[0,0] op_sel_hi:[0,1]\n"
    "ds_write2st64_b32 v132, v28, v78 offset0:33 offset1:99\n"
    "v_pk_fma_f32 v[190:191], v[194:195], v[164:165], v[182:183] op_sel:[1,1,0] op_sel_hi:[1,0,1] neg_lo:[0,1,0]\n"
    "s_nop 0\n"
    "v_cvt_pk_bf16_f32 v79, v190, v191\n"
    "v_pk_mul_f32 v[184:185], v[190:191], v[164:165] op_sel:[0,0] op_sel_hi:[0,1]\n"
    "buffer_load_dwordx4 v238, s[16:19], s76 offen offset:2048 lds\n"
    "ds_write2st64_b32 v133, v29, v79 offset0:33 offset1:99\n"
    "v_pk_fma_f32 v[192:193], v[190:191], v[164:165], v[184:185] op_sel:[1,1,0] op_sel_hi:[1,0,1] neg_lo:[0,1,0]\n"
    "s_nop 0\n"
    "v_cvt_pk_bf16_f32 v80, v192, v193\n"
    "v_pk_mul_f32 v[186:187], v[192:193], v[164:165] op_sel:[0,0] op_sel_hi:[0,1]\n"
    "ds_write2st64_b32 v134, v30, v80 offset0:33 offset1:99\n"
    "v_pk_fma_f32 v[194:195], v[192:193], v[164:165], v[186:187] op_sel:[1,1,0] op_sel_hi:[1,0,1] neg_lo:[0,1,0]\n"
    "s_nop 0\n"
    "v_cvt_pk_bf16_f32 v81, v194, v195\n"
    "v_pk_mul_f32 v[188:189], v[194:195], v[164:165] op_sel:[0,0] op_sel_hi:[0,1]\n"
    "s_nop 0\n"
    "v_pk_fma_f32 v[190:191], v[194:195], v[164:165], v[188:189] op_sel:[1,1,0] op_sel_hi:[1,0,1] neg_lo:[0,1,0]\n"
    "buffer_load_dwordx4 v239, s[16:19], s76 offen offset:3072 lds\n"
    "ds_write2st64_b32 v135, v31, v81 offset0:33 offset1:99\n"
    "v_cvt_pk_bf16_f32 v82, v190, v191\n"
    "s_add_u32 m0, s63, 98304\n"
    "ds_write2st64_b32 v136, v32, v82 offset0:33 offset1:99\n"
    "s_add_u32 s76, s45, 0x2000\n"
    "buffer_load_dwordx4 v[124:127], v0, s[96:99], s77 offen\n"
    "s_add_u32 s77, s84, 0x401000\n"
    "s_waitcnt lgkmcnt(0)\n"
    "s_barrier\n"
    "ds_read_b128 v[160:163], v252 offset:16896\n"
    "buffer_store_dwordx4 v[228:231], v246, s[4:7], s88 offen sc1\n"
    "ds_read_b128 v[164:167], v253\n"
    "buffer_store_dwordx4 v[228:231], v4, s[4:7], s83 offen sc1\n"
    "ds_read_b128 v[168:171], v252 offset:8448\n"
    "ds_read_b128 v[172:175], v252 offset:16912\n"
    "ds_read_b128 v[176:179], v253 offset:16\n"
    "ds_read_b128 v[180:183], v252 offset:8464\n"
    "s_xor_b32 s88, s93, 1\n"
    "s_lshl_b32 s88, s88, 15\n"
    "ds_read_b128 v[184:187], v252 offset:16928\n"
    "ds_read_b128 v[188:191], v253 offset:32\n"
    "ds_read_b128 v[192:195], v252 offset:8480\n"
    "s_waitcnt lgkmcnt(7)\n"
    "v_mfma_f32_16x16x32_bf16 v[8:11], v[164:167], v[160:163], 0\n"
    "ds_read_b128 v[196:199], v252 offset:16944\n"
    "s_waitcnt lgkmcnt(7)\n"
    "v_mfma_f32_16x16x32_bf16 v[12:15], v[168:171], v[160:163], 0\n"
    "ds_read_b128 v[200:203], v253 offset:48\n"
    "ds_read_b128 v[204:207], v252 offset:8496\n"
    "s_waitcnt lgkmcnt(7)\n"
    "v_mfma_f32_16x16x32_bf16 v[8:11], v[176:179], v[172:175], v[8:11]\n"
    "ds_read_b128 v[208:211], v252 offset:16960\n"
    "s_waitcnt lgkmcnt(7)\n"
    "v_mfma_f32_16x16x32_bf16 v[12:15], v[180:183], v[172:175], v[12:15]\n"
    "ds_read_b128 v[212:215], v253 offset:64\n"
    "ds_read_b128 v[216:219], v252 offset:8512\n"
    "s_waitcnt lgkmcnt(7)\n"
    "v_mfma_f32_16x16x32_bf16 v[8:11], v[188:191], v[184:187], v[8:11]\n"
    "ds_read_b128 v[160:163], v252 offset:16976\n"
    "s_waitcnt lgkmcnt(7)\n"
    "v_mfma_f32_16x16x32_bf16 v[12:15], v[192:195], v[184:187], v[12:15]\n"
    "ds_read_b128 v[164:167], v253 offset:80\n"
    "ds_read_b128 v[168:171], v252 offset:8528\n"
    "ds_read_b128 v[172:175], v252 offset:16992\n"
    "s_waitcnt lgkmcnt(8)\n"
    "v_mfma_f32_16x16x32_bf16 v[8:11], v[200:203], v[196:199], v[8:11]\n"
    "ds_read_b128 v[176:179], v253 offset:96\n"
    "s_waitcnt lgkmcnt(8)\n"
    "v_mfma_f32_16x16x32_bf16 v[12:15], v[204:207], v[196:199], v[12:15]\n"
    "ds_read_b128 v[180:183], v252 offset:8544\n"
    "s_waitcnt lgkmcnt(7)\n"
    "v_mfma_f32_16x16x32_bf16 v[8:11], v[212:215], v[208:211], v[8:11]\n"
    "buffer_store_dwordx4 v[228:231], v4, s[4:7], s89 offen sc1\n"
    "ds_read_b128 v[184:187], v252 offset:17008\n"
    "ds_read_b128 v[188:191], v253 offset:112\n"
    "ds_read_b128 v[192:195], v252 offset:8560\n"
    "s_waitcnt lgkmcnt(8)\n"
    "v_mfma_f32_16x16x32_bf16 v[12:15], v[216:219], v[208:211], v[12:15]\n"
    "s_waitcnt vmcnt(16)\n"
    "v_readfirstlane_b32 s56, v72\n"
    "s_waitcnt lgkmcnt(7)\n"
    "v_mfma_f32_16x16x32_bf16 v[8:11], v[164:167], v[160:163], v[8:11]\n"
    "s_add_u32 s56, s56, 1\n"
    "v_mov_b32 v17, s56\n"
    "s_waitcnt lgkmcnt(6)\n"
    "v_mfma_f32_16x16x32_bf16 v[12:15], v[168:171], v[160:163], v[12:15]\n"
    "s_add_u32 s89, s88, 0x2000\n"
    "s_waitcnt lgkmcnt(4)\n"
    "v_mfma_f32_16x16x32_bf16 v[8:11], v[176:179], v[172:175], v[8:11]\n"
    "s_waitcnt lgkmcnt(3)\n"
    "v_mfma_f32_16x16x32_bf16 v[12:15], v[180:183], v[172:175], v[12:15]\n"
    "s_waitcnt lgkmcnt(1)\n"
    "v_mfma_f32_16x16x32_bf16 v[8:11], v[188:191], v[184:187], v[8:11]\n"
    "s_nop 1\n"
    "s_waitcnt vmcnt(12) lgkmcnt(0)\n"
    "s_barrier\n"
    "ds_read_b128 a[196:199], v3 offset:256\n"
    "ds_read_b128 a[212:215], v3 offset:16640\n"
    "ds_read_b128 a[228:231], v3 offset:33024\n"
    "v_mfma_f32_16x16x32_bf16 v[12:15], v[192:195], v[184:187], v[12:15]\n"
    "ds_read_b128 a[200:203], v3 offset:512\n"
    "s_nop 0\n"
    "ds_read_b128 a[216:219], v3 offset:16896\n"
    "v_cvt_pk_bf16_f32 v8, v8, v9\n"
    "s_nop 0\n"
    "ds_read_b128 a[232:235], v3 offset:33280\n"
    "v_cvt_pk_bf16_f32 v9, v10, v11\n"
    "ds_read_b128 a[204:207], v3 offset:768\n"
    "v_cvt_pk_bf16_f32 v12, v12, v13\n"
    "ds_read_b128 a[220:223], v3 offset:17152\n"
    "v_cvt_pk_bf16_f32 v13, v14, v15\n"
    "s_waitcnt lgkmcnt(7)\n"
    "v_mfma_f32_16x16x32_bf16 v[128:131], a[196:199], a[196:199], 0\n"
    "ds_read_b128 a[236:239], v3 offset:33536\n"
    "buffer_store_dwordx4 v[228:231], v244, s[4:7], s90 offen sc1\n"
    "v_mfma_f32_16x16x32_bf16 v[140:143], v[120:123], a[196:199], 0\n"
    "ds_read_b128 a[192:195], v3\n"
    "s_waitcnt lgkmcnt(7)\n"
    "v_mfma_f32_16x16x32_bf16 v[132:135], a[212:215], a[212:215], 0\n"
    "ds_read_b128 a[208:211], v3 offset:16384\n"
    "s_add_u32 s90, s87, 0x800000\n"
    "v_mfma_f32_16x16x32_bf16 v[144:147], v[120:123], a[212:215], 0\n"
    "ds_read_b128 a[224:227], v3 offset:32768\n"
    "v_mfma_f32_16x16x32_bf16 v[136:139], a[228:231], a[228:231], 0\n"
    "ds_read_b64_tr_b16 a[4:5], v152\n"
    "v_mfma_f32_16x16x32_bf16 v[148:151], v[120:123], a[228:231], 0\n"
    "ds_read_b64_tr_b16 a[6:7], v152 offset:512\n"
    "s_waitcnt lgkmcnt(7)\n"
    "v_mfma_f32_16x16x32_bf16 v[128:131], a[200:203], a[200:203], v[128:131]\n"
    "ds_read_b64_tr_b16 a[16:17], v153\n"
    "v_mfma_f32_16x16x32_bf16 v[140:143], v[120:123], a[200:203], v[140:143]\n"
    "ds_read_b64_tr_b16 a[18:19], v153 offset:512\n"
    "v_mfma_f32_16x16x32_bf16 v[132:135], a[216:219], a[216:219], v[132:135]\n"
    "ds_read_b64_tr_b16 a[28:29], v154\n"
    "v_mfma_f32_16x16x32_bf16 v[144:147], v[120:123], a[216:219], v[144:147]\n"
    "ds_read_b64_tr_b16 a[30:31], v154 offset:512\n"
    "v_mfma_f32_16x16x32_bf16 v[136:139], a[232:235], a[232:235], v[136:139]\n"
    "ds_read_b64_tr_b16 a[40:41], v155\n"
    "v_mfma_f32_16x16x32_bf16 v[148:151], v[120:123], a[232:235], v[148:151]\n"
    "ds_read_b64_tr_b16 a[42:43], v155 offset:512\n"
    "v_mfma_f32_16x16x32_bf16 v[128:131], a[204:207], a[204:207], v[128:131]\n"
    "ds_read_b128 a[196:199], v89\n"
    "v_mfma_f32_16x16x32_bf16 v[140:143], v[120:123], a[204:207], v[140:143]\n"
    "ds_read_b128 a[212:215], v89 offset:16384\n"
    "s_waitcnt lgkmcnt(7)\n"
    "v_mfma_f32_16x16x32_bf16 v[132:135], a[220:223], a[220:223], v[132:135]\n"
    "ds_read_b128 a[228:231], v89 offset:32768\n"
    "v_mfma_f32_16x16x32_bf16 v[144:147], v[120:123], a[220:223], v[144:147]\n"
    "ds_read_b128 a[200:203], v90\n"
    "v_mfma_f32_16x16x32_bf16 v[136:139], a[236:239], a[236:239], v[136:139]\n"
    "ds_read_b128 a[216:219], v90 offset:16384\n"
    "v_mfma_f32_16x16x32_bf16 v[148:151], v[120:123], a[236:239], v[148:151]\n"
    "ds_read_b128 a[232:235], v90 offset:32768\n"
    "v_mfma_f32_16x16x32_bf16 v[128:131], a[192:195], a[192:195], v[128:131]\n"
    "ds_read_b128 a[204:207], v91\n"
    "buffer_load_dwordx4 v236, s[16:19], s92 offen lds\n"
    "v_mfma_f32_16x16x32_bf16 v[140:143], v[120:123], a[192:195], v[140:143]\n"
    "ds_read_b128 a[220:223], v91 offset:16384\n"
    "s_nop 0\n"
    "v_mfma_f32_16x16x32_bf16 v[132:135], a[208:211], a[208:211], v[132:135]\n"
    "s_nop 1\n"
    "v_cndmask_b32 v128, v128, v129, s[64:65]\n"
    "v_mfma_f32_16x16x32_bf16 v[144:147], v[120:123], a[208:211], v[144:147]\n"
    "s_nop 0\n"
    "v_cndmask_b32 v130, v130, v131, s[64:65]\n"
    "v_cndmask_b32 v141, v128, v130, s[70:71]\n"
    "v_mfma_f32_16x16x32_bf16 v[136:139], a[224:227], a[224:227], v[136:139]\n"
    "s_waitcnt vmcnt(5)\n"
    "ds_write_b64 v124, v[140:141]\n"
    "v_mfma_f32_16x16x32_bf16 v[148:151], v[120:123], a[224:227], v[148:151]\n"
    "s_nop 0\n"
    "buffer_load_dwordx4 v237, s[16:19], s92 offen offset:1024 lds\n"
    "v_cndmask_b32 v132, v132, v133, s[64:65]\n"
    "v_cndmask_b32 v134, v134, v135, s[64:65]\n"
    "v_cndmask_b32 v145, v132, v134, s[70:71]\n"
    "v_mfma_f32_16x16x32_bf16 v[96:99], a[4:7], v[120:123], 0\n"
    "ds_write_b64 v124, v[144:145] offset:256\n"
    "v_cndmask_b32 v136, v136, v137, s[64:65]\n"
    "v_cndmask_b32 v138, v138, v139, s[64:65]\n"
    "v_cndmask_b32 v149, v136, v138, s[70:71]\n"
    "v_mfma_f32_16x16x32_bf16 v[56:59], a[4:7], a[192:195], 0\n"
    "ds_write_b64 v124, v[148:149] offset:512\n"
    "v_mfma_f32_16x16x32_bf16 v[68:71], a[4:7], a[208:211], 0\n"
    "ds_read_b128 a[236:239], v91 offset:32768\n"
    "buffer_load_dwordx4 v238, s[16:19], s92 offen offset:2048 lds\n"
    "v_mfma_f32_16x16x32_bf16 v[80:83], a[4:7], a[224:227], 0\n"
    "s_waitcnt lgkmcnt(3)\n"
    "v_mfma_f32_16x16x32_bf16 v[96:99], a[16:19], v[120:123], v[96:99]\n"
    "v_mfma_f32_16x16x32_bf16 v[56:59], a[16:19], a[196:199], v[56:59]\n"
    "s_waitcnt lgkmcnt(1)\n"
    "s_barrier\n"
    "ds_read_b64_tr_b16 a[0:1], v152 offset:16384\n"
    "v_mfma_f32_16x16x32_bf16 v[68:71], a[16:19], a[212:215], v[68:71]\n"
    "buffer_load_dwordx4 v239, s[16:19], s92 offen offset:3072 lds\n"
    "s_add_u32 m0, s63, 114688\n"
    "ds_read_b64_tr_b16 a[2:3], v152 offset:16896\n"
    "ds_read_b64_tr_b16 a[12:13], v153 offset:16384\n"
    "ds_read_b64_tr_b16 a[14:15], v153 offset:16896\n"
    "ds_read_b64_tr_b16 a[24:25], v154 offset:16384\n"
    "ds_read_b64_tr_b16 a[26:27], v154 offset:16896\n"
    "ds_read_b64_tr_b16 a[36:37], v155 offset:16384\n"
    "ds_read_b64_tr_b16 a[38:39], v155 offset:16896\n"
    "ds_read_b64_tr_b16 a[8:9], v152 offset:32768\n"
    "ds_read_b64_tr_b16 a[10:11], v152 offset:33280\n"
    "v_mfma_f32_16x16x32_bf16 v[80:83], a[16:19], a[228:231], v[80:83]\n"
    "buffer_load_dwordx4 v236, s[16:19], s29 offen lds\n"
    "ds_read_b64_tr_b16 a[20:21], v153 offset:32768\n"
    "ds_read_b64_tr_b16 a[22:23], v153 offset:33280\n"
    "ds_read_b64_tr_b16 a[32:33], v154 offset:32768\n"
    "ds_read_b64_tr_b16 a[34:35], v154 offset:33280\n"
    "ds_read_b64_tr_b16 a[44:45], v155 offset:32768\n"
    "ds_read_b64_tr_b16 a[46:47], v155 offset:33280\n"
    "v_mfma_f32_16x16x32_bf16 v[96:99], a[28:31], v[120:123], v[96:99]\n"
    "ds_read_b128 v[140:143], v104\n"
    "s_add_u32 s92, s91, 0x2000\n"
    "v_mfma_f32_16x16x32_bf16 v[56:59], a[28:31], a[200:203], v[56:59]\n"
    "ds_read_b128 v[144:147], v104 offset:1024\n"
    "buffer_load_dwordx4 v237, s[16:19], s29 offen offset:1024 lds\n"
    "v_mfma_f32_16x16x32_bf16 v[68:71], a[28:31], a[216:219], v[68:71]\n"
    "v_mfma_f32_16x16x32_bf16 v[80:83], a[28:31], a[232:235], v[80:83]\n"
    "v_mfma_f32_16x16x32_bf16 v[96:99], a[40:43], v[120:123], v[96:99]\n"
    "v_mfma_f32_16x16x32_bf16 v[56:59], a[40:43], a[204:207], v[56:59]\n"
    "v_mfma_f32_16x16x32_bf16 v[68:71], a[40:43], a[220:223], v[68:71]\n"
    "s_waitcnt lgkmcnt(1)\n"
    "v_add_f32 v140, v140, v142\n"
    "v_add_f32 v141, v141, v143\n"
    "buffer_load_dwordx4 v238, s[16:19], s29 offen offset:2048 lds\n"
    "v_mfma_f32_16x16x32_bf16 v[80:83], a[40:43], a[236:239], v[80:83]\n"
    "s_waitcnt lgkmcnt(0)\n"
    "v_add_f32 v144, v144, v146\n"
    "v_add_f32 v140, v140, v144\n"
    "v_mfma_f32_16x16x32_bf16 v[92:95], a[0:3], v[120:123], 0\n"
    "v_mul_f32 v140, 0xbb000000, v140\n"
    "v_add_f32 v145, v145, v147\n"
    "v_mfma_f32_16x16x32_bf16 v[64:67], a[0:3], a[208:211], 0\n"
    "ds_write_b32 v125, v140 offset:256\n"
    "v_mfma_f32_16x16x32_bf16 v[76:79], a[0:3], a[224:227], 0\n"
    "ds_read2_b32 v[102:103], v126 offset0:64 offset1:80\n"
    "v_add_f32 v141, v141, v145\n"
    "v_mfma_f32_16x16x32_bf16 v[92:95], a[12:15], v[120:123], v[92:95]\n"
    "v_mul_f32 v141, 0x3b000000, v141\n"
    "v_fma_f32 v141, -v140, v140, v141\n"
    "buffer_load_dwordx4 v239, s[16:19], s29 offen offset:3072 lds\n"
    "s_add_u32 m0, s63, 131072\n"
    "v_mfma_f32_16x16x32_bf16 v[64:67], a[12:15], a[212:215], v[64:67]\n"
    "ds_read_b32 v101, v126 offset:384\n"
    "v_max_f32 v141, 0, v141\n"
    "v_mfma_f32_16x16x32_bf16 v[76:79], a[12:15], a[228:231], v[76:79]\n"
    "v_add_f32 v141, 0x3727c5ac, v141\n"
    "s_mov_b32 s29, 0xa00000\n"
    "v_mfma_f32_16x16x32_bf16 v[92:95], a[24:27], v[120:123], v[92:95]\n"
    "v_rsq_f32 v141, v141\n"
    "v_mfma_f32_16x16x32_bf16 v[64:67], a[24:27], a[216:219], v[64:67]\n"
    "ds_write_b32 v125, v141\n"
    "v_mfma_f32_16x16x32_bf16 v[76:79], a[24:27], a[232:235], v[76:79]\n"
    "ds_read2_b32 v[104:105], v126 offset0:16 offset1:32\n"
    "buffer_load_dwordx4 v236, s[16:19], s38 offen lds\n"
    "v_mfma_f32_16x16x32_bf16 v[92:95], a[36:39], v[120:123], v[92:95]\n"
    "ds_read2_b32 v[106:107], v6 offset0:0 offset1:16\n"
    "v_mfma_f32_16x16x32_bf16 v[64:67], a[36:39], a[220:223], v[64:67]\n"
    "ds_read2_b32 v[108:109], v7 offset0:2 offset1:18\n"
    "v_mfma_f32_16x16x32_bf16 v[76:79], a[36:39], a[236:239], v[76:79]\n"
    "ds_read_b128 v[128:131], v127 offset:256\n"
    "v_mfma_f32_16x16x32_bf16 v[116:119], v[120:123], a[8:11], 0\n"
    "ds_read_b128 v[132:135], v127 offset:320\n"
    "v_mfma_f32_16x16x32_bf16 v[60:63], a[192:195], a[8:11], 0\n"
    "ds_read_b128 a[192:195], v3 offset:256\n"
    "buffer_load_dwordx4 v237, s[16:19], s38 offen offset:1024 lds\n"
    "v_mfma_f32_16x16x32_bf16 v[72:75], a[208:211], a[8:11], 0\n"
    "ds_read_b128 a[208:211], v3 offset:16640\n"
    "s_waitcnt lgkmcnt(6)\n"
    "v_mfma_f32_16x16x32_bf16 v[84:87], a[224:227], a[8:11], 0\n"
    "ds_read_b128 a[224:227], v3 offset:33024\n"
    "v_mul_f32 v104, 0x3eb8aa3b, v104\n"
    "v_mfma_f32_16x16x32_bf16 v[116:119], v[120:123], a[20:23], v[116:119]\n"
    "s_waitcnt lgkmcnt(6)\n"
    "v_mul_f32 v249, v104, v106\n"
    "v_mul_f32 v105, 0x3eb8aa3b, v105\n"
    "v_mfma_f32_16x16x32_bf16 v[60:63], a[196:199], a[20:23], v[60:63]\n"
    "ds_read_b128 a[196:199], v89 offset:256\n"
    "v_mul_f32 v100, v105, v107\n"
    "v_mfma_f32_16x16x32_bf16 v[72:75], a[212:215], a[20:23], v[72:75]\n"
    "ds_read_b128 a[212:215], v89 offset:16640\n"
    "s_waitcnt lgkmcnt(5)\n"
    "buffer_load_dwordx4 v238, s[16:19], s38 offen offset:2048 lds\n"
    "v_mfma_f32_16x16x32_bf16 v[84:87], a[228:231], a[20:23], v[84:87]\n"
    "ds_read_b128 a[228:231], v89 offset:33024\n"
    "v_mul_f32 v250, v104, v108\n"
    "v_mfma_f32_16x16x32_bf16 v[116:119], v[120:123], a[32:35], v[116:119]\n"
    "ds_read_b128 v[136:139], v127 offset:384\n"
    "v_add_u32 v104, 147456, v1\n"
    "v_mfma_f32_16x16x32_bf16 v[60:63], a[200:203], a[32:35], v[60:63]\n"
    "ds_read_b128 a[200:203], v90 offset:256\n"
    "v_mfma_f32_16x16x32_bf16 v[72:75], a[216:219], a[32:35], v[72:75]\n"
    "ds_read_b128 a[216:219], v90 offset:16640\n"
    "v_mfma_f32_16x16x32_bf16 v[84:87], a[232:235], a[32:35], v[84:87]\n"
    "ds_read_b128 a[232:235], v90 offset:33024\n"
    "buffer_load_dwordx4 v239, s[16:19], s38 offen offset:3072 lds\n"
    "s_add_u32 m0, s63, 49152\n"
    "v_mfma_f32_16x16x32_bf16 v[116:119], v[120:123], a[44:47], v[116:119]\n"
    "s_xor_b32 s38, s93, 3\n"
    "s_lshl_b32 s38, s38, 15\n"
    "v_mfma_f32_16x16x32_bf16 v[60:63], a[204:207], a[44:47], v[60:63]\n"
    "ds_read_b128 a[204:207], v91 offset:256\n"
    "v_mfma_f32_16x16x32_bf16 v[72:75], a[220:223], a[44:47], v[72:75]\n"
    "ds_read_b128 a[220:223], v91 offset:16640\n"
    "v_mfma_f32_16x16x32_bf16 v[84:87], a[236:239], a[44:47], v[84:87]\n"
    "ds_read_b128 a[236:239], v91 offset:33024\n"
    "ds_write_b64 v255, v[8:9]\n"
    "buffer_load_dwordx4 v[112:115], v0, s[96:99], s39 offen\n"
    "ds_write_b64 v254, v[12:13]\n"
    "s_add_u32 s39, s38, 0x2000\n"
    "buffer_load_dwordx4 v236, s[16:19], s40 offen lds\n"
    "buffer_load_dwordx4 v237, s[16:19], s40 offen offset:1024 lds\n"
    "buffer_load_dwordx4 v238, s[16:19], s40 offen offset:2048 lds\n"
    "buffer_load_dwordx4 v239, s[16:19], s40 offen offset:3072 lds\n"
    "s_add_u32 m0, s63, 65536\n"
    "s_add_u32 s40, s83, 0x100000\n"
    "s_waitcnt vmcnt(9)\n"
    "s_barrier\n"
    "buffer_load_dwordx4 v236, s[16:19], s31 offen lds\n"
    "ds_read_b64_tr_b16 a[52:53], v156\n"
    "ds_read_b64_tr_b16 a[54:55], v156 offset:512\n"
    "ds_read_b64_tr_b16 a[64:65], v157\n"
    "ds_read_b64_tr_b16 a[66:67], v157 offset:512\n"
    "buffer_load_dwordx4 v237, s[16:19], s31 offen offset:1024 lds\n"
    "ds_read_b64_tr_b16 a[76:77], v158\n"
    "ds_read_b64_tr_b16 a[78:79], v158 offset:512\n"
    "ds_read_b64_tr_b16 a[88:89], v159\n"
    "ds_read_b64_tr_b16 a[90:91], v159 offset:512\n"
    "ds_read_b64_tr_b16 a[48:49], v156 offset:16384\n"
    "ds_read_b64_tr_b16 a[50:51], v156 offset:16896\n"
    "ds_read_b64_tr_b16 a[60:61], v157 offset:16384\n"
    "ds_read_b64_tr_b16 a[62:63], v157 offset:16896\n"
    "ds_read_b64_tr_b16 a[72:73], v158 offset:16384\n"
    "ds_read_b64_tr_b16 a[74:75], v158 offset:16896\n"
    "buffer_load_dwordx4 v238, s[16:19], s31 offen offset:2048 lds\n"
    "ds_read_b64_tr_b16 a[84:85], v159 offset:16384\n"
    "ds_read_b64_tr_b16 a[86:87], v159 offset:16896\n"
    "s_waitcnt lgkmcnt(12)\n"
    "v_mfma_f32_16x16x32_bf16 v[96:99], a[52:55], v[120:123], v[96:99]\n"
    "v_mfma_f32_16x16x32_bf16 v[56:59], a[52:55], a[192:195], v[56:59]\n"
    "v_mfma_f32_16x16x32_bf16 v[68:71], a[52:55], a[208:211], v[68:71]\n"
    "v_mfma_f32_16x16x32_bf16 v[80:83], a[52:55], a[224:227], v[80:83]\n"
    "buffer_load_dwordx4 v239, s[16:19], s31 offen offset:3072 lds\n"
    "s_add_u32 m0, s63, 81920\n"
    "v_mfma_f32_16x16x32_bf16 v[96:99], a[64:67], v[120:123], v[96:99]\n"
    "s_mov_b32 s31, 0xa04000\n"
    "v_mfma_f32_16x16x32_bf16 v[56:59], a[64:67], a[196:199], v[56:59]\n"
    "v_mfma_f32_16x16x32_bf16 v[68:71], a[64:67], a[212:215], v[68:71]\n"
    "v_mfma_f32_16x16x32_bf16 v[80:83], a[64:67], a[228:231], v[80:83]\n"
    "s_waitcnt lgkmcnt(0)\n"
    "v_mfma_f32_16x16x32_bf16 v[96:99], a[76:79], v[120:123], v[96:99]\n"
    "buffer_load_dwordx4 v236, s[16:19], s41 offen lds\n"
    "v_mfma_f32_16x16x32_bf16 v[56:59], a[76:79], a[200:203], v[56:59]\n"
    "v_mfma_f32_16x16x32_bf16 v[68:71], a[76:79], a[216:219], v[68:71]\n"
    "v_mfma_f32_16x16x32_bf16 v[80:83], a[76:79], a[232:235], v[80:83]\n"
    "v_mfma_f32_16x16x32_bf16 v[96:99], a[88:91], v[120:123], v[96:99]\n"
    "v_mfma_f32_16x16x32_bf16 v[56:59], a[88:91], a[204:207], v[56:59]\n"
    "buffer_load_dwordx4 v237, s[16:19], s41 offen offset:1024 lds\n"
    "v_mfma_f32_16x16x32_bf16 v[68:71], a[88:91], a[220:223], v[68:71]\n"
    "v_mfma_f32_16x16x32_bf16 v[80:83], a[88:91], a[236:239], v[80:83]\n"
    "v_mfma_f32_16x16x32_bf16 v[92:95], a[48:51], v[120:123], v[92:95]\n"
    "v_mfma_f32_16x16x32_bf16 v[64:67], a[48:51], a[208:211], v[64:67]\n"
    "v_mfma_f32_16x16x32_bf16 v[76:79], a[48:51], a[224:227], v[76:79]\n"
    "buffer_load_dwordx4 v238, s[16:19], s41 offen offset:2048 lds\n"
    "v_mfma_f32_16x16x32_bf16 v[92:95], a[60:63], v[120:123], v[92:95]\n"
    "v_mfma_f32_16x16x32_bf16 v[64:67], a[60:63], a[212:215], v[64:67]\n"
    "v_mfma_f32_16x16x32_bf16 v[76:79], a[60:63], a[228:231], v[76:79]\n"
    "v_mfma_f32_16x16x32_bf16 v[92:95], a[72:75], v[120:123], v[92:95]\n"
    "v_mfma_f32_16x16x32_bf16 v[64:67], a[72:75], a[216:219], v[64:67]\n"
    "buffer_load_dwordx4 v239, s[16:19], s41 offen offset:3072 lds\n"
    "s_add_u32 m0, s63, 98304\n"
    "v_mfma_f32_16x16x32_bf16 v[76:79], a[72:75], a[232:235], v[76:79]\n"
    "s_mov_b32 s41, 20480\n"
    "v_mfma_f32_16x16x32_bf16 v[92:95], a[84:87], v[120:123], v[92:95]\n"
    "v_mfma_f32_16x16x32_bf16 v[64:67], a[84:87], a[220:223], v[64:67]\n"
    "v_mfma_f32_16x16x32_bf16 v[76:79], a[84:87], a[236:239], v[76:79]\n"
    "s_waitcnt vmcnt(8)\n"
    "s_barrier\n"
    "buffer_load_dwordx4 v236, s[16:19], s42 offen lds\n"
    "ds_read_b64_tr_b16 a[56:57], v156 offset:32768\n"
    "ds_read_b64_tr_b16 a[58:59], v156 offset:33280\n"
    "ds_read_b64_tr_b16 a[68:69], v157 offset:32768\n"
    "ds_read_b64_tr_b16 a[70:71], v157 offset:33280\n"
    "buffer_load_dwordx4 v237, s[16:19], s42 offen offset:1024 lds\n"
    "ds_read_b64_tr_b16 a[80:81], v158 offset:32768\n"
    "ds_read_b64_tr_b16 a[82:83], v158 offset:33280\n"
    "ds_read_b64_tr_b16 a[92:93], v159 offset:32768\n"
    "ds_read_b64_tr_b16 a[94:95], v159 offset:33280\n"
    "ds_read_b64_tr_b16 a[100:101], v152\n"
    "ds_read_b64_tr_b16 a[102:103], v152 offset:512\n"
    "ds_read_b64_tr_b16 a[112:113], v153\n"
    "ds_read_b64_tr_b16 a[114:115], v153 offset:512\n"
    "ds_read_b64_tr_b16 a[124:125], v154\n"
    "ds_read_b64_tr_b16 a[126:127], v154 offset:512\n"
    "buffer_load_dwordx4 v238, s[16:19], s42 offen offset:2048 lds\n"
    "ds_read_b64_tr_b16 a[136:137], v155\n"
    "ds_read_b64_tr_b16 a[138:139], v155 offset:512\n"
    "s_waitcnt lgkmcnt(12)\n"
    "v_mfma_f32_16x16x32_bf16 v[116:119], v[120:123], a[56:59], v[116:119]\n"
    "v_mfma_f32_16x16x32_bf16 v[60:63], a[192:195], a[56:59], v[60:63]\n"
    "ds_read_b128 a[192:195], v3 offset:512\n"
    "v_mfma_f32_16x16x32_bf16 v[72:75], a[208:211], a[56:59], v[72:75]\n"
    "ds_read_b128 a[208:211], v3 offset:16896\n"
    "v_mfma_f32_16x16x32_bf16 v[84:87], a[224:227], a[56:59], v[84:87]\n"
    "ds_read_b128 a[224:227], v3 offset:33280\n"
    "buffer_load_dwordx4 v239, s[16:19], s42 offen offset:3072 lds\n"
    "s_add_u32 m0, s63, 114688\n"
    "v_mfma_f32_16x16x32_bf16 v[116:119], v[120:123], a[68:71], v[116:119]\n"
    "s_mov_b32 s42, 24576\n"
    "v_mfma_f32_16x16x32_bf16 v[60:63], a[196:199], a[68:71], v[60:63]\n"
    "ds_read_b128 a[196:199], v89 offset:512\n"
    "v_mfma_f32_16x16x32_bf16 v[72:75], a[212:215], a[68:71], v[72:75]\n"
    "ds_read_b128 a[212:215], v89 offset:16896\n"
    "v_mfma_f32_16x16x32_bf16 v[84:87], a[228:231], a[68:71], v[84:87]\n"
    "ds_read_b128 a[228:231], v89 offset:33280\n"
    "s_waitcnt lgkmcnt(5)\n"
    "v_mfma_f32_16x16x32_bf16 v[116:119], v[120:123], a[80:83], v[116:119]\n"
    "buffer_load_dwordx4 v236, s[16:19], s34 offen lds\n"
    "v_mfma_f32_16x16x32_bf16 v[60:63], a[200:203], a[80:83], v[60:63]\n"
    "ds_read_b128 a[200:203], v90 offset:512\n"
    "v_mfma_f32_16x16x32_bf16 v[72:75], a[216:219], a[80:83], v[72:75]\n"
    "ds_read_b128 a[216:219], v90 offset:16896\n"
    "v_mfma_f32_16x16x32_bf16 v[84:87], a[232:235], a[80:83], v[84:87]\n"
    "ds_read_b128 a[232:235], v90 offset:33280\n"
    "v_mfma_f32_16x16x32_bf16 v[116:119], v[120:123], a[92:95], v[116:119]\n"
    "v_mfma_f32_16x16x32_bf16 v[60:63], a[204:207], a[92:95], v[60:63]\n"
    "ds_read_b128 a[204:207], v91 offset:512\n"
    "buffer_load_dwordx4 v237, s[16:19], s34 offen offset:1024 lds\n"
    "v_mfma_f32_16x16x32_bf16 v[72:75], a[220:223], a[92:95], v[72:75]\n"
    "ds_read_b128 a[220:223], v91 offset:16896\n"
    "v_mfma_f32_16x16x32_bf16 v[84:87], a[236:239], a[92:95], v[84:87]\n"
    "ds_read_b128 a[236:239], v91 offset:33280\n"
    "v_mfma_f32_16x16x32_bf16 v[96:99], a[100:103], v[120:123], v[96:99]\n"
    "v_mfma_f32_16x16x32_bf16 v[56:59], a[100:103], a[192:195], v[56:59]\n"
    "s_waitcnt lgkmcnt(3)\n"
    "v_mfma_f32_16x16x32_bf16 v[68:71], a[100:103], a[208:211], v[68:71]\n"
    "buffer_load_dwordx4 v238, s[16:19], s34 offen offset:2048 lds\n"
    "v_mfma_f32_16x16x32_bf16 v[80:83], a[100:103], a[224:227], v[80:83]\n"
    "v_mfma_f32_16x16x32_bf16 v[96:99], a[112:115], v[120:123], v[96:99]\n"
    "v_mfma_f32_16x16x32_bf16 v[56:59], a[112:115], a[196:199], v[56:59]\n"
    "v_mfma_f32_16x16x32_bf16 v[68:71], a[112:115], a[212:215], v[68:71]\n"
    "v_mfma_f32_16x16x32_bf16 v[80:83], a[112:115], a[228:231], v[80:83]\n"
    "buffer_load_dwordx4 v239, s[16:19], s34 offen offset:3072 lds\n"
    "s_add_u32 m0, s63, 131072\n"
    "v_mfma_f32_16x16x32_bf16 v[96:99], a[124:127], v[120:123], v[96:99]\n"
    "s_mov_b32 s34, 0xa06000\n"
    "v_mfma_f32_16x16x32_bf16 v[56:59], a[124:127], a[200:203], v[56:59]\n"
    "v_mfma_f32_16x16x32_bf16 v[68:71], a[124:127], a[216:219], v[68:71]\n"
    "v_mfma_f32_16x16x32_bf16 v[80:83], a[124:127], a[232:235], v[80:83]\n"
    "v_mfma_f32_16x16x32_bf16 v[96:99], a[136:139], v[120:123], v[96:99]\n"
    "s_waitcnt lgkmcnt(0)\n"
    "v_mfma_f32_16x16x32_bf16 v[56:59], a[136:139], a[204:207], v[56:59]\n"
    "buffer_store_dword v17, v16, s[4:7], s43 offen sc1\n"
    "s_add_u32 s43, s84, 0x400000\n"
    "v_mfma_f32_16x16x32_bf16 v[68:71], a[136:139], a[220:223], v[68:71]\n"
    "v_mfma_f32_16x16x32_bf16 v[80:83], a[136:139], a[236:239], v[80:83]\n"
    "s_waitcnt vmcnt(9)\n"
    "s_barrier\n"
    "buffer_load_dwordx4 v236, s[16:19], s44 offen lds\n"
    "ds_read_b64_tr_b16 a[96:97], v152 offset:16384\n"
    "ds_read_b64_tr_b16 a[98:99], v152 offset:16896\n"
    "ds_read_b64_tr_b16 a[108:109], v153 offset:16384\n"
    "ds_read_b64_tr_b16 a[110:111], v153 offset:16896\n"
    "buffer_load_dwordx4 v237, s[16:19], s44 offen offset:1024 lds\n"
    "ds_read_b64_tr_b16 a[120:121], v154 offset:16384\n"
    "ds_read_b64_tr_b16 a[122:123], v154 offset:16896\n"
    "ds_read_b64_tr_b16 a[132:133], v155 offset:16384\n"
    "ds_read_b64_tr_b16 a[134:135], v155 offset:16896\n"
    "ds_read_b64_tr_b16 a[104:105], v152 offset:32768\n"
    "ds_read_b64_tr_b16 a[106:107], v152 offset:33280\n"
    "ds_read_b64_tr_b16 a[116:117], v153 offset:32768\n"
    "ds_read_b64_tr_b16 a[118:119], v153 offset:33280\n"
    "ds_read_b64_tr_b16 a[128:129], v154 offset:32768\n"
    "ds_read_b64_tr_b16 a[130:131], v154 offset:33280\n"
    "buffer_load_dwordx4 v238, s[16:19], s44 offen offset:2048 lds\n"
    "ds_read_b64_tr_b16 a[140:141], v155 offset:32768\n"
    "ds_read_b64_tr_b16 a[142:143], v155 offset:33280\n"
    "s_waitcnt lgkmcnt(12)\n"
    "v_mfma_f32_16x16x32_bf16 v[92:95], a[96:99], v[120:123], v[92:95]\n"
    "v_mfma_f32_16x16x32_bf16 v[64:67], a[96:99], a[208:211], v[64:67]\n"
    "v_mfma_f32_16x16x32_bf16 v[76:79], a[96:99], a[224:227], v[76:79]\n"
    "v_mfma_f32_16x16x32_bf16 v[92:95], a[108:111], v[120:123], v[92:95]\n"
    "buffer_load_dwordx4 v239, s[16:19], s44 offen offset:3072 lds\n"
    "s_add_u32 m0, s63, 49152\n"
    "v_mfma_f32_16x16x32_bf16 v[64:67], a[108:111], a[212:215], v[64:67]\n"
    "s_add_u32 s44, s85, 0xa00000\n"
    "v_mfma_f32_16x16x32_bf16 v[76:79], a[108:111], a[228:231], v[76:79]\n"
    "s_waitcnt lgkmcnt(0)\n"
    "v_mfma_f32_16x16x32_bf16 v[92:95], a[120:123], v[120:123], v[92:95]\n"
    "v_mfma_f32_16x16x32_bf16 v[64:67], a[120:123], a[216:219], v[64:67]\n"
    "v_mfma_f32_16x16x32_bf16 v[76:79], a[120:123], a[232:235], v[76:79]\n"
    "buffer_load_dwordx4 v232, s[8:11], s79 offen lds\n"
    "v_mfma_f32_16x16x32_bf16 v[92:95], a[132:135], v[120:123], v[92:95]\n"
    "v_mfma_f32_16x16x32_bf16 v[64:67], a[132:135], a[220:223], v[64:67]\n"
    "v_mfma_f32_16x16x32_bf16 v[76:79], a[132:135], a[236:239], v[76:79]\n"
    "v_mfma_f32_16x16x32_bf16 v[116:119], v[120:123], a[104:107], v[116:119]\n"
    "buffer_load_dwordx4 v233, s[8:11], s79 offen offset:1024 lds\n"
    "v_mfma_f32_16x16x32_bf16 v[60:63], a[192:195], a[104:107], v[60:63]\n"
    "ds_read_b128 a[192:195], v3 offset:768\n"
    "v_mfma_f32_16x16x32_bf16 v[72:75], a[208:211], a[104:107], v[72:75]\n"
    "ds_read_b128 a[208:211], v3 offset:17152\n"
    "v_mfma_f32_16x16x32_bf16 v[84:87], a[224:227], a[104:107], v[84:87]\n"
    "ds_read_b128 a[224:227], v3 offset:33536\n"
    "v_mfma_f32_16x16x32_bf16 v[116:119], v[120:123], a[116:119], v[116:119]\n"
    "buffer_load_dwordx4 v234, s[8:11], s79 offen offset:2048 lds\n"
    "v_mfma_f32_16x16x32_bf16 v[60:63], a[196:199], a[116:119], v[60:63]\n"
    "ds_read_b128 a[196:199], v89 offset:768\n"
    "v_mfma_f32_16x16x32_bf16 v[72:75], a[212:215], a[116:119], v[72:75]\n"
    "ds_read_b128 a[212:215], v89 offset:17152\n"
    "v_mfma_f32_16x16x32_bf16 v[84:87], a[228:231], a[116:119], v[84:87]\n"
    "ds_read_b128 a[228:231], v89 offset:33536\n"
    "v_mfma_f32_16x16x32_bf16 v[116:119], v[120:123], a[128:131], v[116:119]\n"
    "buffer_load_dwordx4 v235, s[8:11], s79 offen offset:3072 lds\n"
    "s_add_u32 m0, s63, 65536\n"
    "v_mfma_f32_16x16x32_bf16 v[60:63], a[200:203], a[128:131], v[60:63]\n"
    "ds_read_b128 a[200:203], v90 offset:768\n"
    "v_xor_b32 v89, 64, v88\n"
    "v_mfma_f32_16x16x32_bf16 v[72:75], a[216:219], a[128:131], v[72:75]\n"
    "ds_read_b128 a[216:219], v90 offset:17152\n"
    "v_mfma_f32_16x16x32_bf16 v[84:87], a[232:235], a[128:131], v[84:87]\n"
    "ds_read_b128 a[232:235], v90 offset:33536\n"
    "v_xor_b32 v90, 128, v88\n"
    "v_mfma_f32_16x16x32_bf16 v[116:119], v[120:123], a[140:143], v[116:119]\n"
    "v_mfma_f32_16x16x32_bf16 v[60:63], a[204:207], a[140:143], v[60:63]\n"
    "ds_read_b128 a[204:207], v91 offset:768\n"
    "v_mfma_f32_16x16x32_bf16 v[72:75], a[220:223], a[140:143], v[72:75]\n"
    "ds_read_b128 a[220:223], v91 offset:17152\n"
    "v_mfma_f32_16x16x32_bf16 v[84:87], a[236:239], a[140:143], v[84:87]\n"
    "ds_read_b128 a[236:239], v91 offset:33536\n"
    "v_xor_b32 v91, 192, v88\n"
    "s_waitcnt vmcnt(9)\n"
    "s_barrier\n"
    "buffer_load_dwordx4 v232, s[8:11], s80 offen lds\n"
    "ds_read_b64_tr_b16 a[148:149], v156\n"
    "buffer_load_dwordx4 v233, s[8:11], s80 offen offset:1024 lds\n"
    "ds_read_b64_tr_b16 a[150:151], v156 offset:512\n"
    "ds_read_b64_tr_b16 a[160:161], v157\n"
    "ds_read_b64_tr_b16 a[162:163], v157 offset:512\n"
    "ds_read_b64_tr_b16 a[172:173], v158\n"
    "ds_read_b64_tr_b16 a[174:175], v158 offset:512\n"
    "ds_read_b64_tr_b16 a[184:185], v159\n"
    "ds_read_b64_tr_b16 a[186:187], v159 offset:512\n"
    "buffer_load_dwordx4 v234, s[8:11], s80 offen offset:2048 lds\n"
    "ds_read_b64_tr_b16 a[144:145], v156 offset:16384\n"
    "ds_read_b64_tr_b16 a[146:147], v156 offset:16896\n"
    "ds_read_b64_tr_b16 a[156:157], v157 offset:16384\n"
    "ds_read_b64_tr_b16 a[158:159], v157 offset:16896\n"
    "s_waitcnt lgkmcnt(10)\n"
    "v_mfma_f32_16x16x32_bf16 v[96:99], a[148:151], v[120:123], v[96:99]\n"
    "ds_read_b64_tr_b16 a[168:169], v158 offset:16384\n"
    "buffer_load_dwordx4 v235, s[8:11], s80 offen offset:3072 lds\n"
    "s_add_u32 m0, s63, 81920\n"
    "v_mfma_f32_16x16x32_bf16 v[56:59], a[148:151], a[192:195], v[56:59]\n"
    "ds_read_b64_tr_b16 a[170:171], v158 offset:16896\n"
    "s_add_u32 s80, s58, 0x800000\n"
    "v_mfma_f32_16x16x32_bf16 v[68:71], a[148:151], a[208:211], v[68:71]\n"
    "ds_read_b64_tr_b16 a[180:181], v159 offset:16384\n"
    "s_sub_u32 s79, s80, 0x4000\n"
    "v_mfma_f32_16x16x32_bf16 v[80:83], a[148:151], a[224:227], v[80:83]\n"
    "s_waitcnt lgkmcnt(6)\n"
    "v_mfma_f32_16x16x32_bf16 v[96:99], a[160:163], v[120:123], v[96:99]\n"
    "buffer_load_dwordx4 v232, s[8:11], s81 offen lds\n"
    "v_mfma_f32_16x16x32_bf16 v[56:59], a[160:163], a[196:199], v[56:59]\n"
    "ds_read_b64_tr_b16 a[182:183], v159 offset:16896\n"
    "v_mfma_f32_16x16x32_bf16 v[68:71], a[160:163], a[212:215], v[68:71]\n"
    "s_cmp_eq_u32 s51, 0\n"
    "s_cselect_b32 s79, s80, s79\n"
    "v_mfma_f32_16x16x32_bf16 v[80:83], a[160:163], a[228:231], v[80:83]\n"
    "v_mfma_f32_16x16x32_bf16 v[96:99], a[172:175], v[120:123], v[96:99]\n"
    "buffer_load_dwordx4 v233, s[8:11], s81 offen offset:1024 lds\n"
    "v_mfma_f32_16x16x32_bf16 v[56:59], a[172:175], a[200:203], v[56:59]\n"
    "v_mfma_f32_16x16x32_bf16 v[68:71], a[172:175], a[216:219], v[68:71]\n"
    "v_mfma_f32_16x16x32_bf16 v[80:83], a[172:175], a[232:235], v[80:83]\n"
    "v_mfma_f32_16x16x32_bf16 v[96:99], a[184:187], v[120:123], v[96:99]\n"
    "buffer_load_dwordx4 v234, s[8:11], s81 offen offset:2048 lds\n"
    "v_mfma_f32_16x16x32_bf16 v[56:59], a[184:187], a[204:207], v[56:59]\n"
    "s_nop 2\n"
    "v_mfma_f32_16x16x32_bf16 v[68:71], a[184:187], a[220:223], v[68:71]\n"
    "s_nop 2\n"
    "v_mfma_f32_16x16x32_bf16 v[80:83], a[184:187], a[236:239], v[80:83]\n"
    "s_nop 1\n"
    "s_waitcnt lgkmcnt(0)\n"
    "v_mfma_f32_16x16x32_bf16 v[92:95], a[144:147], v[120:123], v[92:95]\n"
    "buffer_load_dwordx4 v235, s[8:11], s81 offen offset:3072 lds\n"
    "v_fmac_f32 v56, v102, v96\n"
    "v_mfma_f32_16x16x32_bf16 v[64:67], a[144:147], a[208:211], v[64:67]\n"
    "v_fmac_f32 v68, v103, v96\n"
    "v_fmac_f32 v57, v102, v97\n"
    "v_mfma_f32_16x16x32_bf16 v[76:79], a[144:147], a[224:227], v[76:79]\n"
    "v_fmac_f32 v69, v103, v97\n"
    "v_fmac_f32 v58, v102, v98\n"
    "v_mfma_f32_16x16x32_bf16 v[92:95], a[156:159], v[120:123], v[92:95]\n"
    "v_mfma_f32_16x16x32_bf16 v[64:67], a[156:159], a[212:215], v[64:67]\n"
    "buffer_store_dwordx4 v[228:231], v244, s[4:7], s77 offen sc1\n"
    "v_mfma_f32_16x16x32_bf16 v[76:79], a[156:159], a[228:231], v[76:79]\n"
    "v_fmac_f32 v70, v103, v98\n"
    "v_fmac_f32 v59, v102, v99\n"
    "v_mfma_f32_16x16x32_bf16 v[92:95], a[168:171], v[120:123], v[92:95]\n"
    "v_fmac_f32 v71, v103, v99\n"
    "v_cvt_pk_bf16_f32 v56, v56, v57\n"
    "v_mfma_f32_16x16x32_bf16 v[64:67], a[168:171], a[216:219], v[64:67]\n"
    "v_cvt_pk_bf16_f32 v68, v68, v69\n"
    "v_cvt_pk_bf16_f32 v57, v58, v59\n"
    "v_mfma_f32_16x16x32_bf16 v[76:79], a[168:171], a[232:235], v[76:79]\n"
    "v_cvt_pk_bf16_f32 v69, v70, v71\n"
    "v_fmac_f32 v80, v101, v96\n"
    "v_mfma_f32_16x16x32_bf16 v[92:95], a[180:183], v[120:123], v[92:95]\n"
    "s_nop 0\n"
    "v_fmac_f32 v81, v101, v97\n"
    "v_fmac_f32 v82, v101, v98\n"
    "v_mfma_f32_16x16x32_bf16 v[64:67], a[180:183], a[220:223], v[64:67]\n"
    "s_nop 0\n"
    "v_fmac_f32 v83, v101, v99\n"
    "v_cvt_pk_bf16_f32 v80, v80, v81\n"
    "s_waitcnt vmcnt(13)\n"
    "s_barrier\n"
    "ds_read_b64_tr_b16 a[152:153], v156 offset:32768\n"
    "ds_read_b64_tr_b16 a[154:155], v156 offset:33280\n"
    "ds_read_b64_tr_b16 a[164:165], v157 offset:32768\n"
    "ds_read_b64_tr_b16 a[166:167], v157 offset:33280\n"
    "v_mfma_f32_16x16x32_bf16 v[76:79], a[180:183], a[236:239], v[76:79]\n"
    "ds_read_b64_tr_b16 a[176:177], v158 offset:32768\n"
    "ds_read_b64_tr_b16 a[178:179], v158 offset:33280\n"
    "ds_read_b64_tr_b16 a[188:189], v159 offset:32768\n"
    "ds_read_b64_tr_b16 a[190:191], v159 offset:33280\n"
    "ds_write_b128 v127, v[92:95] offset:512\n"
    "v_fmac_f32 v64, v103, v92\n"
    "v_fmac_f32 v65, v103, v93\n"
    "v_fmac_f32 v66, v103, v94\n"
    "s_waitcnt lgkmcnt(7)\n"
    "v_mfma_f32_16x16x32_bf16 v[116:119], v[120:123], a[152:155], v[116:119]\n"
    "v_fmac_f32 v67, v103, v95\n"
    "v_cvt_pk_bf16_f32 v64, v64, v65\n"
    "v_mfma_f32_16x16x32_bf16 v[60:63], a[192:195], a[152:155], v[60:63]\n"
    "v_cvt_pk_bf16_f32 v65, v66, v67\n"
    "v_fmac_f32 v76, v101, v92\n"
    "v_mfma_f32_16x16x32_bf16 v[72:75], a[208:211], a[152:155], v[72:75]\n"
    "v_fmac_f32 v77, v101, v93\n"
    "v_fmac_f32 v78, v101, v94\n"
    "buffer_load_dwordx4 v[160:163], v111, s[20:23], s45 offen\n"
    "v_mfma_f32_16x16x16_bf16 v[144:147], v[56:57], v[64:65], 0\n"
    "ds_write_b128 v127, v[96:99] offset:576\n"
    "v_mfma_f32_16x16x16_bf16 v[148:151], v[68:69], v[64:65], 0\n"
    "s_nop 0\n"
    "v_fmac_f32 v79, v101, v95\n"
    "v_cvt_pk_bf16_f32 v76, v76, v77\n"
    "v_mfma_f32_16x16x32_bf16 v[84:87], a[224:227], a[152:155], v[84:87]\n"
    "s_waitcnt lgkmcnt(1)\n"
    "v_cvt_pk_bf16_f32 v81, v82, v83\n"
    "v_cvt_pk_bf16_f32 v77, v78, v79\n"
    "buffer_load_dwordx4 v[164:167], v111, s[20:23], s45 offen offset:1024\n"
    "v_mfma_f32_16x16x32_bf16 v[116:119], v[120:123], a[164:167], v[116:119]\n"
    "v_cndmask_b32 v144, v144, v145, s[64:65]\n"
    "v_cndmask_b32 v148, v148, v149, s[64:65]\n"
    "v_mfma_f32_16x16x16_bf16 v[92:95], v[68:69], v[76:77], 0\n"
    "s_nop 0\n"
    "v_cndmask_b32 v146, v146, v147, s[64:65]\n"
    "v_cndmask_b32 v150, v150, v151, s[64:65]\n"
    "v_mfma_f32_16x16x16_bf16 v[96:99], v[80:81], v[76:77], 0\n"
    "s_nop 0\n"
    "v_cndmask_b32 v144, v144, v148, s[66:67]\n"
    "v_cndmask_b32 v146, v146, v150, s[68:69]\n"
    "v_mfma_f32_16x16x32_bf16 v[60:63], a[196:199], a[164:167], v[60:63]\n"
    "s_nop 0\n"
    "v_mul_f32 v249, v249, v144\n"
    "buffer_load_dwordx4 v[168:171], v111, s[20:23], s45 offen offset:2048\n"
    "v_mfma_f32_16x16x32_bf16 v[72:75], a[212:215], a[164:167], v[72:75]\n"
    "v_mul_f32 v250, v250, v146\n"
    "v_cndmask_b32 v249, v251, v249, s[72:73]\n"
    "v_mfma_f32_16x16x32_bf16 v[84:87], a[228:231], a[164:167], v[84:87]\n"
    "v_cndmask_b32 v250, v251, v250, s[74:75]\n"
    "v_max_f32 v148, v249, v250\n"
    "v_mfma_f32_16x16x32_bf16 v[116:119], v[120:123], a[176:179], v[116:119]\n"
    "v_max_f32 v149, v249, v250\n"
    "v_cndmask_b32 v92, v92, v93, s[64:65]\n"
    "v_mfma_f32_16x16x32_bf16 v[60:63], a[200:203], a[176:179], v[60:63]\n"
    "v_cndmask_b32 v96, v96, v97, s[64:65]\n"
    "buffer_load_dwordx4 v[172:175], v111, s[20:23], s45 offen offset:3072\n"
    "v_mfma_f32_16x16x32_bf16 v[72:75], a[216:219], a[176:179], v[72:75]\n"
    "v_permlane16_swap_b32 v148, v149\n"
    "v_mfma_f32_16x16x32_bf16 v[84:87], a[232:235], a[176:179], v[84:87]\n"
    "v_cndmask_b32 v94, v94, v95, s[64:65]\n"
    "v_cndmask_b32 v98, v98, v99, s[64:65]\n"
    "v_mfma_f32_16x16x32_bf16 v[116:119], v[120:123], a[188:191], v[116:119]\n"
    "s_nop 0\n"
    "v_mul_f32 v101, v105, v109\n"
    "v_cndmask_b32 v92, v92, v96, s[66:67]\n"
    "v_mfma_f32_16x16x32_bf16 v[60:63], a[204:207], a[188:191], v[60:63]\n"
    "s_nop 0\n"
    "v_cndmask_b32 v94, v94, v98, s[68:69]\n"
    "buffer_load_dwordx4 v[176:179], v111, s[20:23], s76 offen\n"
    "v_max_f32 v150, v148, v149\n"
    "v_max_f32 v151, v148, v149\n"
    "v_mul_f32 v100, v100, v92\n"
    "v_mul_f32 v101, v101, v94\n"
    "v_mfma_f32_16x16x32_bf16 v[72:75], a[220:223], a[188:191], v[72:75]\n"
    "s_mov_b32 s77, 57344\n"
    "v_max_f32 v96, v100, v101\n"
    "v_max_f32 v97, v100, v101\n"
    "v_permlane32_swap_b32 v150, v151\n"
    "v_max_f32 v148, v150, v151\n"
    "v_permlane16_swap_b32 v96, v97\n"
    "v_sub_f32 v249, v249, v148\n"
    "v_sub_f32 v250, v250, v148\n"
    "buffer_load_dwordx4 v[180:183], v111, s[20:23], s76 offen offset:1024\n"
    "v_max_f32 v98, v96, v97\n"
    "v_max_f32 v99, v96, v97\n"
    "v_exp_f32 v249, v249\n"
    "v_exp_f32 v250, v250\n"
    "v_mfma_f32_16x16x32_bf16 v[84:87], a[236:239], a[188:191], v[84:87]\n"
    "s_mov_b32 s45, 32768\n"
    "v_fmac_f32 v60, v128, v116\n"
    "v_fmac_f32 v61, v129, v116\n"
    "v_permlane32_swap_b32 v98, v99\n"
    "v_fmac_f32 v62, v130, v116\n"
    "v_fmac_f32 v63, v131, v116\n"
    "v_cvt_pk_bf16_f32 v60, v60, v61\n"
    "buffer_load_dwordx4 v[184:187], v111, s[20:23], s76 offen offset:2048\n"
    "v_mul_f32 v144, v249, v106\n"
    "v_mul_f32 v145, v250, v108\n"
    "v_fmac_f32 v72, v132, v116\n"
    "v_fmac_f32 v73, v133, v116\n"
    "v_fmac_f32 v74, v134, v116\n"
    "v_fmac_f32 v75, v135, v116\n"
    "v_cvt_pk_bf16_f32 v61, v62, v63\n"
    "v_cvt_pk_bf16_f32 v251, v144, v145\n"
    "v_max_f32 v96, v98, v99\n"
    "v_cvt_pk_bf16_f32 v62, v72, v73\n"
    "v_cvt_pk_bf16_f32 v63, v74, v75\n"
    "v_perm_b32 v144, v251, v251, v112\n"
    "v_perm_b32 v145, v251, v251, v113\n"
    "v_perm_b32 v146, v251, v251, v114\n"
    "v_perm_b32 v147, v251, v251, v115\n"
    "buffer_load_dwordx4 v[188:191], v111, s[20:23], s76 offen offset:3072\n"
    "v_sub_f32 v100, v100, v96\n"
    "v_sub_f32 v101, v101, v96\n"
    "v_exp_f32 v100, v100\n"
    "v_exp_f32 v101, v101\n"
    "v_mfma_f32_16x16x32_bf16 v[148:151], v[60:63], v[144:147], 0\n"
    "s_mov_b32 s76, 28672\n"
    "v_mul_f32 v92, v100, v107\n"
    "v_mul_f32 v93, v101, v109\n"
    "v_cvt_pk_bf16_f32 v102, v92, v93\n"
    "v_perm_b32 v92, v102, v102, v112\n"
    "v_perm_b32 v93, v102, v102, v113\n"
    "v_perm_b32 v94, v102, v102, v114\n"
    "v_perm_b32 v95, v102, v102, v115\n"
    "buffer_store_dwordx4 v[228:231], v245, s[4:7], s90 offen sc1\n"
    "v_add_f32 v251, v249, v250\n"
    "v_add_f32 v102, v100, v101\n"
    "v_cvt_pk_bf16_f32 v72, v72, v73\n"
    "v_add_f32 v250, v249, v250\n"
    "v_add_f32 v101, v100, v101\n"
    "v_fmac_f32 v84, v136, v116\n"
    "v_fmac_f32 v85, v137, v116\n"
    "v_fmac_f32 v86, v138, v116\n"
    "v_fmac_f32 v87, v139, v116\n"
    "v_cvt_pk_bf16_f32 v73, v74, v75\n"
    "v_cvt_pk_bf16_f32 v74, v84, v85\n"
    "v_cvt_pk_bf16_f32 v75, v86, v87\n"
    "v_permlane16_swap_b32 v251, v250\n"
    "v_permlane16_swap_b32 v102, v101\n"
    "v_mfma_f32_16x16x32_bf16 v[96:99], v[72:75], v[92:95], 0\n"
    "s_mov_b32 s90, 53248\n"
    "v_add_f32 v249, v251, v250\n"
    "v_add_f32 v100, v102, v101\n"
    "v_add_f32 v250, v251, v250\n"
    "v_add_f32 v101, v102, v101\n"
    "s_add_u32 s81, s80, 0x4000\n"
    "v_permlane32_swap_b32 v249, v250\n"
    "v_permlane32_swap_b32 v100, v101\n"
    "v_add_f32 v249, v249, v250\n"
    "v_add_f32 v100, v100, v101\n"
    "v_rcp_f32 v249, v249\n"
    "v_rcp_f32 v100, v100\n"
    "v_mul_f32 v148, v148, v249\n"
    "v_mul_f32 v149, v149, v249\n"
    "v_mul_f32 v150, v150, v249\n"
    "v_mul_f32 v151, v151, v249\n"
    "v_mul_f32 v96, v96, v100\n"
    "v_mul_f32 v97, v97, v100\n"
    "v_mul_f32 v98, v98, v100\n"
    "v_mul_f32 v99, v99, v100\n"
    "v_cvt_pk_bf16_f32 v64, v148, v149\n"
    "v_cvt_pk_bf16_f32 v65, v150, v151\n"
    "s_waitcnt vmcnt(10)\n"
    "s_barrier\n"
    "ds_read_b128 a[200:203], v88\n"
    "ds_read_b128 a[216:219], v88 offset:256\n"
    "ds_read_b128 a[232:235], v88 offset:512\n"
    "ds_read_b128 a[248:251], v88 offset:768\n"
    "v_cvt_pk_bf16_f32 v66, v96, v97\n"
    "v_cvt_pk_bf16_f32 v67, v98, v99\n"
    "v_min_u32 v64, -2, v64\n"
    "v_min_u32 v65, -2, v65\n"
    "v_min_u32 v66, -2, v66\n"
    "v_min_u32 v67, -2, v67\n"
    "s_nop 0\n"
    "v_permlane32_swap_b32 v64, v66\n"
    "v_permlane32_swap_b32 v65, v67\n"
    "buffer_load_dwordx4 v[192:195], v111, s[20:23], s88 offen\n"
    "ds_read_b128 a[192:195], v88 offset:16384\n"
    "s_waitcnt lgkmcnt(4)\n"
    "v_mfma_f32_16x16x32_bf16 v[56:59], a[4:7], a[200:203], 0\n"
    "ds_read_b128 a[196:199], v88 offset:32768\n"
    "v_mfma_f32_16x16x32_bf16 v[60:63], a[200:203], a[8:11], 0\n"
    "ds_read_b128 a[208:211], v88 offset:16640\n"
    "v_mfma_f32_16x16x32_bf16 v[8:11], a[200:203], a[200:203], 0\n"
    "ds_read_b128 a[212:215], v88 offset:33024\n"
    "buffer_load_dwordx4 v[196:199], v111, s[20:23], s88 offen offset:1024\n"
    "v_mfma_f32_16x16x32_bf16 v[20:23], v[120:123], a[200:203], 0\n"
    "ds_read_b128 a[224:227], v88 offset:16896\n"
    "s_waitcnt lgkmcnt(5)\n"
    "v_mfma_f32_16x16x32_bf16 v[56:59], a[52:55], a[216:219], v[56:59]\n"
    "ds_read_b128 a[228:231], v88 offset:33280\n"
    "v_mfma_f32_16x16x32_bf16 v[60:63], a[216:219], a[56:59], v[60:63]\n"
    "ds_read_b128 a[240:243], v88 offset:17152\n"
    "v_mfma_f32_16x16x32_bf16 v[8:11], a[216:219], a[216:219], v[8:11]\n"
    "ds_read_b128 a[244:247], v88 offset:33536\n"
    "buffer_load_dwordx4 v[200:203], v111, s[20:23], s88 offen offset:2048\n"
    "v_mfma_f32_16x16x32_bf16 v[20:23], v[120:123], a[216:219], v[20:23]\n"
    "ds_read_b128 a[204:207], v89\n"
    "v_mfma_f32_16x16x32_bf16 v[56:59], a[100:103], a[232:235], v[56:59]\n"
    "ds_read_b128 a[220:223], v89 offset:256\n"
    "v_mfma_f32_16x16x32_bf16 v[60:63], a[232:235], a[104:107], v[60:63]\n"
    "ds_read_b128 a[236:239], v89 offset:512\n"
    "v_mfma_f32_16x16x32_bf16 v[8:11], a[232:235], a[232:235], v[8:11]\n"
    "ds_read_b128 a[252:255], v89 offset:768\n"
    "buffer_load_dwordx4 v[204:207], v111, s[20:23], s88 offen offset:3072\n"
    "v_mfma_f32_16x16x32_bf16 v[20:23], v[120:123], a[232:235], v[20:23]\n"
    "ds_read_b128 a[200:203], v90\n"
    "s_mov_b32 s88, 45056\n"
    "v_mfma_f32_16x16x32_bf16 v[56:59], a[148:151], a[248:251], v[56:59]\n"
    "ds_read_b128 a[216:219], v90 offset:256\n"
    "v_mfma_f32_16x16x32_bf16 v[60:63], a[248:251], a[152:155], v[60:63]\n"
    "ds_read_b128 a[232:235], v90 offset:512\n"
    "v_mfma_f32_16x16x32_bf16 v[8:11], a[248:251], a[248:251], v[8:11]\n"
    "s_nop 1\n"
    "buffer_load_dwordx4 v[208:211], v111, s[20:23], s89 offen\n"
    "v_mfma_f32_16x16x32_bf16 v[20:23], v[120:123], a[248:251], v[20:23]\n"
    "ds_read_b128 a[248:251], v90 offset:768\n"
    "s_waitcnt lgkmcnt(5)\n"
    "v_mfma_f32_16x16x32_bf16 v[12:15], a[192:195], a[192:195], 0\n"
    "s_nop 0\n"
    "v_cndmask_b32 v10, v10, v11, s[64:65]\n"
    "v_cndmask_b32 v8, v8, v9, s[64:65]\n"
    "v_mfma_f32_16x16x32_bf16 v[76:79], v[120:123], a[192:195], 0\n"
    "s_nop 0\n"
    "v_cndmask_b32 v21, v8, v10, s[70:71]\n"
    "v_mfma_f32_16x16x32_bf16 v[16:19], a[196:199], a[196:199], 0\n"
    "s_waitcnt vmcnt(12)\n"
    "v_perm_b32 v128, v164, v160, s35\n"
    "buffer_store_dwordx4 v[64:67], v4, s[4:7], s83 offen sc1\n"
    "v_mfma_f32_16x16x32_bf16 v[80:83], v[120:123], a[196:199], 0\n"
    "ds_read_b128 a[196:199], v89 offset:16384\n"
    "v_perm_b32 v132, v164, v160, s36\n"
    "v_mfma_f32_16x16x32_bf16 v[12:15], a[208:211], a[208:211], v[12:15]\n"
    "ds_write_b64 v124, v[20:21]\n"
    "v_mfma_f32_16x16x32_bf16 v[76:79], v[120:123], a[208:211], v[76:79]\n"
    "v_perm_b32 v136, v165, v161, s35\n"
    "buffer_load_dwordx4 v[212:215], v111, s[20:23], s89 offen offset:1024\n"
    "v_mfma_f32_16x16x32_bf16 v[16:19], a[212:215], a[212:215], v[16:19]\n"
    "ds_write_b32 v126, v116 offset:640\n"
    "v_mfma_f32_16x16x32_bf16 v[80:83], v[120:123], a[212:215], v[80:83]\n"
    "ds_read_b128 a[212:215], v89 offset:16640\n"
    "v_perm_b32 v140, v165, v161, s36\n"
    "v_mfma_f32_16x16x32_bf16 v[12:15], a[224:227], a[224:227], v[12:15]\n"
    "ds_read_b32 v100, v126 offset:640\n"
    "v_perm_b32 v144, v166, v162, s35\n"
    "v_mfma_f32_16x16x32_bf16 v[76:79], v[120:123], a[224:227], v[76:79]\n"
    "ds_read_b128 v[96:99], v127 offset:576\n"
    "buffer_load_dwordx4 v[216:219], v111, s[20:23], s89 offen offset:2048\n"
    "v_mfma_f32_16x16x32_bf16 v[16:19], a[228:231], a[228:231], v[16:19]\n"
    "ds_read_b128 v[92:95], v127 offset:512\n"
    "v_perm_b32 v148, v166, v162, s36\n"
    "v_mfma_f32_16x16x32_bf16 v[80:83], v[120:123], a[228:231], v[80:83]\n"
    "ds_read_b128 a[228:231], v89 offset:16896\n"
    "v_perm_b32 v152, v167, v163, s35\n"
    "v_mfma_f32_16x16x32_bf16 v[12:15], a[240:243], a[240:243], v[12:15]\n"
    "s_waitcnt vmcnt(13)\n"
    "v_perm_b32 v129, v172, v168, s35\n"
    "v_perm_b32 v133, v172, v168, s36\n"
    "v_mfma_f32_16x16x32_bf16 v[76:79], v[120:123], a[240:243], v[76:79]\n"
    "s_nop 0\n"
    "v_perm_b32 v156, v167, v163, s36\n"
    "buffer_load_dwordx4 v[220:223], v111, s[20:23], s89 offen offset:3072\n"
    "v_mfma_f32_16x16x32_bf16 v[16:19], a[244:247], a[244:247], v[16:19]\n"
    "s_nop 0\n"
    "v_cndmask_b32 v12, v12, v13, s[64:65]\n"
    "v_cndmask_b32 v14, v14, v15, s[64:65]\n"
    "v_mfma_f32_16x16x32_bf16 v[80:83], v[120:123], a[244:247], v[80:83]\n"
    "ds_read_b128 a[244:247], v89 offset:17152\n"
    "v_cndmask_b32 v77, v12, v14, s[70:71]\n"
    "v_mfma_f32_16x16x32_bf16 v[56:59], a[16:19], a[204:207], v[56:59]\n"
    "ds_write_b64 v124, v[76:77] offset:256\n"
    "v_mfma_f32_16x16x32_bf16 v[60:63], a[204:207], a[20:23], v[60:63]\n"
    "s_mov_b32 s89, 49152\n"
    "buffer_load_dwordx4 v[224:227], v111, s[20:23], s91 offen\n"
    "v_mfma_f32_16x16x32_bf16 v[56:59], a[64:67], a[220:223], v[56:59]\n"
    "ds_read_b128 a[204:207], v91\n"
    "v_cndmask_b32 v16, v16, v17, s[64:65]\n"
    "v_mfma_f32_16x16x32_bf16 v[60:63], a[220:223], a[68:71], v[60:63]\n"
    "ds_read_b128 a[220:223], v91 offset:256\n"
    "v_cndmask_b32 v18, v18, v19, s[64:65]\n"
    "v_mfma_f32_16x16x32_bf16 v[56:59], a[112:115], a[236:239], v[56:59]\n"
    "v_cndmask_b32 v81, v16, v18, s[70:71]\n"
    "v_perm_b32 v137, v173, v169, s35\n"
    "v_mfma_f32_16x16x32_bf16 v[60:63], a[236:239], a[116:119], v[60:63]\n"
    "ds_read_b128 a[236:239], v91 offset:512\n"
    "buffer_load_dwordx4 v[228:231], v111, s[20:23], s91 offen offset:1024\n"
    "s_waitcnt lgkmcnt(3)\n"
    "v_mfma_f32_16x16x32_bf16 v[56:59], a[160:163], a[252:255], v[56:59]\n"
    "ds_write_b64 v124, v[80:81] offset:512\n"
    "v_mfma_f32_16x16x32_bf16 v[60:63], a[252:255], a[164:167], v[60:63]\n"
    "ds_read_b128 a[252:255], v91 offset:768\n"
    "v_perm_b32 v141, v173, v169, s36\n"
    "v_mfma_f32_16x16x32_bf16 v[56:59], a[28:31], a[200:203], v[56:59]\n"
    "s_waitcnt vmcnt(14)\n"
    "v_perm_b32 v130, v180, v176, s35\n"
    "v_mfma_f32_16x16x32_bf16 v[60:63], a[200:203], a[32:35], v[60:63]\n"
    "buffer_load_dword v11, v110, s[4:7], s37 offen sc1\n"
    "v_mfma_f32_16x16x32_bf16 v[56:59], a[76:79], a[216:219], v[56:59]\n"
    "buffer_load_dwordx4 v[232:235], v111, s[20:23], s91 offen offset:2048\n"
    "v_mfma_f32_16x16x32_bf16 v[60:63], a[216:219], a[80:83], v[60:63]\n"
    "ds_read_b128 a[200:203], v90 offset:16384\n"
    "v_perm_b32 v134, v180, v176, s36\n"
    "v_mfma_f32_16x16x32_bf16 v[56:59], a[124:127], a[232:235], v[56:59]\n"
    "ds_read_b128 a[216:219], v90 offset:16640\n"
    "v_perm_b32 v138, v181, v177, s35\n"
    "v_mfma_f32_16x16x32_bf16 v[60:63], a[232:235], a[128:131], v[60:63]\n"
    "ds_read_b128 a[232:235], v90 offset:16896\n"
    "v_mfma_f32_16x16x32_bf16 v[56:59], a[172:175], a[248:251], v[56:59]\n"
    "s_waitcnt vmcnt(14)\n"
    "v_perm_b32 v131, v188, v184, s35\n"
    "buffer_load_dwordx4 v[236:239], v111, s[20:23], s91 offen offset:3072\n"
    "v_mfma_f32_16x16x32_bf16 v[60:63], a[248:251], a[176:179], v[60:63]\n"
    "ds_read_b128 a[248:251], v90 offset:17152\n"
    "s_waitcnt lgkmcnt(4)\n"
    "v_mfma_f32_16x16x32_bf16 v[56:59], a[40:43], a[204:207], v[56:59]\n"
    "s_barrier\n"
    "v_mfma_f32_16x16x32_bf16 v[60:63], a[204:207], a[44:47], v[60:63]\n"
    "ds_read_b128 v[116:119], v104\n"
    "v_perm_b32 v135, v188, v184, s36\n"
    "buffer_load_dwordx4 v[240:243], v111, s[20:23], s92 offen\n"
    "v_mfma_f32_16x16x32_bf16 v[56:59], a[88:91], a[220:223], v[56:59]\n"
    "ds_read_b128 v[120:123], v104 offset:1024\n"
    "v_perm_b32 v139, v189, v185, s35\n"
    "v_mfma_f32_16x16x32_bf16 v[60:63], a[220:223], a[92:95], v[60:63]\n"
    "ds_read_b128 a[204:207], v91 offset:16384\n"
    "v_perm_b32 v142, v181, v177, s36\n"
    "v_mfma_f32_16x16x32_bf16 v[56:59], a[136:139], a[236:239], v[56:59]\n"
    "ds_read_b128 a[220:223], v91 offset:16640\n"
    "v_perm_b32 v143, v189, v185, s36\n"
    "v_mfma_f32_16x16x32_bf16 v[60:63], a[236:239], a[140:143], v[60:63]\n"
    "ds_read_b128 a[236:239], v91 offset:16896\n"
    "buffer_load_dwordx4 v[244:247], v111, s[20:23], s92 offen offset:1024\n"
    "v_mfma_f32_16x16x32_bf16 v[56:59], a[184:187], a[252:255], v[56:59]\n"
    "s_waitcnt lgkmcnt(4)\n"
    "v_add_f32 v116, v116, v118\n"
    "v_mfma_f32_16x16x32_bf16 v[60:63], a[252:255], a[188:191], v[60:63]\n"
    "ds_read_b128 a[252:255], v91 offset:17152\n"
    "v_add_f32 v117, v117, v119\n"
    "v_mfma_f32_16x16x32_bf16 v[64:67], a[0:3], a[192:195], 0\n"
    "s_waitcnt lgkmcnt(4)\n"
    "v_add_f32 v120, v120, v122\n"
    "v_add_f32 v116, v116, v120\n"
    "v_mfma_f32_16x16x32_bf16 v[68:71], a[4:7], a[192:195], 0\n"
    "v_mul_f32 v116, 0xbb000000, v116\n"
    "buffer_load_dwordx4 v[248:251], v111, s[20:23], s92 offen offset:2048\n"
    "v_mfma_f32_16x16x32_bf16 v[64:67], a[48:51], a[208:211], v[64:67]\n"
    "ds_write_b32 v125, v116 offset:256\n"
    "v_mfma_f32_16x16x32_bf16 v[68:71], a[52:55], a[208:211], v[68:71]\n"
    "ds_read_b128 v[72:75], v127 offset:320\n"
    "v_add_f32 v121, v121, v123\n"
    "v_mfma_f32_16x16x32_bf16 v[64:67], a[96:99], a[224:227], v[64:67]\n"
    "ds_read_b32 v101, v126 offset:384\n"
    "v_add_f32 v117, v117, v121\n"
    "v_mfma_f32_16x16x32_bf16 v[68:71], a[100:103], a[224:227], v[68:71]\n"
    "ds_read2_b32 v[102:103], v126 offset0:64 offset1:80\n"
    "buffer_load_dwordx4 v[252:255], v111, s[20:23], s92 offen offset:3072\n"
    "v_mfma_f32_16x16x32_bf16 v[64:67], a[144:147], a[240:243], v[64:67]\n"
    "v_mul_f32 v117, 0x3b000000, v117\n"
    "v_fma_f32 v117, -v116, v116, v117\n"
    "v_mfma_f32_16x16x32_bf16 v[68:71], a[148:151], a[240:243], v[68:71]\n"
    "v_max_f32 v117, 0, v117\n"
    "v_add_f32 v117, 0x3727c5ac, v117\n"
    "v_mfma_f32_16x16x32_bf16 v[64:67], a[12:15], a[196:199], v[64:67]\n"
    "v_rsq_f32 v117, v117\n"
    "v_mfma_f32_16x16x32_bf16 v[68:71], a[16:19], a[196:199], v[68:71]\n"
    "s_waitcnt lgkmcnt(2)\n"
    "v_mul_f32 v72, v72, v100\n"
    "buffer_load_dwordx4 v[24:27], v111, s[20:23], s38 offen\n"
    "v_mfma_f32_16x16x32_bf16 v[64:67], a[60:63], a[212:215], v[64:67]\n"
    "s_waitcnt lgkmcnt(1)\n"
    "v_mul_f32 v73, v73, v100\n"
    "v_mul_f32 v74, v74, v100\n"
    "v_mfma_f32_16x16x32_bf16 v[68:71], a[64:67], a[212:215], v[68:71]\n"
    "s_waitcnt lgkmcnt(0)\n"
    "v_mul_f32 v75, v75, v100\n"
    "v_mul_f32 v80, v101, v96\n"
    "v_mfma_f32_16x16x32_bf16 v[64:67], a[108:111], a[228:231], v[64:67]\n"
    "v_mul_f32 v81, v101, v97\n"
    "v_mul_f32 v82, v101, v98\n"
    "v_mfma_f32_16x16x32_bf16 v[68:71], a[112:115], a[228:231], v[68:71]\n"
    "v_mul_f32 v83, v101, v99\n"
    "buffer_load_dwordx4 v[28:31], v111, s[20:23], s38 offen offset:1024\n"
    "v_mfma_f32_16x16x32_bf16 v[64:67], a[156:159], a[244:247], v[64:67]\n"
    "ds_write_b32 v125, v117\n"
    "v_mfma_f32_16x16x32_bf16 v[68:71], a[160:163], a[244:247], v[68:71]\n"
    "v_fmac_f32 v56, v102, v96\n"
    "v_fmac_f32 v57, v102, v97\n"
    "v_mfma_f32_16x16x32_bf16 v[64:67], a[24:27], a[200:203], v[64:67]\n"
    "v_fmac_f32 v58, v102, v98\n"
    "v_fmac_f32 v59, v102, v99\n"
    "v_mfma_f32_16x16x32_bf16 v[68:71], a[28:31], a[200:203], v[68:71]\n"
    "v_cvt_pk_bf16_f32 v56, v56, v57\n"
    "buffer_load_dwordx4 v[32:35], v111, s[20:23], s38 offen offset:2048\n"
    "v_mfma_f32_16x16x32_bf16 v[64:67], a[72:75], a[216:219], v[64:67]\n"
    "v_cvt_pk_bf16_f32 v57, v58, v59\n"
    "v_mul_f32 v76, v101, v92\n"
    "v_mfma_f32_16x16x32_bf16 v[68:71], a[76:79], a[216:219], v[68:71]\n"
    "v_mul_f32 v77, v101, v93\n"
    "v_mul_f32 v78, v101, v94\n"
    "v_mfma_f32_16x16x32_bf16 v[64:67], a[120:123], a[232:235], v[64:67]\n"
    "v_mul_f32 v79, v101, v95\n"
    "v_perm_b32 v145, v174, v170, s35\n"
    "v_mfma_f32_16x16x32_bf16 v[68:71], a[124:127], a[232:235], v[68:71]\n"
    "ds_read_b128 v[84:87], v127 offset:384\n"
    "buffer_load_dwordx4 v[36:39], v111, s[20:23], s38 offen offset:3072\n"
    "v_mfma_f32_16x16x32_bf16 v[64:67], a[168:171], a[248:251], v[64:67]\n"
    "ds_read_b128 v[120:123], v127 offset:256\n"
    "v_perm_b32 v149, v174, v170, s36\n"
    "v_mfma_f32_16x16x32_bf16 v[68:71], a[172:175], a[248:251], v[68:71]\n"
    "ds_read2_b32 v[104:105], v126 offset0:16 offset1:32\n"
    "v_perm_b32 v153, v175, v171, s35\n"
    "v_mfma_f32_16x16x32_bf16 v[64:67], a[36:39], a[204:207], v[64:67]\n"
    "ds_read2_b32 v[106:107], v6 offset0:0 offset1:16\n"
    "v_perm_b32 v157, v175, v171, s36\n"
    "v_mfma_f32_16x16x32_bf16 v[68:71], a[40:43], a[204:207], v[68:71]\n"
    "ds_read2_b32 v[108:109], v7 offset0:2 offset1:18\n"
    "buffer_load_dwordx4 v[40:43], v111, s[20:23], s39 offen\n"
    "v_mfma_f32_16x16x32_bf16 v[64:67], a[84:87], a[220:223], v[64:67]\n"
    "s_waitcnt vmcnt(21)\n"
    "v_perm_b32 v160, v196, v192, s35\n"
    "v_perm_b32 v164, v196, v192, s36\n"
    "v_mfma_f32_16x16x32_bf16 v[68:71], a[88:91], a[220:223], v[68:71]\n"
    "s_waitcnt lgkmcnt(4)\n"
    "v_mul_f32 v84, v84, v100\n"
    "v_mul_f32 v85, v85, v100\n"
    "v_mfma_f32_16x16x32_bf16 v[64:67], a[132:135], a[236:239], v[64:67]\n"
    "s_waitcnt lgkmcnt(3)\n"
    "v_mul_f32 v86, v86, v100\n"
    "v_mul_f32 v87, v87, v100\n"
    "v_mfma_f32_16x16x32_bf16 v[68:71], a[136:139], a[236:239], v[68:71]\n"
    "s_waitcnt vmcnt(19)\n"
    "v_perm_b32 v161, v204, v200, s35\n"
    "buffer_load_dwordx4 v[44:47], v111, s[20:23], s39 offen offset:1024\n"
    "v_mfma_f32_16x16x32_bf16 v[64:67], a[180:183], a[252:255], v[64:67]\n"
    "s_waitcnt vmcnt(17)\n"
    "v_perm_b32 v162, v212, v208, s35\n"
    "v_fmac_f32 v60, v120, v100\n"
    "v_mfma_f32_16x16x32_bf16 v[68:71], a[184:187], a[252:255], v[68:71]\n"
    "s_waitcnt lgkmcnt(0)\n"
    "v_fmac_f32 v61, v121, v100\n"
    "v_fmac_f32 v62, v122, v100\n"
    "v_mfma_f32_16x16x32_bf16 v[72:75], a[192:195], a[8:11], v[72:75]\n"
    "ds_read_b128 a[192:195], v88 offset:32768\n"
    "v_fmac_f32 v64, v103, v92\n"
    "v_mfma_f32_16x16x32_bf16 v[72:75], a[208:211], a[56:59], v[72:75]\n"
    "ds_read_b128 a[208:211], v88 offset:33024\n"
    "buffer_load_dwordx4 v[48:51], v111, s[20:23], s39 offen offset:2048\n"
    "v_mfma_f32_16x16x32_bf16 v[72:75], a[224:227], a[104:107], v[72:75]\n"
    "ds_read_b128 a[224:227], v88 offset:33280\n"
    "v_fmac_f32 v65, v103, v93\n"
    "v_mfma_f32_16x16x32_bf16 v[72:75], a[240:243], a[152:155], v[72:75]\n"
    "ds_read_b128 a[240:243], v88 offset:33536\n"
    "v_fmac_f32 v66, v103, v94\n"
    "v_mfma_f32_16x16x32_bf16 v[72:75], a[196:199], a[20:23], v[72:75]\n"
    "ds_read_b128 a[196:199], v89 offset:32768\n"
    "v_fmac_f32 v67, v103, v95\n"
    "v_mfma_f32_16x16x32_bf16 v[72:75], a[212:215], a[68:71], v[72:75]\n"
    "ds_read_b128 a[212:215], v89 offset:33024\n"
    "buffer_load_dwordx4 v[52:55], v111, s[20:23], s39 offen offset:3072\n"
    "v_mfma_f32_16x16x32_bf16 v[72:75], a[228:231], a[116:119], v[72:75]\n"
    "ds_read_b128 a[228:231], v89 offset:33280\n"
    "v_cvt_pk_bf16_f32 v64, v64, v65\n"
    "v_mfma_f32_16x16x32_bf16 v[72:75], a[244:247], a[164:167], v[72:75]\n"
    "ds_read_b128 a[244:247], v89 offset:33536\n"
    "v_cvt_pk_bf16_f32 v65, v66, v67\n"
    "v_mfma_f32_16x16x32_bf16 v[72:75], a[200:203], a[32:35], v[72:75]\n"
    "ds_read_b128 a[200:203], v90 offset:32768\n"
    "v_fmac_f32 v68, v103, v96\n"
    "v_mfma_f32_16x16x16_bf16 v[116:119], v[56:57], v[64:65], 0\n"
    "s_nop 0\n"
    "v_fmac_f32 v69, v103, v97\n"
    "buffer_load_dwordx4 v[56:59], v0, s[96:99], s41 offen\n"
    "v_mfma_f32_16x16x32_bf16 v[72:75], a[216:219], a[80:83], v[72:75]\n"
    "ds_read_b128 a[216:219], v90 offset:33024\n"
    "v_fmac_f32 v70, v103, v98\n"
    "v_mfma_f32_16x16x32_bf16 v[72:75], a[232:235], a[128:131], v[72:75]\n"
    "ds_read_b128 a[232:235], v90 offset:33280\n"
    "v_fmac_f32 v71, v103, v99\n"
    "v_mfma_f32_16x16x32_bf16 v[72:75], a[248:251], a[176:179], v[72:75]\n"
    "ds_read_b128 a[248:251], v90 offset:33536\n"
    "s_waitcnt vmcnt(15)\n"
    "v_cmp_ne_u32 vcc, s56, v11\n"
    "v_mfma_f32_16x16x32_bf16 v[72:75], a[204:207], a[44:47], v[72:75]\n"
    "ds_read_b128 a[204:207], v91 offset:32768\n"
    "v_mfma_f32_16x16x32_bf16 v[72:75], a[220:223], a[92:95], v[72:75]\n"
    "s_add_u32 s91, s84, 0x401000\n"
    "s_cbranch_vccnz .R_1\n"
    ".R_1c:\n"
    "buffer_load_dwordx4 v[8:11], v1, s[4:7], s46 offen sc1\n"
    "v_mfma_f32_16x16x32_bf16 v[72:75], a[236:239], a[140:143], v[72:75]\n"
    "ds_read_b128 a[220:223], v91 offset:33024\n"
    "v_mfma_f32_16x16x32_bf16 v[72:75], a[252:255], a[188:191], v[72:75]\n"
    "buffer_load_dwordx4 v[12:15], v1, s[4:7], s47 offen sc1\n"
    "s_waitcnt lgkmcnt(3)\n"
    "v_mfma_f32_16x16x32_bf16 v[80:83], a[4:7], a[192:195], v[80:83]\n"
    "ds_read_b128 a[236:239], v91 offset:33280\n"
    "v_cvt_pk_bf16_f32 v68, v68, v69\n"
    "v_mfma_f32_16x16x32_bf16 v[80:83], a[52:55], a[208:211], v[80:83]\n"
    "ds_read_b128 a[252:255], v91 offset:33536\n"
    "v_cvt_pk_bf16_f32 v69, v70, v71\n"
    "v_mfma_f32_16x16x32_bf16 v[80:83], a[100:103], a[224:227], v[80:83]\n"
    "v_mfma_f32_16x16x32_bf16 v[80:83], a[148:151], a[240:243], v[80:83]\n"
    "buffer_load_dwordx4 v[16:19], v1, s[4:7], s48 offen sc1\n"
    "v_fmac_f32 v63, v123, v100\n"
    "v_mfma_f32_16x16x16_bf16 v[120:123], v[68:69], v[64:65], 0\n"
    "s_nop 0\n"
    "v_perm_b32 v163, v220, v216, s35\n"
    "v_perm_b32 v168, v197, v193, s35\n"
    "v_mfma_f32_16x16x32_bf16 v[80:83], a[16:19], a[196:199], v[80:83]\n"
    "s_nop 0\n"
    "v_perm_b32 v172, v197, v193, s36\n"
    "v_perm_b32 v165, v204, v200, s36\n"
    "v_mfma_f32_16x16x32_bf16 v[80:83], a[64:67], a[212:215], v[80:83]\n"
    "v_mfma_f32_16x16x32_bf16 v[80:83], a[112:115], a[228:231], v[80:83]\n"
    "buffer_load_dwordx4 v[20:23], v1, s[4:7], s49 offen sc1\n"
    "v_perm_b32 v166, v212, v208, s36\n"
    "v_mfma_f32_16x16x32_bf16 v[80:83], a[160:163], a[244:247], v[80:83]\n"
    "v_perm_b32 v176, v198, v194, s35\n"
    "v_perm_b32 v167, v220, v216, s36\n"
    "v_mfma_f32_16x16x32_bf16 v[80:83], a[28:31], a[200:203], v[80:83]\n"
    "v_mul_f32 v104, 0x3eb8aa3b, v104\n"
    "v_cndmask_b32 v116, v116, v117, s[64:65]\n"
    "v_mfma_f32_16x16x32_bf16 v[80:83], a[76:79], a[216:219], v[80:83]\n"
    "v_cndmask_b32 v120, v120, v121, s[64:65]\n"
    "v_cndmask_b32 v118, v118, v119, s[64:65]\n"
    "v_mfma_f32_16x16x32_bf16 v[80:83], a[124:127], a[232:235], v[80:83]\n"
    "s_waitcnt lgkmcnt(0)\n"
    "v_cndmask_b32 v122, v122, v123, s[64:65]\n"
    "v_mul_f32 v70, v104, v106\n"
    "v_mfma_f32_16x16x32_bf16 v[80:83], a[172:175], a[248:251], v[80:83]\n"
    "v_mul_f32 v71, v104, v108\n"
    "v_cndmask_b32 v116, v116, v120, s[66:67]\n"
    "v_mfma_f32_16x16x32_bf16 v[80:83], a[40:43], a[204:207], v[80:83]\n"
    "v_cndmask_b32 v118, v118, v122, s[68:69]\n"
    "v_perm_b32 v180, v198, v194, s36\n"
    "v_mfma_f32_16x16x32_bf16 v[80:83], a[88:91], a[220:223], v[80:83]\n"
    "v_mul_f32 v70, v70, v116\n"
    "v_mul_f32 v71, v71, v118\n"
    "v_mfma_f32_16x16x32_bf16 v[80:83], a[136:139], a[236:239], v[80:83]\n"
    "v_max_f32 v120, v70, v71\n"
    "v_max_f32 v121, v70, v71\n"
    "v_mfma_f32_16x16x32_bf16 v[80:83], a[184:187], a[252:255], v[80:83]\n"
    "s_nop 0\n"
    "v_perm_b32 v184, v199, v195, s35\n"
    "v_perm_b32 v188, v199, v195, s36\n"
    "v_mfma_f32_16x16x32_bf16 v[76:79], a[0:3], a[192:195], v[76:79]\n"
    "s_nop 0\n"
    "v_permlane16_swap_b32 v120, v121\n"
    "v_mfma_f32_16x16x32_bf16 v[76:79], a[48:51], a[208:211], v[76:79]\n"
    "v_max_f32 v122, v120, v121\n"
    "v_max_f32 v123, v120, v121\n"
    "v_mfma_f32_16x16x32_bf16 v[76:79], a[96:99], a[224:227], v[76:79]\n"
    "s_nop 0\n"
    "v_permlane32_swap_b32 v122, v123\n"
    "v_mfma_f32_16x16x32_bf16 v[76:79], a[144:147], a[240:243], v[76:79]\n"
    "v_max_f32 v120, v122, v123\n"
    "v_sub_f32 v70, v70, v120\n"
    "v_mfma_f32_16x16x32_bf16 v[76:79], a[12:15], a[196:199], v[76:79]\n"
    "v_sub_f32 v71, v71, v120\n"
    "v_perm_b32 v192, v228, v224, s35\n"
    "v_mfma_f32_16x16x32_bf16 v[76:79], a[60:63], a[212:215], v[76:79]\n"
    "v_exp_f32 v70, v70\n"
    "v_mfma_f32_16x16x32_bf16 v[76:79], a[108:111], a[228:231], v[76:79]\n"
    "v_exp_f32 v71, v71\n"
    "v_mfma_f32_16x16x32_bf16 v[76:79], a[156:159], a[244:247], v[76:79]\n"
    "s_waitcnt vmcnt(17)\n"
    "v_perm_b32 v193, v236, v232, s35\n"
    "v_cvt_pk_bf16_f32 v60, v60, v61\n"
    "v_mfma_f32_16x16x32_bf16 v[76:79], a[24:27], a[200:203], v[76:79]\n"
    "s_waitcnt vmcnt(15)\n"
    "v_perm_b32 v194, v244, v240, s35\n"
    "v_mul_f32 v116, v70, v106\n"
    "v_mfma_f32_16x16x32_bf16 v[76:79], a[72:75], a[216:219], v[76:79]\n"
    "s_waitcnt vmcnt(13)\n"
    "v_perm_b32 v195, v252, v248, s35\n"
    "v_mul_f32 v117, v71, v108\n"
    "v_mfma_f32_16x16x32_bf16 v[76:79], a[120:123], a[232:235], v[76:79]\n"
    "v_perm_b32 v196, v228, v224, s36\n"
    "v_cvt_pk_bf16_f32 v61, v62, v63\n"
    "v_mfma_f32_16x16x32_bf16 v[76:79], a[168:171], a[248:251], v[76:79]\n"
    "v_cvt_pk_bf16_f32 v103, v116, v117\n"
    "v_perm_b32 v197, v236, v232, s36\n"
    "v_mfma_f32_16x16x32_bf16 v[76:79], a[36:39], a[204:207], v[76:79]\n"
    "v_cvt_pk_bf16_f32 v62, v72, v73\n"
    "v_cvt_pk_bf16_f32 v63, v74, v75\n"
    "v_mfma_f32_16x16x32_bf16 v[76:79], a[84:87], a[220:223], v[76:79]\n"
    "v_perm_b32 v116, v103, v103, v112\n"
    "v_perm_b32 v117, v103, v103, v113\n"
    "v_mfma_f32_16x16x32_bf16 v[76:79], a[132:135], a[236:239], v[76:79]\n"
    "v_perm_b32 v118, v103, v103, v114\n"
    "v_perm_b32 v119, v103, v103, v115\n"
    "v_mfma_f32_16x16x32_bf16 v[76:79], a[180:183], a[252:255], v[76:79]\n"
    "s_nop 0\n"
    "v_perm_b32 v198, v244, v240, s36\n"
    "v_perm_b32 v199, v252, v248, s36\n"
    "v_mfma_f32_16x16x32_bf16 v[120:123], v[60:63], v[116:119], 0\n"
    "buffer_load_dwordx4 v[60:63], v0, s[96:99], s42 offen\n"
    "v_perm_b32 v169, v205, v201, s35\n"
    "v_mfma_f32_16x16x32_bf16 v[84:87], a[200:203], a[32:35], v[84:87]\n"
    "v_perm_b32 v173, v205, v201, s36\n"
    "v_perm_b32 v170, v213, v209, s35\n"
    "v_mfma_f32_16x16x32_bf16 v[84:87], a[216:219], a[80:83], v[84:87]\n"
    "v_perm_b32 v171, v221, v217, s35\n"
    "v_perm_b32 v146, v182, v178, s35\n"
    "v_mfma_f32_16x16x32_bf16 v[84:87], a[232:235], a[128:131], v[84:87]\n"
    "v_perm_b32 v177, v206, v202, s35\n"
    "v_perm_b32 v147, v190, v186, s35\n"
    "v_mfma_f32_16x16x32_bf16 v[84:87], a[248:251], a[176:179], v[84:87]\n"
    "v_perm_b32 v200, v229, v225, s35\n"
    "v_perm_b32 v150, v182, v178, s36\n"
    "v_mfma_f32_16x16x32_bf16 v[84:87], a[204:207], a[44:47], v[84:87]\n"
    "v_perm_b32 v181, v206, v202, s36\n"
    "v_perm_b32 v151, v190, v186, s36\n"
    "v_mfma_f32_16x16x32_bf16 v[84:87], a[220:223], a[92:95], v[84:87]\n"
    "v_perm_b32 v204, v229, v225, s36\n"
    "v_perm_b32 v174, v213, v209, s36\n"
    "v_mfma_f32_16x16x32_bf16 v[84:87], a[236:239], a[140:143], v[84:87]\n"
    "v_perm_b32 v175, v221, v217, s36\n"
    "v_perm_b32 v185, v207, v203, s35\n"
    "v_mfma_f32_16x16x32_bf16 v[84:87], a[252:255], a[188:191], v[84:87]\n"
    "v_perm_b32 v208, v230, v226, s35\n"
    "v_perm_b32 v154, v183, v179, s35\n"
    "v_mfma_f32_16x16x32_bf16 v[84:87], a[192:195], a[8:11], v[84:87]\n"
    "v_perm_b32 v189, v207, v203, s36\n"
    "v_perm_b32 v212, v230, v226, s36\n"
    "v_mfma_f32_16x16x32_bf16 v[84:87], a[208:211], a[56:59], v[84:87]\n"
    "v_perm_b32 v158, v183, v179, s36\n"
    "v_perm_b32 v216, v231, v227, s35\n"
    "v_mfma_f32_16x16x32_bf16 v[84:87], a[224:227], a[104:107], v[84:87]\n"
    "s_waitcnt vmcnt(12)\n"
    "v_perm_b32 v224, v28, v24, s35\n"
    "v_perm_b32 v228, v28, v24, s36\n"
    "v_mfma_f32_16x16x32_bf16 v[84:87], a[240:243], a[152:155], v[84:87]\n"
    "v_perm_b32 v220, v231, v227, s36\n"
    "v_perm_b32 v201, v237, v233, s35\n"
    "v_mfma_f32_16x16x32_bf16 v[84:87], a[196:199], a[20:23], v[84:87]\n"
    "v_perm_b32 v232, v29, v25, s35\n"
    "v_perm_b32 v202, v245, v241, s35\n"
    "v_mfma_f32_16x16x32_bf16 v[84:87], a[212:215], a[68:71], v[84:87]\n"
    "v_perm_b32 v203, v253, v249, s35\n"
    "v_perm_b32 v205, v237, v233, s36\n"
    "v_mfma_f32_16x16x32_bf16 v[84:87], a[228:231], a[116:119], v[84:87]\n"
    "s_waitcnt vmcnt(10)\n"
    "v_perm_b32 v225, v36, v32, s35\n"
    "v_perm_b32 v236, v29, v25, s36\n"
    "v_mfma_f32_16x16x32_bf16 v[84:87], a[244:247], a[164:167], v[84:87]\n"
    "s_waitcnt vmcnt(8)\n"
    "v_perm_b32 v226, v44, v40, s35\n"
    "v_perm_b32 v206, v245, v241, s36\n"
    "v_perm_b32 v207, v253, v249, s36\n"
    "v_cvt_pk_bf16_f32 v76, v76, v77\n"
    "v_cvt_pk_bf16_f32 v80, v80, v81\n"
    "v_cvt_pk_bf16_f32 v77, v78, v79\n"
    "v_cvt_pk_bf16_f32 v81, v82, v83\n"
    "v_perm_b32 v178, v214, v210, s35\n"
    "v_perm_b32 v240, v30, v26, s35\n"
    "v_mfma_f32_16x16x16_bf16 v[92:95], v[68:69], v[76:77], 0\n"
    "s_nop 0\n"
    "v_perm_b32 v182, v214, v210, s36\n"
    "v_perm_b32 v244, v30, v26, s36\n"
    "v_mfma_f32_16x16x16_bf16 v[96:99], v[80:81], v[76:77], 0\n"
    "s_mov_b32 s37, 0xb32000\n"
    "v_perm_b32 v179, v222, v218, s35\n"
    "v_perm_b32 v155, v191, v187, s35\n"
    "v_perm_b32 v186, v215, v211, s35\n"
    "v_perm_b32 v248, v31, v27, s35\n"
    "v_perm_b32 v209, v238, v234, s35\n"
    "v_perm_b32 v159, v191, v187, s36\n"
    "v_perm_b32 v190, v215, v211, s36\n"
    "v_perm_b32 v252, v31, v27, s36\n"
    "v_mul_f32 v105, 0x3eb8aa3b, v105\n"
    "v_cndmask_b32 v92, v92, v93, s[64:65]\n"
    "v_cndmask_b32 v96, v96, v97, s[64:65]\n"
    "v_cndmask_b32 v94, v94, v95, s[64:65]\n"
    "v_cndmask_b32 v98, v98, v99, s[64:65]\n"
    "v_mul_f32 v100, v105, v107\n"
    "s_waitcnt vmcnt(4)\n"
    "v_max3_u32 v90, v8, v9, v10\n"
    "s_waitcnt vmcnt(3)\n"
    "v_max3_u32 v90, v90, v11, v12\n"
    "v_max3_u32 v90, v90, v13, v14\n"
    "v_perm_b32 v227, v52, v48, s35\n"
    "v_mul_f32 v101, v105, v109\n"
    "v_cndmask_b32 v92, v92, v96, s[66:67]\n"
    "v_cndmask_b32 v94, v94, v98, s[68:69]\n"
    "v_perm_b32 v213, v238, v234, s36\n"
    "v_mul_f32 v100, v100, v92\n"
    "v_mul_f32 v101, v101, v94\n"
    "v_perm_b32 v183, v222, v218, s36\n"
    "v_perm_b32 v229, v36, v32, s36\n"
    "s_waitcnt vmcnt(2)\n"
    "v_max3_u32 v90, v90, v15, v16\n"
    "v_max3_u32 v90, v90, v17, v18\n"
    "v_max_f32 v96, v100, v101\n"
    "v_max_f32 v97, v100, v101\n"
    "v_perm_b32 v230, v44, v40, s36\n"
    "v_perm_b32 v231, v52, v48, s36\n"
    "v_perm_b32 v217, v239, v235, s35\n"
    "v_permlane16_swap_b32 v96, v97\n"
    "v_max_f32 v98, v96, v97\n"
    "v_max_f32 v99, v96, v97\n"
    "v_perm_b32 v221, v239, v235, s36\n"
    "v_perm_b32 v233, v37, v33, s35\n"
    "v_permlane32_swap_b32 v98, v99\n"
    "s_waitcnt vmcnt(1)\n"
    "v_max3_u32 v90, v90, v19, v20\n"
    "v_max3_u32 v90, v90, v21, v22\n"
    "v_max_u32 v90, v90, v23\n"
    "v_cmp_eq_u32 vcc, -1, v90\n"
    "v_max_f32 v96, v98, v99\n"
    "v_sub_f32 v100, v100, v96\n"
    "v_sub_f32 v101, v101, v96\n"
    "v_exp_f32 v100, v100\n"
    "v_perm_b32 v237, v37, v33, s36\n"
    "s_cbranch_vccnz .R_2\n"
    ".R_2c:\n"
    "v_mfma_f32_16x16x32_bf16 v[24:27], v[8:11], v[128:131], 0\n"
    "v_exp_f32 v101, v101\n"
    "v_mfma_f32_16x16x32_bf16 v[24:27], v[12:15], v[160:163], v[24:27]\n"
    "v_mul_f32 v92, v100, v107\n"
    "v_mul_f32 v93, v101, v109\n"
    "v_mfma_f32_16x16x32_bf16 v[24:27], v[16:19], v[192:195], v[24:27]\n"
    "v_cvt_pk_bf16_f32 v102, v92, v93\n"
    "v_perm_b32 v92, v102, v102, v112\n"
    "v_mfma_f32_16x16x32_bf16 v[24:27], v[20:23], v[224:227], v[24:27]\n"
    "s_nop 0\n"
    "v_perm_b32 v93, v102, v102, v113\n"
    "v_perm_b32 v94, v102, v102, v114\n"
    "v_mfma_f32_16x16x32_bf16 v[28:31], v[8:11], v[132:135], 0\n"
    "s_nop 0\n"
    "v_perm_b32 v95, v102, v102, v115\n"
    "v_perm_b32 v210, v246, v242, s35\n"
    "v_mfma_f32_16x16x32_bf16 v[28:31], v[12:15], v[164:167], v[28:31]\n"
    "v_perm_b32 v241, v38, v34, s35\n"
    "v_add_f32 v103, v70, v71\n"
    "v_mfma_f32_16x16x32_bf16 v[28:31], v[16:19], v[196:199], v[28:31]\n"
    "v_add_f32 v102, v100, v101\n"
    "v_perm_b32 v211, v254, v250, s35\n"
    "v_mfma_f32_16x16x32_bf16 v[28:31], v[20:23], v[228:231], v[28:31]\n"
    "s_add_u32 s46, s82, 0x100000\n"
    "v_cvt_pk_bf16_f32 v72, v72, v73\n"
    "v_add_f32 v71, v70, v71\n"
    "v_add_f32 v101, v100, v101\n"
    "v_cvt_pk_bf16_f32 v73, v74, v75\n"
    "v_perm_b32 v234, v45, v41, s35\n"
    "v_cvt_pk_bf16_f32 v74, v84, v85\n"
    "v_cvt_pk_bf16_f32 v75, v86, v87\n"
    "v_perm_b32 v214, v246, v242, s36\n"
    "v_perm_b32 v235, v53, v49, s35\n"
    "v_perm_b32 v245, v38, v34, s36\n"
    "v_permlane16_swap_b32 v103, v71\n"
    "v_permlane16_swap_b32 v102, v101\n"
    "v_perm_b32 v215, v254, v250, s36\n"
    "v_mfma_f32_16x16x32_bf16 v[96:99], v[72:75], v[92:95], 0\n"
    "ds_write_b128 v56, v[24:27]\n"
    "v_add_f32 v70, v103, v71\n"
    "v_add_f32 v100, v102, v101\n"
    "v_add_f32 v71, v103, v71\n"
    "v_add_f32 v101, v102, v101\n"
    "v_perm_b32 v187, v223, v219, s35\n"
    "v_perm_b32 v249, v39, v35, s35\n"
    "v_permlane32_swap_b32 v70, v71\n"
    "v_permlane32_swap_b32 v100, v101\n"
    "v_perm_b32 v191, v223, v219, s36\n"
    "v_perm_b32 v253, v39, v35, s36\n"
    "v_mfma_f32_16x16x32_bf16 v[32:35], v[8:11], v[136:139], 0\n"
    "v_add_f32 v70, v70, v71\n"
    "v_add_f32 v100, v100, v101\n"
    "v_mfma_f32_16x16x32_bf16 v[32:35], v[12:15], v[168:171], v[32:35]\n"
    "v_rcp_f32 v70, v70\n"
    "v_mfma_f32_16x16x32_bf16 v[32:35], v[16:19], v[200:203], v[32:35]\n"
    "v_rcp_f32 v100, v100\n"
    "v_mfma_f32_16x16x32_bf16 v[32:35], v[20:23], v[232:235], v[32:35]\n"
    "ds_write_b128 v56, v[28:31] offset:1024\n"
    "v_mfma_f32_16x16x32_bf16 v[36:39], v[8:11], v[140:143], 0\n"
    "v_mul_f32 v120, v120, v70\n"
    "v_mul_f32 v121, v121, v70\n"
    "v_mfma_f32_16x16x32_bf16 v[36:39], v[12:15], v[172:175], v[36:39]\n"
    "v_mul_f32 v122, v122, v70\n"
    "v_mul_f32 v123, v123, v70\n"
    "v_mfma_f32_16x16x32_bf16 v[36:39], v[16:19], v[204:207], v[36:39]\n"
    "s_xor_b32 s47, s46, 1024\n"
    "v_mul_f32 v96, v96, v100\n"
    "v_mul_f32 v97, v97, v100\n"
    "v_mul_f32 v98, v98, v100\n"
    "v_mul_f32 v99, v99, v100\n"
    "v_cvt_pk_bf16_f32 v64, v120, v121\n"
    "v_cvt_pk_bf16_f32 v65, v122, v123\n"
    "v_cvt_pk_bf16_f32 v66, v96, v97\n"
    "v_cvt_pk_bf16_f32 v67, v98, v99\n"
    "v_perm_b32 v218, v247, v243, s35\n"
    "v_min_u32 v64, -2, v64\n"
    "v_min_u32 v65, -2, v65\n"
    "v_min_u32 v66, -2, v66\n"
    "v_min_u32 v67, -2, v67\n"
    "v_perm_b32 v222, v247, v243, s36\n"
    "v_permlane32_swap_b32 v64, v66\n"
    "v_permlane32_swap_b32 v65, v67\n"
    "v_perm_b32 v238, v45, v41, s36\n"
    "buffer_store_dwordx4 v[64:67], v4, s[4:7], s40 offen sc1\n"
    "v_perm_b32 v239, v53, v49, s36\n"
    "v_xor_b32 v116, 64, v56\n"
    "v_perm_b32 v242, v46, v42, s35\n"
    "v_mfma_f32_16x16x32_bf16 v[36:39], v[20:23], v[236:239], v[36:39]\n"
    "ds_write_b128 v116, v[32:35] offset:2048\n"
    "v_perm_b32 v246, v46, v42, s36\n"
    "v_perm_b32 v219, v255, v251, s35\n"
    "v_perm_b32 v250, v47, v43, s35\n"
    "v_perm_b32 v243, v54, v50, s35\n"
    "v_perm_b32 v223, v255, v251, s36\n"
    "v_perm_b32 v254, v47, v43, s36\n"
    "v_mfma_f32_16x16x32_bf16 v[40:43], v[8:11], v[144:147], 0\n"
    "ds_write_b128 v116, v[36:39] offset:3072\n"
    "v_mfma_f32_16x16x32_bf16 v[40:43], v[12:15], v[176:179], v[40:43]\n"
    "v_xor_b32 v117, 128, v56\n"
    "v_perm_b32 v247, v54, v50, s36\n"
    "v_mfma_f32_16x16x32_bf16 v[40:43], v[16:19], v[208:211], v[40:43]\n"
    "v_perm_b32 v251, v55, v51, s35\n"
    "v_xor_b32 v118, 192, v56\n"
    "v_mfma_f32_16x16x32_bf16 v[40:43], v[20:23], v[240:243], v[40:43]\n"
    "s_nop 0\n"
    "v_perm_b32 v255, v55, v51, s36\n"
    "s_xor_b32 s48, s46, 2048\n"
    "v_mfma_f32_16x16x32_bf16 v[44:47], v[8:11], v[148:151], 0\n"
    "s_waitcnt vmcnt(1)\n"
    "v_add_u32 v61, s59, v61\n"
    "v_xor_b32 v121, 1040, v61\n"
    "v_mfma_f32_16x16x32_bf16 v[44:47], v[12:15], v[180:183], v[44:47]\n"
    "ds_write_b128 v117, v[40:43] offset:4096\n"
    "v_mfma_f32_16x16x32_bf16 v[44:47], v[16:19], v[212:215], v[44:47]\n"
    "ds_read_b32 v120, v61\n"
    "v_xor_b32 v122, 2080, v61\n"
    "v_mfma_f32_16x16x32_bf16 v[44:47], v[20:23], v[244:247], v[44:47]\n"
    "ds_read_b32 v121, v121\n"
    "v_xor_b32 v123, 3120, v61\n"
    "v_mfma_f32_16x16x32_bf16 v[48:51], v[8:11], v[152:155], 0\n"
    "ds_read_b32 v122, v122\n"
    "v_lshlrev_b32 v108, 3, v0\n"
    "v_mfma_f32_16x16x32_bf16 v[48:51], v[12:15], v[184:187], v[48:51]\n"
    "ds_read_b32 v123, v123\n"
    "s_xor_b32 s49, s46, 3072\n"
    "v_mfma_f32_16x16x32_bf16 v[48:51], v[16:19], v[216:219], v[48:51]\n"
    "ds_write_b128 v117, v[44:47] offset:5120\n"
    "v_mfma_f32_16x16x32_bf16 v[48:51], v[20:23], v[248:251], v[48:51]\n"
    "buffer_load_dwordx4 v[80:83], v0, s[96:99], s77 offen\n"
    "s_add_u32 s92, s85, 0xa01000\n"
    "v_mfma_f32_16x16x32_bf16 v[52:55], v[8:11], v[156:159], 0\n"
    "buffer_load_dwordx4 v[68:71], v0, s[96:99], s89 offen\n"
    "s_mov_b32 s35, 0x0000bf00\n"
    "v_mfma_f32_16x16x32_bf16 v[52:55], v[12:15], v[188:191], v[52:55]\n"
    "s_waitcnt lgkmcnt(4)\n"
    "v_lshlrev_b32 v88, 16, v120\n"
    "v_and_b32 v92, 0xffff0000, v120\n"
    "v_mfma_f32_16x16x32_bf16 v[52:55], v[16:19], v[220:223], v[52:55]\n"
    "ds_write_b128 v118, v[48:51] offset:6144\n"
    "v_mfma_f32_16x16x32_bf16 v[52:55], v[20:23], v[252:255], v[52:55]\n"
    "s_waitcnt lgkmcnt(2)\n"
    "v_lshlrev_b32 v89, 16, v121\n"
    "v_lshlrev_b32 v90, 16, v122\n"
    "v_lshlrev_b32 v91, 16, v123\n"
    "v_and_b32 v93, 0xffff0000, v121\n"
    "v_and_b32 v94, 0xffff0000, v122\n"
    "v_and_b32 v95, 0xffff0000, v123\n"
    "s_mov_b32 s36, 0x3827c5ac\n"
    "ds_write_b128 v118, v[52:55] offset:7168\n"
    "buffer_load_dwordx4 v[64:67], v0, s[96:99], s88 offen\n"
    "s_add_u32 s38, s87, 0x900000\n"
    "s_add_u32 s39, s83, 0x200000\n"
    "buffer_load_dwordx4 v[72:75], v0, s[96:99], s90 offen\n"
    "s_add_u32 s40, s84, 0x600000\n"
    "s_add_u32 s41, s87, 0x800000\n"
    "s_add_u32 s42, s87, 0x900000\n"
    "s_mov_b32 s77, 36864\n"
    "s_add_u32 s88, s84, 0x601000\n"
    "s_add_u32 s89, s87, 0xa32000\n"
    "s_add_u32 s90, s83, 0x300000\n"
    "buffer_load_dword v109, v108, s[4:7], s37 offen sc1\n"
    "s_waitcnt lgkmcnt(0)\n"
    "s_barrier\n"
    "ds_read_b128 v[24:27], v57\n"
    "ds_read_b128 v[28:31], v57 offset:1024\n"
    "ds_read_b128 v[32:35], v57 offset:8192\n"
    "ds_read_b128 v[36:39], v57 offset:9216\n"
    "ds_read_b128 v[40:43], v57 offset:49152\n"
    "ds_read_b128 v[44:47], v57 offset:50176\n"
    "ds_read_b128 v[48:51], v57 offset:57344\n"
    "ds_read_b128 v[52:55], v57 offset:58368\n"
    "s_waitcnt lgkmcnt(7)\n"
    "v_pk_add_f32 v[88:89], v[88:89], v[24:25]\n"
    "v_pk_add_f32 v[90:91], v[90:91], v[26:27]\n"
    "s_waitcnt lgkmcnt(6)\n"
    "v_pk_add_f32 v[92:93], v[92:93], v[28:29]\n"
    "v_pk_add_f32 v[94:95], v[94:95], v[30:31]\n"
    "s_waitcnt lgkmcnt(5)\n"
    "v_pk_add_f32 v[88:89], v[88:89], v[32:33]\n"
    "v_pk_add_f32 v[90:91], v[90:91], v[34:35]\n"
    "s_waitcnt lgkmcnt(4)\n"
    "v_pk_add_f32 v[92:93], v[92:93], v[36:37]\n"
    "v_pk_add_f32 v[94:95], v[94:95], v[38:39]\n"
    "s_waitcnt lgkmcnt(3)\n"
    "v_pk_add_f32 v[88:89], v[88:89], v[40:41]\n"
    "v_pk_add_f32 v[90:91], v[90:91], v[42:43]\n"
    "s_waitcnt lgkmcnt(2)\n"
    "v_pk_add_f32 v[92:93], v[92:93], v[44:45]\n"
    "v_pk_add_f32 v[94:95], v[94:95], v[46:47]\n"
    "s_waitcnt lgkmcnt(1)\n"
    "v_pk_add_f32 v[88:89], v[88:89], v[48:49]\n"
    "v_pk_add_f32 v[90:91], v[90:91], v[50:51]\n"
    "v_pk_mul_f32 v[36:37], v[88:89], v[88:89]\n"
    "v_pk_mul_f32 v[38:39], v[90:91], v[90:91]\n"
    "s_waitcnt lgkmcnt(0)\n"
    "v_pk_add_f32 v[92:93], v[92:93], v[52:53]\n"
    "v_pk_add_f32 v[94:95], v[94:95], v[54:55]\n"
    "v_pk_add_f32 v[32:33], v[88:89], v[92:93]\n"
    "v_pk_fma_f32 v[36:37], v[92:93], v[92:93], v[36:37]\n"
    "v_pk_add_f32 v[34:35], v[90:91], v[94:95]\n"
    "v_pk_fma_f32 v[38:39], v[94:95], v[94:95], v[38:39]\n"
    "v_cvt_pk_bf16_f32 v120, v88, v92\n"
    "v_permlane32_swap_b32 v32, v34\n"
    "v_permlane32_swap_b32 v33, v35\n"
    "v_permlane32_swap_b32 v36, v38\n"
    "v_permlane32_swap_b32 v37, v39\n"
    "v_pk_add_f32 v[32:33], v[32:33], v[34:35]\n"
    "v_pk_add_f32 v[36:37], v[36:37], v[38:39]\n"
    "v_cvt_pk_bf16_f32 v121, v89, v93\n"
    "v_permlane16_swap_b32 v32, v33\n"
    "v_permlane16_swap_b32 v36, v37\n"
    "v_add_f32 v32, v32, v33\n"
    "v_add_f32 v36, v36, v37\n"
    "v_cvt_pk_bf16_f32 v122, v90, v94\n"
    "v_add_f32_dpp v32, v32, v32 row_ror:8 row_mask:0xf bank_mask:0xf\n"
    "v_add_f32_dpp v36, v36, v36 row_ror:8 row_mask:0xf bank_mask:0xf\n"
    "v_cvt_pk_bf16_f32 v123, v91, v95\n"
    "v_add_f32_dpp v34, v32, v32 row_ror:4 row_mask:0xf bank_mask:0xf\n"
    "v_add_f32_dpp v35, v36, v36 row_ror:4 row_mask:0xf bank_mask:0xf\n"
    "ds_write_b64 v58, v[34:35]\n"
    "v_min_u32 v120, -2, v120\n"
    "v_min_u32 v121, -2, v121\n"
    "v_min_u32 v122, -2, v122\n"
    "v_min_u32 v123, -2, v123\n"
    "buffer_store_dwordx4 v[120:123], v62, s[4:7], s43 offen sc1\n"
    "s_mov_b32 s43, 4096\n"
    "s_nop 0\n"
    "v_xor_b32 v121, 1040, v61\n"
    "v_xor_b32 v122, 2080, v61\n"
    "v_xor_b32 v123, 3120, v61\n"
    "ds_read_b32 v120, v61 offset:49152\n"
    "s_waitcnt lgkmcnt(1)\n"
    "s_barrier\n"
    "ds_read_b128 v[24:27], v59\n"
    "ds_read_b128 v[28:31], v59 offset:16\n"
    "buffer_load_dwordx4 v[124:127], v0, s[96:99], s45 offen\n"
    "ds_read_b32 v121, v121 offset:49152\n"
    "s_add_u32 s45, s83, 0x300000\n"
    "buffer_load_dwordx4 v[112:115], v0, s[96:99], s76 offen\n"
    "ds_read_b32 v122, v122 offset:49152\n"
    "s_add_u32 s76, s83, 0x200000\n"
    "s_waitcnt vmcnt(7)\n"
    "ds_write_b32 v82, v83\n"
    "ds_write_b32 v80, v83\n"
    "ds_write_b32 v81, v83\n"
    "s_waitcnt lgkmcnt(6)\n"
    "v_pk_add_f32 v[24:25], v[24:25], v[26:27]\n"
    "s_waitcnt lgkmcnt(5)\n"
    "v_pk_add_f32 v[28:29], v[28:29], v[30:31]\n"
    "s_nop 0\n"
    "v_pk_add_f32 v[24:25], v[24:25], v[28:29]\n"
    "s_nop 0\n"
    "v_mul_f32 v24, 0x3c000000, v24\n"
    "v_mul_f32 v25, 0x3c000000, v25\n"
    "v_fma_f32 v25, -v24, v24, v25\n"
    "v_max_f32 v25, 0, v25\n"
    "v_sqrt_f32 v25, v25\n"
    "s_nop 0\n"
    "v_cvt_pk_bf16_f32 v24, v24, v25\n"
    "v_min_u32 v24, -2, v24\n"
    "s_nop 1\n"
    "v_mov_b32_dpp v25, v24 row_ror:12 row_mask:0xf bank_mask:0xf\n"
    "v_mov_b32_dpp v26, v24 row_ror:8 row_mask:0xf bank_mask:0xf\n"
    "v_mov_b32_dpp v27, v24 row_ror:4 row_mask:0xf bank_mask:0xf\n"
    "buffer_store_dwordx4 v[24:27], v60, s[4:7], s44 offen sc1\n"
    "buffer_load_dwordx4 v[8:11], v1, s[4:7], s46 offen sc1\n"
    "s_waitcnt vmcnt(8)\n"
    "ds_read_u16 v89, v69\n"
    "s_mov_b32 s44, 8192\n"
    "buffer_load_dwordx4 v[12:15], v1, s[4:7], s47 offen sc1\n"
    "ds_read_u16 v90, v70\n"
    "ds_read_u16 v91, v71\n"
    "ds_read_u16 v88, v68\n"
    "s_waitcnt vmcnt(8)\n"
    "ds_read_u16 v87, v67\n"
    "s_waitcnt vmcnt(7)\n"
    "ds_read_u16 v92, v72\n"
    "ds_read_u16 v93, v73\n"
    "buffer_load_dwordx4 v[16:19], v1, s[4:7], s48 offen sc1\n"
    "ds_read_u16 v94, v74\n"
    "ds_read_u16 v86, v66\n"
    "ds_read_u16 v85, v65\n"
    "ds_read_u16 v84, v64\n"
    "ds_read_u16 v99, v67 offset:1936\n"
    "ds_read_u16 v100, v68 offset:1936\n"
    "ds_read_u16 v101, v69 offset:1936\n"
    "buffer_load_dwordx4 v[20:23], v1, s[4:7], s49 offen sc1\n"
    "ds_read_u16 v102, v70 offset:1936\n"
    "ds_read_u16 v103, v71 offset:1936\n"
    "ds_read_u16 v104, v72 offset:1936\n"
    "ds_read_u16 v105, v73 offset:1936\n"
    "ds_read_u16 v106, v74 offset:1936\n"
    "ds_read_u16 v98, v66 offset:1936\n"
    "s_waitcnt lgkmcnt(12)\n"
    "v_lshl_or_b32 v41, v90, 16, v89\n"
    "v_lshl_or_b32 v46, v91, 16, v90\n"
    "v_lshl_or_b32 v50, v90, 16, v89\n"
    "v_lshl_or_b32 v55, v91, 16, v90\n"
    "ds_read_u16 v97, v65 offset:1936\n"
    "ds_read_u16 v96, v64 offset:1936\n"
    "v_lshl_or_b32 v40, v88, 16, v87\n"
    "v_lshl_or_b32 v42, v92, 16, v91\n"
    "v_lshl_or_b32 v43, v94, 16, v93\n"
    "ds_write_b128 v75, v[40:43]\n"
    "v_lshl_or_b32 v45, v89, 16, v88\n"
    "v_lshl_or_b32 v47, v93, 16, v92\n"
    "s_waitcnt lgkmcnt(5)\n"
    "v_lshl_or_b32 v44, v87, 16, v86\n"
    "ds_write_b128 v75, v[44:47] offset:4160\n"
    "v_lshl_or_b32 v48, v86, 16, v85\n"
    "v_lshl_or_b32 v49, v88, 16, v87\n"
    "v_lshl_or_b32 v51, v92, 16, v91\n"
    "ds_write_b128 v75, v[48:51] offset:8320\n"
    "v_lshl_or_b32 v52, v85, 16, v84\n"
    "v_lshl_or_b32 v53, v87, 16, v86\n"
    "v_lshl_or_b32 v54, v89, 16, v88\n"
    "v_lshl_or_b32 v40, v100, 16, v99\n"
    "ds_write_b128 v75, v[52:55] offset:12480\n"
    "v_lshl_or_b32 v41, v102, 16, v101\n"
    "v_lshl_or_b32 v42, v104, 16, v103\n"
    "s_waitcnt lgkmcnt(3)\n"
    "v_lshl_or_b32 v43, v106, 16, v105\n"
    "v_lshl_or_b32 v44, v99, 16, v98\n"
    "ds_write_b128 v75, v[40:43] offset:16640\n"
    "v_lshl_or_b32 v45, v101, 16, v100\n"
    "v_lshl_or_b32 v46, v103, 16, v102\n"
    "v_lshl_or_b32 v47, v105, 16, v104\n"
    "v_lshl_or_b32 v48, v98, 16, v97\n"
    "v_lshl_or_b32 v49, v100, 16, v99\n"
    "ds_write_b128 v75, v[44:47] offset:20800\n"
    "v_lshl_or_b32 v50, v102, 16, v101\n"
    "v_lshl_or_b32 v51, v104, 16, v103\n"
    "v_lshl_or_b32 v52, v97, 16, v96\n"
    "v_lshl_or_b32 v53, v99, 16, v98\n"
    "v_lshl_or_b32 v54, v101, 16, v100\n"
    "ds_write_b128 v75, v[48:51] offset:24960\n"
    "v_lshl_or_b32 v55, v103, 16, v102\n"
    "v_lshlrev_b32 v88, 16, v120\n"
    "v_lshlrev_b32 v89, 16, v121\n"
    "v_and_b32 v92, 0xffff0000, v120\n"
    "v_and_b32 v93, 0xffff0000, v121\n"
    "ds_write_b128 v75, v[52:55] offset:29120\n"
    "v_and_b32 v94, 0xffff0000, v122\n"
    "ds_read_b32 v123, v123 offset:49152\n"
    "s_waitcnt lgkmcnt(1)\n"
    "s_barrier\n"
    "s_waitcnt vmcnt(6)\n"
    "ds_read_b64 a[204:205], v124 offset:3584\n"
    "s_waitcnt lgkmcnt(1)\n"
    "v_lshlrev_b32 v91, 16, v123\n"
    "v_and_b32 v95, 0xffff0000, v123\n"
    "ds_read_b64 a[206:207], v124 offset:3592\n"
    "ds_read_b64 a[220:221], v124 offset:3072\n"
    "ds_read_b64 a[222:223], v124 offset:3080\n"
    "ds_read_b64 a[236:237], v124 offset:2560\n"
    "ds_read_b64 a[238:239], v124 offset:2568\n"
    "ds_read_b64 a[252:253], v124 offset:2048\n"
    "ds_read_b64 a[254:255], v124 offset:2056\n"
    "ds_read_b64 a[200:201], v124 offset:3712\n"
    "ds_read_b64 a[202:203], v124 offset:3720\n"
    "ds_read_b64 a[216:217], v124 offset:3200\n"
    "ds_read_b64 a[218:219], v124 offset:3208\n"
    "ds_read_b64 a[232:233], v124 offset:2688\n"
    "ds_read_b64 a[234:235], v124 offset:2696\n"
    "ds_read_b64 a[248:249], v124 offset:2176\n"
    "s_waitcnt vmcnt(3)\n"
    "v_max3_u32 v90, v8, v9, v10\n"
    "ds_read_b64 a[250:251], v124 offset:2184\n"
    "ds_read_b64 a[196:197], v124 offset:3840\n"
    "s_waitcnt vmcnt(2)\n"
    "v_max3_u32 v90, v90, v11, v12\n"
    "v_max3_u32 v90, v90, v13, v14\n"
    "ds_read_b64 a[198:199], v124 offset:3848\n"
    "ds_read_b64 a[212:213], v124 offset:3328\n"
    "ds_read_b64 a[214:215], v124 offset:3336\n"
    "ds_read_b64 a[228:229], v124 offset:2816\n"
    "ds_read_b64 a[230:231], v124 offset:2824\n"
    "ds_read_b64 a[244:245], v124 offset:2304\n"
    "s_waitcnt vmcnt(1)\n"
    "v_max3_u32 v90, v90, v15, v16\n"
    "v_max3_u32 v90, v90, v17, v18\n"
    "ds_read_b64 a[246:247], v124 offset:2312\n"
    "ds_read_b64 a[192:193], v124 offset:3968\n"
    "ds_read_b64 a[194:195], v124 offset:3976\n"
    "ds_read_b64 a[208:209], v124 offset:3456\n"
    "ds_read_b64 a[210:211], v124 offset:3464\n"
    "ds_read_b64 a[224:225], v124 offset:2944\n"
    "s_waitcnt vmcnt(0)\n"
    "v_max3_u32 v90, v90, v19, v20\n"
    "v_max3_u32 v90, v90, v21, v22\n"
    "v_max_u32 v90, v90, v23\n"
    "v_cmp_eq_u32 vcc, -1, v90\n"
    "ds_read_b64 a[226:227], v124 offset:2952\n"
    "ds_read_b64 a[240:241], v124 offset:2432\n"
    "s_cbranch_vccnz .R_3\n"
    ".R_3c:\n"
    "v_mfma_f32_16x16x32_bf16 v[24:27], v[8:11], v[128:131], 0\n"
    "v_cmp_ne_u32 vcc, s56, v109\n"
    "v_lshlrev_b32 v90, 16, v122\n"
    "v_mfma_f32_16x16x32_bf16 v[24:27], v[12:15], v[160:163], v[24:27]\n"
    "ds_read_b64 a[242:243], v124 offset:2440\n"
    "s_add_u32 s46, s82, 0x200000\n"
    "v_mfma_f32_16x16x32_bf16 v[24:27], v[16:19], v[192:195], v[24:27]\n"
    "s_cbranch_vccnz .R_4\n"
    ".R_4c:\n"
    "v_mfma_f32_16x16x32_bf16 v[24:27], v[20:23], v[224:227], v[24:27]\n"
    "s_xor_b32 s47, s46, 1024\n"
    "s_xor_b32 s48, s46, 2048\n"
    "s_xor_b32 s49, s46, 3072\n"
    "v_mfma_f32_16x16x32_bf16 v[28:31], v[8:11], v[132:135], 0\n"
    "s_lshl_b32 s37, s50, 2\n"
    "s_nop 1\n"
    "v_mfma_f32_16x16x32_bf16 v[28:31], v[12:15], v[164:167], v[28:31]\n"
    "ds_write_b128 v56, v[24:27]\n"
    "v_mfma_f32_16x16x32_bf16 v[28:31], v[16:19], v[196:199], v[28:31]\n"
    "v_mfma_f32_16x16x32_bf16 v[28:31], v[20:23], v[228:231], v[28:31]\n"
    "s_nop 2\n"
    "v_mfma_f32_16x16x32_bf16 v[32:35], v[8:11], v[136:139], 0\n"
    "s_nop 2\n"
    "v_mfma_f32_16x16x32_bf16 v[32:35], v[12:15], v[168:171], v[32:35]\n"
    "ds_write_b128 v56, v[28:31] offset:1024\n"
    "v_mfma_f32_16x16x32_bf16 v[32:35], v[16:19], v[200:203], v[32:35]\n"
    "v_mov_b32 v56, -1\n"
    "v_mfma_f32_16x16x32_bf16 v[32:35], v[20:23], v[232:235], v[32:35]\n"
    "s_nop 2\n"
    "v_mfma_f32_16x16x32_bf16 v[36:39], v[8:11], v[140:143], 0\n"
    "s_nop 2\n"
    "v_mfma_f32_16x16x32_bf16 v[36:39], v[12:15], v[172:175], v[36:39]\n"
    "ds_write_b128 v116, v[32:35] offset:2048\n"
    "v_mfma_f32_16x16x32_bf16 v[36:39], v[16:19], v[204:207], v[36:39]\n"
    "v_mfma_f32_16x16x32_bf16 v[36:39], v[20:23], v[236:239], v[36:39]\n"
    "s_nop 2\n"
    "v_mfma_f32_16x16x32_bf16 v[40:43], v[8:11], v[144:147], 0\n"
    "s_nop 2\n"
    "v_mfma_f32_16x16x32_bf16 v[40:43], v[12:15], v[176:179], v[40:43]\n"
    "ds_write_b128 v116, v[36:39] offset:3072\n"
    "v_mfma_f32_16x16x32_bf16 v[40:43], v[16:19], v[208:211], v[40:43]\n"
    "v_mfma_f32_16x16x32_bf16 v[40:43], v[20:23], v[240:243], v[40:43]\n"
    "s_nop 2\n"
    "v_mfma_f32_16x16x32_bf16 v[44:47], v[8:11], v[148:151], 0\n"
    "s_nop 2\n"
    "buffer_load_dwordx4 v[64:67], v0, s[4:7], s28 offen sc1\n"
    "v_mfma_f32_16x16x32_bf16 v[44:47], v[12:15], v[180:183], v[44:47]\n"
    "ds_write_b128 v117, v[40:43] offset:4096\n"
    "v_mfma_f32_16x16x32_bf16 v[44:47], v[16:19], v[212:215], v[44:47]\n"
    "v_mfma_f32_16x16x32_bf16 v[44:47], v[20:23], v[244:247], v[44:47]\n"
    "s_nop 2\n"
    "v_mfma_f32_16x16x32_bf16 v[48:51], v[8:11], v[152:155], 0\n"
    "s_nop 2\n"
    "v_mfma_f32_16x16x32_bf16 v[48:51], v[12:15], v[184:187], v[48:51]\n"
    "ds_write_b128 v117, v[44:47] offset:5120\n"
    "v_mfma_f32_16x16x32_bf16 v[48:51], v[16:19], v[216:219], v[48:51]\n"
    "v_mfma_f32_16x16x32_bf16 v[48:51], v[20:23], v[248:251], v[48:51]\n"
    "s_nop 2\n"
    "v_mfma_f32_16x16x32_bf16 v[52:55], v[8:11], v[156:159], 0\n"
    "s_nop 2\n"
    "v_mfma_f32_16x16x32_bf16 v[52:55], v[12:15], v[188:191], v[52:55]\n"
    "ds_write_b128 v118, v[48:51] offset:6144\n"
    "v_mfma_f32_16x16x32_bf16 v[52:55], v[16:19], v[220:223], v[52:55]\n"
    "ds_read_b64 v[16:17], v124 offset:512\n"
    "v_mfma_f32_16x16x32_bf16 v[52:55], v[20:23], v[252:255], v[52:55]\n"
    "ds_read_b64 v[18:19], v124 offset:520\n"
    "ds_read_b64 v[20:21], v124\n"
    "ds_read_b64 v[22:23], v124 offset:8\n"
    "s_nop 4\n"
    "ds_write_b128 v118, v[52:55] offset:7168\n"
    "buffer_load_dwordx4 v[68:71], v0, s[4:7], s29 offen sc1\n"
    "buffer_load_dwordx4 v[72:75], v0, s[4:7], s30 offen sc1\n"
    "buffer_load_dwordx4 v[76:79], v0, s[4:7], s31 offen sc1\n"
    "s_waitcnt lgkmcnt(0)\n"
    "s_barrier\n"
    "ds_read_b128 v[24:27], v57\n"
    "ds_read_b128 v[28:31], v57 offset:1024\n"
    "ds_read_b128 v[32:35], v57 offset:8192\n"
    "buffer_load_dwordx4 v[80:83], v0, s[4:7], s34 offen sc1\n"
    "ds_read_b128 v[36:39], v57 offset:9216\n"
    "ds_read_b128 v[40:43], v57 offset:49152\n"
    "ds_read_b128 v[44:47], v57 offset:50176\n"
    "ds_read_b128 v[48:51], v57 offset:57344\n"
    "ds_read_b128 v[52:55], v57 offset:58368\n"
    "v_mov_b32 v57, -1\n"
    "s_waitcnt lgkmcnt(7)\n"
    "v_pk_add_f32 v[88:89], v[88:89], v[24:25]\n"
    "v_pk_add_f32 v[90:91], v[90:91], v[26:27]\n"
    "s_waitcnt lgkmcnt(6)\n"
    "v_pk_add_f32 v[92:93], v[92:93], v[28:29]\n"
    "v_pk_add_f32 v[94:95], v[94:95], v[30:31]\n"
    "s_waitcnt lgkmcnt(5)\n"
    "v_pk_add_f32 v[88:89], v[88:89], v[32:33]\n"
    "v_pk_add_f32 v[90:91], v[90:91], v[34:35]\n"
    "s_waitcnt lgkmcnt(4)\n"
    "v_pk_add_f32 v[92:93], v[92:93], v[36:37]\n"
    "v_pk_add_f32 v[94:95], v[94:95], v[38:39]\n"
    "s_waitcnt lgkmcnt(3)\n"
    "v_pk_add_f32 v[88:89], v[88:89], v[40:41]\n"
    "v_pk_add_f32 v[90:91], v[90:91], v[42:43]\n"
    "s_waitcnt lgkmcnt(2)\n"
    "v_pk_add_f32 v[92:93], v[92:93], v[44:45]\n"
    "v_pk_add_f32 v[94:95], v[94:95], v[46:47]\n"
    "s_waitcnt lgkmcnt(1)\n"
    "v_pk_add_f32 v[88:89], v[88:89], v[48:49]\n"
    "v_pk_add_f32 v[90:91], v[90:91], v[50:51]\n"
    "v_pk_mul_f32 v[36:37], v[88:89], v[88:89]\n"
    "v_pk_mul_f32 v[38:39], v[90:91], v[90:91]\n"
    "s_waitcnt lgkmcnt(0)\n"
    "v_pk_add_f32 v[92:93], v[92:93], v[52:53]\n"
    "v_pk_add_f32 v[94:95], v[94:95], v[54:55]\n"
    "v_pk_add_f32 v[32:33], v[88:89], v[92:93]\n"
    "v_pk_fma_f32 v[36:37], v[92:93], v[92:93], v[36:37]\n"
    "v_pk_add_f32 v[34:35], v[90:91], v[94:95]\n"
    "v_pk_fma_f32 v[38:39], v[94:95], v[94:95], v[38:39]\n"
    "v_cvt_pk_bf16_f32 v120, v88, v92\n"
    "v_permlane32_swap_b32 v32, v34\n"
    "v_permlane32_swap_b32 v33, v35\n"
    "v_permlane32_swap_b32 v36, v38\n"
    "v_permlane32_swap_b32 v37, v39\n"
    "v_pk_add_f32 v[32:33], v[32:33], v[34:35]\n"
    "v_pk_add_f32 v[36:37], v[36:37], v[38:39]\n"
    "v_cvt_pk_bf16_f32 v121, v89, v93\n"
    "v_permlane16_swap_b32 v32, v33\n"
    "v_permlane16_swap_b32 v36, v37\n"
    "v_add_f32 v32, v32, v33\n"
    "v_add_f32 v36, v36, v37\n"
    "v_cvt_pk_bf16_f32 v122, v90, v94\n"
    "v_add_f32_dpp v32, v32, v32 row_ror:8 row_mask:0xf bank_mask:0xf\n"
    "v_add_f32_dpp v36, v36, v36 row_ror:8 row_mask:0xf bank_mask:0xf\n"
    "v_cvt_pk_bf16_f32 v123, v91, v95\n"
    "v_add_f32_dpp v34, v32, v32 row_ror:4 row_mask:0xf bank_mask:0xf\n"
    "v_add_f32_dpp v35, v36, v36 row_ror:4 row_mask:0xf bank_mask:0xf\n"
    "ds_write_b64 v58, v[34:35]\n"
    "v_min_u32 v120, -2, v120\n"
    "v_min_u32 v121, -2, v121\n"
    "v_min_u32 v122, -2, v122\n"
    "v_min_u32 v123, -2, v123\n"
    "buffer_store_dwordx4 v[120:123], v62, s[4:7], s91 offen sc1\n"
    "v_mov_b32 v58, -1\n"
    "s_mov_b32 s91, 20480\n"
    "s_waitcnt lgkmcnt(0)\n"
    "s_barrier\n"
    "ds_read_b128 v[24:27], v59\n"
    "ds_read_b128 v[28:31], v59 offset:16\n"
    "s_waitcnt vmcnt(5)\n"
    "v_max3_u32 v108, v64, v65, v66\n"
    "v_mov_b32 v59, -1\n"
    "s_waitcnt lgkmcnt(1)\n"
    "v_pk_add_f32 v[24:25], v[24:25], v[26:27]\n"
    "s_waitcnt lgkmcnt(0)\n"
    "v_pk_add_f32 v[28:29], v[28:29], v[30:31]\n"
    "s_nop 0\n"
    "v_pk_add_f32 v[24:25], v[24:25], v[28:29]\n"
    "s_nop 0\n"
    "v_mul_f32 v24, 0x3c000000, v24\n"
    "v_mul_f32 v25, 0x3c000000, v25\n"
    "v_fma_f32 v25, -v24, v24, v25\n"
    "v_max_f32 v25, 0, v25\n"
    "v_sqrt_f32 v25, v25\n"
    "s_nop 0\n"
    "v_cvt_pk_bf16_f32 v24, v24, v25\n"
    "v_min_u32 v24, -2, v24\n"
    "s_nop 1\n"
    "v_mov_b32_dpp v25, v24 row_ror:12 row_mask:0xf bank_mask:0xf\n"
    "v_mov_b32_dpp v26, v24 row_ror:8 row_mask:0xf bank_mask:0xf\n"
    "v_mov_b32_dpp v27, v24 row_ror:4 row_mask:0xf bank_mask:0xf\n"
    "buffer_store_dwordx4 v[24:27], v60, s[4:7], s92 offen sc1\n"
    "s_mov_b32 s92, 24576\n"
    "s_waitcnt vmcnt(5)\n"
    "v_max3_u32 v108, v108, v67, v68\n"
    "v_max3_u32 v108, v108, v69, v70\n"
    "v_dot2_f32_bf16 v100, v68, s35, 0\n"
    "v_dot2_f32_bf16 v104, v68, v68, s36\n"
    "v_dot2_f32_bf16 v101, v69, s35, 0\n"
    "v_dot2_f32_bf16 v105, v69, v69, s36\n"
    "v_dot2_f32_bf16 v102, v70, s35, 0\n"
    "v_dot2_f32_bf16 v106, v70, v70, s36\n"
    "v_dot2_f32_bf16 v103, v71, s35, 0\n"
    "v_dot2_f32_bf16 v107, v71, v71, s36\n"
    "s_waitcnt vmcnt(4)\n"
    "v_max3_u32 v108, v108, v71, v72\n"
    "v_max3_u32 v108, v108, v73, v74\n"
    "v_dot2_f32_bf16 v100, v72, s35, v100\n"
    "v_dot2_f32_bf16 v104, v72, v72, v104\n"
    "v_dot2_f32_bf16 v101, v73, s35, v101\n"
    "v_dot2_f32_bf16 v105, v73, v73, v105\n"
    "v_dot2_f32_bf16 v102, v74, s35, v102\n"
    "v_dot2_f32_bf16 v106, v74, v74, v106\n"
    "v_dot2_f32_bf16 v103, v75, s35, v103\n"
    "v_dot2_f32_bf16 v107, v75, v75, v107\n"
    "s_waitcnt vmcnt(3)\n"
    "v_max3_u32 v108, v108, v75, v76\n"
    "v_max3_u32 v108, v108, v77, v78\n"
    "v_dot2_f32_bf16 v100, v76, s35, v100\n"
    "v_dot2_f32_bf16 v104, v76, v76, v104\n"
    "v_dot2_f32_bf16 v101, v77, s35, v101\n"
    "v_dot2_f32_bf16 v105, v77, v77, v105\n"
    "v_dot2_f32_bf16 v102, v78, s35, v102\n"
    "v_dot2_f32_bf16 v106, v78, v78, v106\n"
    "v_dot2_f32_bf16 v103, v79, s35, v103\n"
    "v_dot2_f32_bf16 v107, v79, v79, v107\n"
    "s_waitcnt vmcnt(2)\n"
    "v_max3_u32 v108, v108, v79, v80\n"
    "v_max3_u32 v108, v108, v81, v82\n"
    "v_max_u32 v108, v108, v83\n"
    "v_cmp_eq_u32 vcc, -1, v108\n"
    "v_dot2_f32_bf16 v100, v80, s35, v100\n"
    "v_dot2_f32_bf16 v104, v80, v80, v104\n"
    "v_dot2_f32_bf16 v101, v81, s35, v101\n"
    "v_dot2_f32_bf16 v105, v81, v81, v105\n"
    "v_dot2_f32_bf16 v102, v82, s35, v102\n"
    "v_dot2_f32_bf16 v106, v82, v82, v106\n"
    "v_dot2_f32_bf16 v103, v83, s35, v103\n"
    "v_dot2_f32_bf16 v107, v83, v83, v107\n"
    "s_nop 2\n"
    "s_cbranch_vccnz .R_5\n"
    ".R_5c:\n"
    "buffer_store_dwordx4 v[56:59], v125, s[4:7], s38 offen sc1\n"
    "v_pk_fma_f32 v[104:105], v[100:101], v[100:101], v[104:105] neg_lo:[1,0,0] neg_hi:[1,0,0]\n"
    "v_pk_fma_f32 v[106:107], v[102:103], v[102:103], v[106:107] neg_lo:[1,0,0] neg_hi:[1,0,0]\n"
    "v_max_f32 v104, s36, v104\n"
    "v_max_f32 v105, s36, v105\n"
    "v_max_f32 v106, s36, v106\n"
    "v_max_f32 v107, s36, v107\n"
    "v_lshlrev_b32 v116, 16, v64\n"
    "v_lshlrev_b32 v117, 16, v65\n"
    "v_lshlrev_b32 v118, 16, v66\n"
    "v_lshlrev_b32 v119, 16, v67\n"
    "v_rsq_f32 v104, v104\n"
    "v_rsq_f32 v105, v105\n"
    "v_rsq_f32 v106, v106\n"
    "v_rsq_f32 v107, v107\n"
    "v_pk_fma_f32 v[8:9], v[116:117], 2.0, v[100:101] op_sel_hi:[1,0,1]\n"
    "v_pk_fma_f32 v[10:11], v[118:119], 2.0, v[102:103] op_sel_hi:[1,0,1]\n"
    "v_and_b32 v120, 0xffff0000, v64\n"
    "v_and_b32 v121, 0xffff0000, v65\n"
    "v_pk_mul_f32 v[8:9], v[8:9], v[104:105]\n"
    "v_pk_mul_f32 v[10:11], v[10:11], v[106:107]\n"
    "v_pk_fma_f32 v[12:13], v[120:121], 2.0, v[100:101] op_sel_hi:[1,0,1]\n"
    "v_and_b32 v122, 0xffff0000, v66\n"
    "v_and_b32 v123, 0xffff0000, v67\n"
    "v_cvt_pk_bf16_f32 v100, v8, v9\n"
    "v_cvt_pk_bf16_f32 v101, v10, v11\n"
    "v_pk_fma_f32 v[14:15], v[122:123], 2.0, v[102:103] op_sel_hi:[1,0,1]\n"
    "s_add_u32 s28, s86, 0x401000\n"
    "ds_write_b64 v112, v[100:101]\n"
    "v_pk_mul_f32 v[12:13], v[12:13], v[104:105]\n"
    "v_pk_mul_f32 v[14:15], v[14:15], v[106:107]\n"
    "v_cvt_pk_bf16_f32 v102, v12, v13\n"
    "v_cvt_pk_bf16_f32 v103, v14, v15\n"
    "s_mov_b32 s29, 0xa01000\n"
    "ds_write_b64 v112, v[102:103] offset:4608\n"
    "v_fmac_f32 v116, s61, v8\n"
    "v_fmac_f32 v120, s62, v12\n"
    "v_fmac_f32 v117, s61, v9\n"
    "v_fmac_f32 v121, s62, v13\n"
    "v_fmac_f32 v118, s61, v10\n"
    "v_fmac_f32 v122, s62, v14\n"
    "v_fmac_f32 v119, s61, v11\n"
    "v_fmac_f32 v123, s62, v15\n"
    "s_mov_b32 s30, 0xa03000\n"
    "s_mov_b32 s31, 0xa05000\n"
    "s_mov_b32 s34, 0xa07000\n"
    "ds_read_b64 v[8:9], v124 offset:1536\n"
    "ds_read_b64 v[10:11], v124 offset:1544\n"
    "buffer_store_dwordx4 v[56:59], v4, s[4:7], s39 offen sc1\n"
    "s_waitcnt lgkmcnt(2)\n"
    "s_barrier\n"
    "ds_read_b128 v[40:43], v113\n"
    "ds_read_b128 v[44:47], v113 offset:128\n"
    "ds_read_b128 v[48:51], v113 offset:256\n"
    "ds_read_b128 v[52:55], v113 offset:384\n"
    "s_add_u32 s38, s84, 0x600000\n"
    "s_add_u32 s39, s85, 0xa08000\n"
    "ds_read_b128 v[24:27], v113 offset:512\n"
    "ds_read_b128 v[28:31], v113 offset:640\n"
    "ds_read_b128 v[32:35], v113 offset:768\n"
    "ds_read_b128 v[36:39], v113 offset:896\n"
    "s_waitcnt lgkmcnt(7)\n"
    "v_mfma_f32_16x16x32_bf16 v[84:87], a[204:207], v[40:43], 0\n"
    "ds_read_b64 v[12:13], v124 offset:1024\n"
    "v_mfma_f32_16x16x32_bf16 v[88:91], a[220:223], v[40:43], 0\n"
    "ds_read_b64 v[14:15], v124 offset:1032\n"
    "v_mfma_f32_16x16x32_bf16 v[92:95], a[236:239], v[40:43], 0\n"
    "v_mfma_f32_16x16x32_bf16 v[96:99], a[252:255], v[40:43], 0\n"
    "ds_read_b128 v[40:43], v113 offset:1024\n"
    "s_waitcnt lgkmcnt(7)\n"
    "v_mfma_f32_16x16x32_bf16 v[84:87], a[200:203], v[44:47], v[84:87]\n"
    "v_mfma_f32_16x16x32_bf16 v[88:91], a[216:219], v[44:47], v[88:91]\n"
    "v_mfma_f32_16x16x32_bf16 v[92:95], a[232:235], v[44:47], v[92:95]\n"
    "v_mfma_f32_16x16x32_bf16 v[96:99], a[248:251], v[44:47], v[96:99]\n"
    "ds_read_b128 v[44:47], v113 offset:1152\n"
    "v_mfma_f32_16x16x32_bf16 v[84:87], a[196:199], v[48:51], v[84:87]\n"
    "v_mfma_f32_16x16x32_bf16 v[88:91], a[212:215], v[48:51], v[88:91]\n"
    "buffer_store_dwordx4 v[56:59], v127, s[4:7], s40 offen sc1\n"
    "v_mfma_f32_16x16x32_bf16 v[92:95], a[228:231], v[48:51], v[92:95]\n"
    "s_mov_b32 s40, 32768\n"
    "v_mfma_f32_16x16x32_bf16 v[96:99], a[244:247], v[48:51], v[96:99]\n"
    "ds_read_b128 v[48:51], v113 offset:1280\n"
    "v_mfma_f32_16x16x32_bf16 v[84:87], a[192:195], v[52:55], v[84:87]\n"
    "s_nop 2\n"
    "v_mfma_f32_16x16x32_bf16 v[88:91], a[208:211], v[52:55], v[88:91]\n"
    "s_nop 2\n"
    "v_mfma_f32_16x16x32_bf16 v[92:95], a[224:227], v[52:55], v[92:95]\n"
    "ds_write_b128 v114, v[84:87]\n"
    "v_mfma_f32_16x16x32_bf16 v[96:99], a[240:243], v[52:55], v[96:99]\n"
    "ds_read_b128 v[52:55], v113 offset:1408\n"
    "s_waitcnt lgkmcnt(3)\n"
    "v_mfma_f32_16x16x32_bf16 v[88:91], a[204:207], v[24:27], v[88:91]\n"
    "v_mfma_f32_16x16x32_bf16 v[92:95], a[220:223], v[24:27], v[92:95]\n"
    "v_mfma_f32_16x16x32_bf16 v[96:99], a[236:239], v[24:27], v[96:99]\n"
    "ds_read_b128 v[24:27], v113 offset:1536\n"
    "v_mfma_f32_16x16x32_bf16 v[88:91], a[200:203], v[28:31], v[88:91]\n"
    "v_mfma_f32_16x16x32_bf16 v[92:95], a[216:219], v[28:31], v[92:95]\n"
    "v_mfma_f32_16x16x32_bf16 v[96:99], a[232:235], v[28:31], v[96:99]\n"
    "ds_read_b128 v[28:31], v113 offset:1664\n"
    "v_mfma_f32_16x16x32_bf16 v[88:91], a[196:199], v[32:35], v[88:91]\n"
    "v_mfma_f32_16x16x32_bf16 v[92:95], a[212:215], v[32:35], v[92:95]\n"
    "v_mfma_f32_16x16x32_bf16 v[96:99], a[228:231], v[32:35], v[96:99]\n"
    "ds_read_b128 v[32:35], v113 offset:1792\n"
    "v_mfma_f32_16x16x32_bf16 v[88:91], a[192:195], v[36:39], v[88:91]\n"
    "s_nop 2\n"
    "v_mfma_f32_16x16x32_bf16 v[92:95], a[208:211], v[36:39], v[92:95]\n"
    "s_nop 2\n"
    "v_mfma_f32_16x16x32_bf16 v[96:99], a[224:227], v[36:39], v[96:99]\n"
    "ds_read_b128 v[36:39], v113 offset:1920\n"
    "v_mfma_f32_16x16x32_bf16 v[92:95], a[204:207], v[40:43], v[92:95]\n"
    "ds_write_b128 v114, v[88:91] offset:1024\n"
    "v_mfma_f32_16x16x32_bf16 v[96:99], a[220:223], v[40:43], v[96:99]\n"
    "ds_read_b128 v[40:43], v113 offset:1536\n"
    "v_mfma_f32_16x16x32_bf16 v[92:95], a[200:203], v[44:47], v[92:95]\n"
    "v_mfma_f32_16x16x32_bf16 v[96:99], a[216:219], v[44:47], v[96:99]\n"
    "ds_read_b128 v[44:47], v113 offset:1024\n"
    "s_waitcnt lgkmcnt(4)\n"
    "v_mfma_f32_16x16x32_bf16 v[92:95], a[196:199], v[48:51], v[92:95]\n"
    "v_mfma_f32_16x16x32_bf16 v[96:99], a[212:215], v[48:51], v[96:99]\n"
    "ds_read_b128 v[48:51], v113 offset:512\n"
    "v_mfma_f32_16x16x32_bf16 v[92:95], a[192:195], v[52:55], v[92:95]\n"
    "s_nop 2\n"
    "v_mfma_f32_16x16x32_bf16 v[96:99], a[208:211], v[52:55], v[96:99]\n"
    "ds_read_b128 v[52:55], v113\n"
    "s_nop 0\n"
    "v_mfma_f32_16x16x32_bf16 v[96:99], a[204:207], v[24:27], v[96:99]\n"
    "ds_read_b64 v[24:25], v124 offset:1664\n"
    "v_mfma_f32_16x16x32_bf16 v[96:99], a[200:203], v[28:31], v[96:99]\n"
    "ds_write_b128 v114, v[92:95] offset:2048\n"
    "v_mfma_f32_16x16x32_bf16 v[96:99], a[196:199], v[32:35], v[96:99]\n"
    "ds_read_b64 v[26:27], v124 offset:1672\n"
    "s_waitcnt lgkmcnt(5)\n"
    "v_mfma_f32_16x16x32_bf16 v[96:99], a[192:195], v[36:39], v[96:99]\n"
    "ds_read_b64 v[28:29], v124 offset:1152\n"
    "s_nop 0\n"
    "v_mfma_f32_16x16x32_bf16 v[84:87], a[220:223], v[40:43], 0\n"
    "ds_read_b64 v[30:31], v124 offset:1160\n"
    "s_nop 0\n"
    "v_mfma_f32_16x16x32_bf16 v[88:91], a[236:239], v[40:43], 0\n"
    "ds_read_b64 v[32:33], v124 offset:640\n"
    "s_nop 0\n"
    "ds_write_b128 v114, v[96:99] offset:3072\n"
    "v_mfma_f32_16x16x32_bf16 v[92:95], a[252:255], v[40:43], 0\n"
    "ds_read_b64 v[34:35], v124 offset:648\n"
    "buffer_load_dwordx4 v[64:67], v0, s[4:7], s28 offen sc1\n"
    "v_mfma_f32_16x16x32_bf16 v[84:87], a[236:239], v[44:47], v[84:87]\n"
    "ds_read_b64 v[36:37], v124 offset:128\n"
    "v_mfma_f32_16x16x32_bf16 v[88:91], a[252:255], v[44:47], v[88:91]\n"
    "ds_read_b64 v[38:39], v124 offset:136\n"
    "v_mfma_f32_16x16x32_bf16 v[92:95], v[8:11], v[44:47], v[92:95]\n"
    "v_mfma_f32_16x16x32_bf16 v[96:99], v[12:15], v[44:47], 0\n"
    "ds_read_b128 v[44:47], v113 offset:1152\n"
    "s_waitcnt vmcnt(3) lgkmcnt(4)\n"
    "s_barrier\n"
    "ds_read_b128 v[100:103], v115\n"
    "ds_read_b128 v[56:59], v115 offset:8192\n"
    "ds_read_b128 v[104:107], v115 offset:4096\n"
    "ds_read_b128 v[60:63], v115 offset:12288\n"
    "v_mfma_f32_16x16x32_bf16 v[96:99], v[8:11], v[40:43], v[96:99]\n"
    "ds_read_b128 v[40:43], v113 offset:1664\n"
    "v_mfma_f32_16x16x32_bf16 v[84:87], a[252:255], v[48:51], v[84:87]\n"
    "v_mfma_f32_16x16x32_bf16 v[88:91], v[8:11], v[48:51], v[88:91]\n"
    "v_mfma_f32_16x16x32_bf16 v[92:95], v[12:15], v[48:51], v[92:95]\n"
    "v_mfma_f32_16x16x32_bf16 v[96:99], v[16:19], v[48:51], v[96:99]\n"
    "s_waitcnt lgkmcnt(4)\n"
    "v_add_f32 v116, v116, v100\n"
    "v_add_f32 v117, v117, v101\n"
    "v_add_f32 v118, v118, v102\n"
    "v_add_f32 v119, v119, v103\n"
    "s_waitcnt lgkmcnt(3)\n"
    "v_add_f32 v120, v120, v56\n"
    "v_add_f32 v121, v121, v57\n"
    "v_add_f32 v122, v122, v58\n"
    "v_add_f32 v123, v123, v59\n"
    "s_waitcnt lgkmcnt(2)\n"
    "v_add_f32 v116, v116, v104\n"
    "v_add_f32 v117, v117, v105\n"
    "v_add_f32 v118, v118, v106\n"
    "v_add_f32 v119, v119, v107\n"
    "s_waitcnt lgkmcnt(1)\n"
    "v_add_f32 v120, v120, v60\n"
    "v_add_f32 v121, v121, v61\n"
    "v_add_f32 v122, v122, v62\n"
    "v_add_f32 v123, v123, v63\n"
    "v_cvt_pk_bf16_f32 v108, v116, v120\n"
    "v_cvt_pk_bf16_f32 v109, v117, v121\n"
    "v_cvt_pk_bf16_f32 v110, v118, v122\n"
    "v_cvt_pk_bf16_f32 v111, v119, v123\n"
    "v_min_u32 v108, -2, v108\n"
    "v_min_u32 v109, -2, v109\n"
    "v_min_u32 v110, -2, v110\n"
    "v_min_u32 v111, -2, v111\n"
    "buffer_store_dwordx4 v[108:111], v125, s[4:7], s41 offen sc1\n"
    "buffer_load_dwordx4 v[68:71], v0, s[4:7], s29 offen sc1\n"
    "v_mfma_f32_16x16x32_bf16 v[84:87], v[8:11], v[52:55], v[84:87]\n"
    "ds_read_b128 v[48:51], v113 offset:640\n"
    "s_mov_b32 s41, 28672\n"
    "v_mfma_f32_16x16x32_bf16 v[88:91], v[12:15], v[52:55], v[88:91]\n"
    "ds_read_b64 v[8:9], v124 offset:1792\n"
    "v_mfma_f32_16x16x32_bf16 v[92:95], v[16:19], v[52:55], v[92:95]\n"
    "ds_read_b64 v[10:11], v124 offset:1800\n"
    "v_mfma_f32_16x16x32_bf16 v[96:99], v[20:23], v[52:55], v[96:99]\n"
    "s_waitcnt lgkmcnt(3)\n"
    "buffer_load_dwordx4 v[72:75], v0, s[4:7], s30 offen sc1\n"
    "v_mfma_f32_16x16x32_bf16 v[84:87], a[216:219], v[40:43], v[84:87]\n"
    "ds_read_b128 v[52:55], v113 offset:128\n"
    "v_mfma_f32_16x16x32_bf16 v[88:91], a[232:235], v[40:43], v[88:91]\n"
    "ds_read_b64 v[12:13], v124 offset:1280\n"
    "v_mfma_f32_16x16x32_bf16 v[92:95], a[248:251], v[40:43], v[92:95]\n"
    "ds_read_b64 v[14:15], v124 offset:1288\n"
    "v_mfma_f32_16x16x32_bf16 v[96:99], v[24:27], v[40:43], v[96:99]\n"
    "buffer_load_dwordx4 v[76:79], v0, s[4:7], s31 offen sc1\n"
    "v_mfma_f32_16x16x32_bf16 v[84:87], a[232:235], v[44:47], v[84:87]\n"
    "ds_read_b128 v[40:43], v113 offset:1792\n"
    "v_mfma_f32_16x16x32_bf16 v[88:91], a[248:251], v[44:47], v[88:91]\n"
    "ds_read_b64 v[16:17], v124 offset:768\n"
    "v_mfma_f32_16x16x32_bf16 v[92:95], v[24:27], v[44:47], v[92:95]\n"
    "ds_read_b64 v[18:19], v124 offset:776\n"
    "v_mfma_f32_16x16x32_bf16 v[96:99], v[28:31], v[44:47], v[96:99]\n"
    "s_waitcnt lgkmcnt(5)\n"
    "buffer_load_dwordx4 v[80:83], v0, s[4:7], s34 offen sc1\n"
    "v_mfma_f32_16x16x32_bf16 v[84:87], a[248:251], v[48:51], v[84:87]\n"
    "ds_read_b128 v[44:47], v113 offset:1280\n"
    "v_mfma_f32_16x16x32_bf16 v[88:91], v[24:27], v[48:51], v[88:91]\n"
    "ds_read_b64 v[20:21], v124 offset:256\n"
    "v_mfma_f32_16x16x32_bf16 v[92:95], v[28:31], v[48:51], v[92:95]\n"
    "ds_read_b64 v[22:23], v124 offset:264\n"
    "v_mfma_f32_16x16x32_bf16 v[96:99], v[32:35], v[48:51], v[96:99]\n"
    "ds_read_b128 v[48:51], v113 offset:768\n"
    "v_mfma_f32_16x16x32_bf16 v[84:87], v[24:27], v[52:55], v[84:87]\n"
    "ds_read_b64 v[24:25], v124 offset:1920\n"
    "v_mfma_f32_16x16x32_bf16 v[88:91], v[28:31], v[52:55], v[88:91]\n"
    "ds_read_b64 v[26:27], v124 offset:1928\n"
    "v_mfma_f32_16x16x32_bf16 v[92:95], v[32:35], v[52:55], v[92:95]\n"
    "ds_read_b64 v[28:29], v124 offset:1408\n"
    "v_mfma_f32_16x16x32_bf16 v[96:99], v[36:39], v[52:55], v[96:99]\n"
    "ds_read_b128 v[52:55], v113 offset:256\n"
    "s_waitcnt lgkmcnt(7)\n"
    "v_mfma_f32_16x16x32_bf16 v[84:87], a[212:215], v[40:43], v[84:87]\n"
    "ds_read_b64 v[30:31], v124 offset:1416\n"
    "v_mfma_f32_16x16x32_bf16 v[88:91], a[228:231], v[40:43], v[88:91]\n"
    "ds_read_b64 v[32:33], v124 offset:896\n"
    "v_mfma_f32_16x16x32_bf16 v[92:95], a[244:247], v[40:43], v[92:95]\n"
    "ds_read_b64 v[34:35], v124 offset:904\n"
    "v_mfma_f32_16x16x32_bf16 v[96:99], v[8:11], v[40:43], v[96:99]\n"
    "ds_read_b128 v[40:43], v113 offset:1920\n"
    "v_mfma_f32_16x16x32_bf16 v[84:87], a[228:231], v[44:47], v[84:87]\n"
    "ds_read_b64 v[36:37], v124 offset:384\n"
    "v_mfma_f32_16x16x32_bf16 v[88:91], a[244:247], v[44:47], v[88:91]\n"
    "ds_read_b64 v[38:39], v124 offset:392\n"
    "v_mfma_f32_16x16x32_bf16 v[92:95], v[8:11], v[44:47], v[92:95]\n"
    "v_mfma_f32_16x16x32_bf16 v[96:99], v[12:15], v[44:47], v[96:99]\n"
    "ds_read_b128 v[44:47], v113 offset:1408\n"
    "s_waitcnt lgkmcnt(6)\n"
    "v_mfma_f32_16x16x32_bf16 v[84:87], a[244:247], v[48:51], v[84:87]\n"
    "v_mfma_f32_16x16x32_bf16 v[88:91], v[8:11], v[48:51], v[88:91]\n"
    "v_mfma_f32_16x16x32_bf16 v[92:95], v[12:15], v[48:51], v[92:95]\n"
    "v_mfma_f32_16x16x32_bf16 v[96:99], v[16:19], v[48:51], v[96:99]\n"
    "ds_read_b128 v[48:51], v113 offset:896\n"
    "v_mfma_f32_16x16x32_bf16 v[84:87], v[8:11], v[52:55], v[84:87]\n"
    "s_waitcnt vmcnt(5)\n"
    "v_max3_u32 v108, v64, v65, v66\n"
    "v_mfma_f32_16x16x32_bf16 v[88:91], v[12:15], v[52:55], v[88:91]\n"
    "v_mfma_f32_16x16x32_bf16 v[92:95], v[16:19], v[52:55], v[92:95]\n"
    "v_mfma_f32_16x16x32_bf16 v[96:99], v[20:23], v[52:55], v[96:99]\n"
    "ds_read_b128 v[52:55], v113 offset:384\n"
    "s_waitcnt lgkmcnt(2)\n"
    "v_mfma_f32_16x16x32_bf16 v[84:87], a[208:211], v[40:43], v[84:87]\n"
    "v_mfma_f32_16x16x32_bf16 v[88:91], a[224:227], v[40:43], v[88:91]\n"
    "v_mfma_f32_16x16x32_bf16 v[92:95], a[240:243], v[40:43], v[92:95]\n"
    "v_mfma_f32_16x16x32_bf16 v[96:99], v[24:27], v[40:43], v[96:99]\n"
    "v_mfma_f32_16x16x32_bf16 v[84:87], a[224:227], v[44:47], v[84:87]\n"
    "v_mfma_f32_16x16x32_bf16 v[88:91], a[240:243], v[44:47], v[88:91]\n"
    "v_mfma_f32_16x16x32_bf16 v[92:95], v[24:27], v[44:47], v[92:95]\n"
    "v_mfma_f32_16x16x32_bf16 v[96:99], v[28:31], v[44:47], v[96:99]\n"
    "s_waitcnt lgkmcnt(0)\n"
    "v_mfma_f32_16x16x32_bf16 v[84:87], a[240:243], v[48:51], v[84:87]\n"
    "v_mfma_f32_16x16x32_bf16 v[88:91], v[24:27], v[48:51], v[88:91]\n"
    "v_mfma_f32_16x16x32_bf16 v[92:95], v[28:31], v[48:51], v[92:95]\n"
    "v_mfma_f32_16x16x32_bf16 v[96:99], v[32:35], v[48:51], v[96:99]\n"
    "v_mfma_f32_16x16x32_bf16 v[84:87], v[24:27], v[52:55], v[84:87]\n"
    "v_mfma_f32_16x16x32_bf16 v[88:91], v[28:31], v[52:55], v[88:91]\n"
    "v_mfma_f32_16x16x32_bf16 v[92:95], v[32:35], v[52:55], v[92:95]\n"
    "v_mfma_f32_16x16x32_bf16 v[96:99], v[36:39], v[52:55], v[96:99]\n"
    "s_waitcnt vmcnt(3)\n"
    "v_max3_u32 v108, v108, v67, v68\n"
    "v_max3_u32 v108, v108, v69, v70\n"
    "v_dot2_f32_bf16 v100, v68, s35, 0\n"
    "v_dot2_f32_bf16 v104, v68, v68, s36\n"
    "v_dot2_f32_bf16 v101, v69, s35, 0\n"
    "v_dot2_f32_bf16 v105, v69, v69, s36\n"
    "s_waitcnt vmcnt(2)\n"
    "v_max3_u32 v108, v108, v71, v72\n"
    "v_max3_u32 v108, v108, v73, v74\n"
    "v_dot2_f32_bf16 v102, v70, s35, 0\n"
    "v_dot2_f32_bf16 v106, v70, v70, s36\n"
    "v_dot2_f32_bf16 v103, v71, s35, 0\n"
    "v_dot2_f32_bf16 v107, v71, v71, s36\n"
    "v_dot2_f32_bf16 v100, v72, s35, v100\n"
    "v_dot2_f32_bf16 v104, v72, v72, v104\n"
    "v_dot2_f32_bf16 v101, v73, s35, v101\n"
    "v_dot2_f32_bf16 v105, v73, v73, v105\n"
    "v_dot2_f32_bf16 v102, v74, s35, v102\n"
    "v_dot2_f32_bf16 v106, v74, v74, v106\n"
    "v_dot2_f32_bf16 v103, v75, s35, v103\n"
    "v_dot2_f32_bf16 v107, v75, v75, v107\n"
    "s_waitcnt vmcnt(1)\n"
    "v_max3_u32 v108, v108, v75, v76\n"
    "v_max3_u32 v108, v108, v77, v78\n"
    "v_dot2_f32_bf16 v100, v76, s35, v100\n"
    "v_dot2_f32_bf16 v104, v76, v76, v104\n"
    "v_dot2_f32_bf16 v101, v77, s35, v101\n"
    "v_dot2_f32_bf16 v105, v77, v77, v105\n"
    "v_dot2_f32_bf16 v102, v78, s35, v102\n"
    "v_dot2_f32_bf16 v106, v78, v78, v106\n"
    "v_dot2_f32_bf16 v103, v79, s35, v103\n"
    "v_dot2_f32_bf16 v107, v79, v79, v107\n"
    "s_waitcnt vmcnt(0)\n"
    "v_max3_u32 v108, v108, v79, v80\n"
    "v_max3_u32 v108, v108, v81, v82\n"
    "v_max_u32 v108, v108, v83\n"
    "v_cmp_eq_u32 vcc, -1, v108\n"
    "v_dot2_f32_bf16 v100, v80, s35, v100\n"
    "v_dot2_f32_bf16 v104, v80, v80, v104\n"
    "v_dot2_f32_bf16 v101, v81, s35, v101\n"
    "v_dot2_f32_bf16 v105, v81, v81, v105\n"
    "v_dot2_f32_bf16 v102, v82, s35, v102\n"
    "v_dot2_f32_bf16 v106, v82, v82, v106\n"
    "v_dot2_f32_bf16 v103, v83, s35, v103\n"
    "v_dot2_f32_bf16 v107, v83, v83, v107\n"
    "s_nop 2\n"
    "s_cbranch_vccnz .R_6\n"
    ".R_6c:\n"
    "v_pk_fma_f32 v[104:105], v[100:101], v[100:101], v[104:105] neg_lo:[1,0,0] neg_hi:[1,0,0]\n"
    "v_pk_fma_f32 v[106:107], v[102:103], v[102:103], v[106:107] neg_lo:[1,0,0] neg_hi:[1,0,0]\n"
    "v_max_f32 v104, s36, v104\n"
    "v_max_f32 v105, s36, v105\n"
    "v_max_f32 v106, s36, v106\n"
    "v_max_f32 v107, s36, v107\n"
    "v_lshlrev_b32 v116, 16, v64\n"
    "v_lshlrev_b32 v117, 16, v65\n"
    "v_lshlrev_b32 v118, 16, v66\n"
    "v_lshlrev_b32 v119, 16, v67\n"
    "v_rsq_f32 v104, v104\n"
    "v_rsq_f32 v105, v105\n"
    "v_rsq_f32 v106, v106\n"
    "v_rsq_f32 v107, v107\n"
    "v_pk_fma_f32 v[56:57], v[116:117], 2.0, v[100:101] op_sel_hi:[1,0,1]\n"
    "v_pk_fma_f32 v[58:59], v[118:119], 2.0, v[102:103] op_sel_hi:[1,0,1]\n"
    "v_and_b32 v120, 0xffff0000, v64\n"
    "v_and_b32 v121, 0xffff0000, v65\n"
    "v_pk_mul_f32 v[56:57], v[56:57], v[104:105]\n"
    "v_pk_mul_f32 v[58:59], v[58:59], v[106:107]\n"
    "v_pk_fma_f32 v[60:61], v[120:121], 2.0, v[100:101] op_sel_hi:[1,0,1]\n"
    "v_and_b32 v122, 0xffff0000, v66\n"
    "v_and_b32 v123, 0xffff0000, v67\n"
    "v_cvt_pk_bf16_f32 v100, v56, v57\n"
    "v_cvt_pk_bf16_f32 v101, v58, v59\n"
    "v_pk_fma_f32 v[62:63], v[122:123], 2.0, v[102:103] op_sel_hi:[1,0,1]\n"
    "s_add_u32 s28, s86, 0x600000\n"
    "ds_write_b64 v112, v[100:101] offset:2048\n"
    "v_pk_mul_f32 v[60:61], v[60:61], v[104:105]\n"
    "v_pk_mul_f32 v[62:63], v[62:63], v[106:107]\n"
    "v_cvt_pk_bf16_f32 v102, v60, v61\n"
    "v_cvt_pk_bf16_f32 v103, v62, v63\n"
    "s_mov_b32 s29, 0xa08000\n"
    "ds_write_b64 v112, v[102:103] offset:6656\n"
    "v_fmac_f32 v116, s61, v56\n"
    "v_fmac_f32 v120, s62, v60\n"
    "v_fmac_f32 v117, s61, v57\n"
    "v_fmac_f32 v121, s62, v61\n"
    "v_fmac_f32 v118, s61, v58\n"
    "v_fmac_f32 v122, s62, v62\n"
    "v_fmac_f32 v119, s61, v59\n"
    "v_fmac_f32 v123, s62, v63\n"
    "s_mov_b32 s30, 0xa0a000\n"
    "s_mov_b32 s31, 0xa0c000\n"
    "s_mov_b32 s34, 0xa0e000\n"
    "s_mov_b32 s35, 0x0000bf00\n"
    "s_mov_b32 s36, 0x3827c5ac\n"
    "s_waitcnt lgkmcnt(0)\n"
    "s_barrier\n"
    "ds_read_b128 v[64:67], v113 offset:2048\n"
    "ds_read_b128 v[68:71], v113 offset:2176\n"
    "ds_read_b128 v[72:75], v113 offset:2304\n"
    "ds_read_b128 v[76:79], v113 offset:2432\n"
    "ds_read_b128 v[80:83], v113 offset:2560\n"
    "ds_read_b128 v[104:107], v113 offset:2688\n"
    "s_waitcnt lgkmcnt(5)\n"
    "v_mfma_f32_16x16x32_bf16 v[84:87], a[204:207], v[64:67], v[84:87]\n"
    "v_mfma_f32_16x16x32_bf16 v[88:91], a[220:223], v[64:67], v[88:91]\n"
    "v_mfma_f32_16x16x32_bf16 v[92:95], a[236:239], v[64:67], v[92:95]\n"
    "v_mfma_f32_16x16x32_bf16 v[96:99], a[252:255], v[64:67], v[96:99]\n"
    "ds_read_b128 v[64:67], v113 offset:2816\n"
    "s_waitcnt lgkmcnt(3)\n"
    "v_mfma_f32_16x16x32_bf16 v[84:87], a[200:203], v[68:71], v[84:87]\n"
    "v_mfma_f32_16x16x32_bf16 v[88:91], a[216:219], v[68:71], v[88:91]\n"
    "v_mfma_f32_16x16x32_bf16 v[92:95], a[232:235], v[68:71], v[92:95]\n"
    "v_mfma_f32_16x16x32_bf16 v[96:99], a[248:251], v[68:71], v[96:99]\n"
    "ds_read_b128 v[68:71], v113 offset:2944\n"
    "v_mfma_f32_16x16x32_bf16 v[84:87], a[196:199], v[72:75], v[84:87]\n"
    "v_mfma_f32_16x16x32_bf16 v[88:91], a[212:215], v[72:75], v[88:91]\n"
    "v_mfma_f32_16x16x32_bf16 v[92:95], a[228:231], v[72:75], v[92:95]\n"
    "v_mfma_f32_16x16x32_bf16 v[96:99], a[244:247], v[72:75], v[96:99]\n"
    "ds_read_b128 v[72:75], v113 offset:3072\n"
    "v_mfma_f32_16x16x32_bf16 v[84:87], a[192:195], v[76:79], v[84:87]\n"
    "s_nop 2\n"
    "v_mfma_f32_16x16x32_bf16 v[88:91], a[208:211], v[76:79], v[88:91]\n"
    "s_nop 2\n"
    "v_mfma_f32_16x16x32_bf16 v[92:95], a[224:227], v[76:79], v[92:95]\n"
    "buffer_load_dwordx4 v[8:11], v1, s[4:7], s79 offen sc1\n"
    "v_mfma_f32_16x16x32_bf16 v[96:99], a[240:243], v[76:79], v[96:99]\n"
    "buffer_load_dwordx4 v[12:15], v1, s[4:7], s79 offen offset:1024 sc1\n"
    "s_waitcnt lgkmcnt(1)\n"
    "v_mfma_f32_16x16x32_bf16 v[88:91], a[204:207], v[80:83], v[88:91]\n"
    "ds_read_b128 v[76:79], v113 offset:3200\n"
    "v_mfma_f32_16x16x32_bf16 v[92:95], a[220:223], v[80:83], v[92:95]\n"
    "ds_write_b128 v114, v[84:87]\n"
    "v_mfma_f32_16x16x32_bf16 v[96:99], a[236:239], v[80:83], v[96:99]\n"
    "ds_read_b128 v[80:83], v113 offset:3328\n"
    "v_xor_b32 v85, 1040, v5\n"
    "v_mfma_f32_16x16x32_bf16 v[88:91], a[200:203], v[104:107], v[88:91]\n"
    "buffer_load_dwordx4 v[16:19], v1, s[4:7], s79 offen offset:2048 sc1\n"
    "v_xor_b32 v86, 2080, v5\n"
    "v_mfma_f32_16x16x32_bf16 v[92:95], a[216:219], v[104:107], v[92:95]\n"
    "v_xor_b32 v87, 3120, v5\n"
    "v_mfma_f32_16x16x32_bf16 v[96:99], a[232:235], v[104:107], v[96:99]\n"
    "ds_read_b128 v[104:107], v113 offset:3456\n"
    "v_mfma_f32_16x16x32_bf16 v[88:91], a[196:199], v[64:67], v[88:91]\n"
    "buffer_load_dwordx4 v[20:23], v1, s[4:7], s79 offen offset:3072 sc1\n"
    "v_mfma_f32_16x16x32_bf16 v[92:95], a[212:215], v[64:67], v[92:95]\n"
    "v_mfma_f32_16x16x32_bf16 v[96:99], a[228:231], v[64:67], v[96:99]\n"
    "ds_read_b128 v[64:67], v113 offset:3584\n"
    "v_mfma_f32_16x16x32_bf16 v[88:91], a[192:195], v[68:71], v[88:91]\n"
    "s_nop 2\n"
    "v_mfma_f32_16x16x32_bf16 v[92:95], a[208:211], v[68:71], v[92:95]\n"
    "buffer_load_dwordx4 v[24:27], v1, s[4:7], s80 offen sc1\n"
    "v_mfma_f32_16x16x32_bf16 v[96:99], a[224:227], v[68:71], v[96:99]\n"
    "ds_read_b128 v[68:71], v113 offset:3712\n"
    "s_waitcnt lgkmcnt(3)\n"
    "v_mfma_f32_16x16x32_bf16 v[92:95], a[204:207], v[72:75], v[92:95]\n"
    "ds_write_b128 v114, v[88:91] offset:1024\n"
    "v_mfma_f32_16x16x32_bf16 v[96:99], a[220:223], v[72:75], v[96:99]\n"
    "ds_read_b128 v[72:75], v113 offset:3840\n"
    "v_xor_b32 v89, 64, v3\n"
    "v_mfma_f32_16x16x32_bf16 v[92:95], a[200:203], v[76:79], v[92:95]\n"
    "buffer_load_dwordx4 v[28:31], v1, s[4:7], s80 offen offset:1024 sc1\n"
    "v_xor_b32 v90, 128, v3\n"
    "v_mfma_f32_16x16x32_bf16 v[96:99], a[216:219], v[76:79], v[96:99]\n"
    "ds_read_b128 v[76:79], v113 offset:3968\n"
    "v_xor_b32 v91, 192, v3\n"
    "v_mfma_f32_16x16x32_bf16 v[92:95], a[196:199], v[80:83], v[92:95]\n"
    "v_add_u32 v88, 49152, v3\n"
    "v_mfma_f32_16x16x32_bf16 v[96:99], a[212:215], v[80:83], v[96:99]\n"
    "s_waitcnt lgkmcnt(3)\n"
    "v_mfma_f32_16x16x32_bf16 v[92:95], a[192:195], v[104:107], v[92:95]\n"
    "buffer_load_dwordx4 v[32:35], v1, s[4:7], s80 offen offset:2048 sc1\n"
    "s_nop 0\n"
    "v_mfma_f32_16x16x32_bf16 v[96:99], a[208:211], v[104:107], v[96:99]\n"
    "s_nop 2\n"
    "v_mfma_f32_16x16x32_bf16 v[96:99], a[204:207], v[64:67], v[96:99]\n"
    "v_mfma_f32_16x16x32_bf16 v[96:99], a[200:203], v[68:71], v[96:99]\n"
    "ds_write_b128 v114, v[92:95] offset:2048\n"
    "s_waitcnt lgkmcnt(1)\n"
    "v_mfma_f32_16x16x32_bf16 v[96:99], a[196:199], v[72:75], v[96:99]\n"
    "buffer_load_dwordx4 v[36:39], v1, s[4:7], s80 offen offset:3072 sc1\n"
    "v_mov_b32 v68, 0x3f803f80\n"
    "v_mfma_f32_16x16x32_bf16 v[96:99], a[192:195], v[76:79], v[96:99]\n"
    "s_nop 0\n"
    "v_mov_b32 v69, 0x3f803f80\n"
    "v_mov_b32 v70, 0x3f803f80\n"
    "v_mov_b32 v71, 0x3f803f80\n"
    "s_nop 0\n"
    "buffer_load_dwordx4 v[40:43], v1, s[4:7], s81 offen sc1\n"
    "s_nop 1\n"
    "ds_write_b128 v114, v[96:99] offset:3072\n"
    "buffer_load_dwordx4 v[44:47], v1, s[4:7], s81 offen offset:1024 sc1\n"
    "buffer_load_dwordx4 v[48:51], v1, s[4:7], s81 offen offset:2048 sc1\n"
    "s_waitcnt lgkmcnt(0)\n"
    "s_barrier\n"
    "ds_read_b128 v[100:103], v115\n"
    "ds_read_b128 v[76:79], v115 offset:8192\n"
    "ds_read_b128 v[104:107], v115 offset:4096\n"
    "ds_read_b128 v[80:83], v115 offset:12288\n"
    "buffer_load_dwordx4 v[52:55], v1, s[4:7], s81 offen offset:3072 sc1\n"
    "buffer_load_dwordx4 v[112:115], v0, s[96:99], s44 offen\n"
    "s_waitcnt lgkmcnt(3)\n"
    "v_pk_add_f32 v[116:117], v[116:117], v[100:101]\n"
    "v_pk_add_f32 v[118:119], v[118:119], v[102:103]\n"
    "s_add_u32 s44, s87, 0xa32000\n"
    "s_waitcnt lgkmcnt(2)\n"
    "v_pk_add_f32 v[120:121], v[120:121], v[76:77]\n"
    "v_pk_add_f32 v[122:123], v[122:123], v[78:79]\n"
    "s_waitcnt lgkmcnt(1)\n"
    "v_pk_add_f32 v[116:117], v[116:117], v[104:105]\n"
    "v_pk_add_f32 v[118:119], v[118:119], v[106:107]\n"
    "v_add_u32 v104, 147456, v1\n"
    "s_waitcnt lgkmcnt(0)\n"
    "v_pk_add_f32 v[120:121], v[120:121], v[80:81]\n"
    "v_pk_add_f32 v[122:123], v[122:123], v[82:83]\n"
    "v_cvt_pk_bf16_f32 v108, v116, v120\n"
    "v_cvt_pk_bf16_f32 v109, v117, v121\n"
    "v_cvt_pk_bf16_f32 v110, v118, v122\n"
    "v_cvt_pk_bf16_f32 v111, v119, v123\n"
    "v_min_u32 v108, -2, v108\n"
    "v_min_u32 v109, -2, v109\n"
    "v_min_u32 v110, -2, v110\n"
    "v_min_u32 v111, -2, v111\n"
    "buffer_store_dwordx4 v[108:111], v125, s[4:7], s42 offen sc1\n"
    "s_add_u32 s42, s84, 0x601000\n"
    "buffer_load_dwordx4 v[124:127], v0, s[96:99], s43 offen\n"
    "s_add_u32 s43, s85, 0xa09000\n"
    "s_waitcnt vmcnt(14)\n"
    "v_max3_u32 v92, v8, v9, v10\n"
    "s_waitcnt vmcnt(13)\n"
    "v_max3_u32 v92, v92, v11, v12\n"
    "v_max3_u32 v92, v92, v13, v14\n"
    "s_waitcnt vmcnt(12)\n"
    "v_max3_u32 v92, v92, v15, v16\n"
    "v_max3_u32 v92, v92, v17, v18\n"
    "s_waitcnt vmcnt(11)\n"
    "v_max3_u32 v92, v92, v19, v20\n"
    "v_max3_u32 v92, v92, v21, v22\n"
    "v_max_u32 v92, v92, v23\n"
    "v_cmp_eq_u32 vcc, -1, v92\n"
    "s_cbranch_vccnz .R_7\n"
    ".R_7c:\n"
    "v_swap_b32 v9, v12\n"
    "v_swap_b32 v10, v16\n"
    "v_swap_b32 v11, v20\n"
    "ds_write_b128 v5, v[8:11]\n"
    "v_swap_b32 v14, v17\n"
    "v_swap_b32 v15, v21\n"
    "ds_write_b128 v85, v[12:15]\n"
    "v_swap_b32 v19, v22\n"
    "ds_write_b128 v86, v[16:19]\n"
    "s_waitcnt vmcnt(10)\n"
    "v_max3_u32 v92, v24, v25, v26\n"
    "s_waitcnt vmcnt(9)\n"
    "v_max3_u32 v92, v92, v27, v28\n"
    "v_max3_u32 v92, v92, v29, v30\n"
    "ds_write_b128 v87, v[20:23]\n"
    "s_waitcnt vmcnt(8)\n"
    "v_max3_u32 v92, v92, v31, v32\n"
    "v_max3_u32 v92, v92, v33, v34\n"
    "s_waitcnt vmcnt(7)\n"
    "v_max3_u32 v92, v92, v35, v36\n"
    "v_max3_u32 v92, v92, v37, v38\n"
    "s_waitcnt vmcnt(6)\n"
    "v_max3_u32 v92, v92, v39, v40\n"
    "v_max3_u32 v92, v92, v41, v42\n"
    "s_waitcnt lgkmcnt(0)\n"
    "s_barrier\n"
    "s_waitcnt vmcnt(5)\n"
    "v_max3_u32 v92, v92, v43, v44\n"
    "v_max3_u32 v92, v92, v45, v46\n"
    "ds_read_b128 a[200:203], v3\n"
    "ds_read_b128 a[216:219], v3 offset:256\n"
    "ds_read_b128 a[232:235], v3 offset:512\n"
    "ds_read_b128 a[248:251], v3 offset:768\n"
    "ds_read_b128 a[204:207], v89\n"
    "s_waitcnt vmcnt(4)\n"
    "v_max3_u32 v92, v92, v47, v48\n"
    "v_max3_u32 v92, v92, v49, v50\n"
    "ds_read_b128 a[220:223], v89 offset:256\n"
    "ds_read_b128 a[236:239], v89 offset:512\n"
    "ds_read_b128 a[252:255], v89 offset:768\n"
    "s_waitcnt lgkmcnt(7)\n"
    "v_mfma_f32_16x16x32_bf16 v[56:59], a[4:7], a[200:203], 0\n"
    "s_waitcnt vmcnt(3)\n"
    "v_max3_u32 v92, v92, v51, v52\n"
    "v_max3_u32 v92, v92, v53, v54\n"
    "v_max_u32 v92, v92, v55\n"
    "v_cmp_eq_u32 vcc, -1, v92\n"
    "v_mfma_f32_16x16x32_bf16 v[60:63], a[200:203], a[8:11], 0\n"
    "v_mfma_f32_16x16x32_bf16 v[8:11], a[200:203], a[200:203], 0\n"
    "s_cbranch_vccnz .R_8\n"
    ".R_8c:\n"
    "v_swap_b32 v25, v28\n"
    "v_swap_b32 v26, v32\n"
    "v_swap_b32 v27, v36\n"
    "v_mfma_f32_16x16x32_bf16 v[20:23], v[68:71], a[200:203], 0\n"
    "ds_write_b128 v5, v[24:27] offset:16384\n"
    "v_swap_b32 v30, v33\n"
    "v_swap_b32 v31, v37\n"
    "s_add_u32 s80, s58, 0x900000\n"
    "ds_write_b128 v85, v[28:31] offset:16384\n"
    "v_swap_b32 v35, v38\n"
    "ds_write_b128 v86, v[32:35] offset:16384\n"
    "s_waitcnt lgkmcnt(3)\n"
    "v_mfma_f32_16x16x32_bf16 v[56:59], a[52:55], a[216:219], v[56:59]\n"
    "v_swap_b32 v41, v44\n"
    "v_mfma_f32_16x16x32_bf16 v[60:63], a[216:219], a[56:59], v[60:63]\n"
    "ds_write_b128 v87, v[36:39] offset:16384\n"
    "v_mfma_f32_16x16x32_bf16 v[8:11], a[216:219], a[216:219], v[8:11]\n"
    "v_swap_b32 v42, v48\n"
    "v_mfma_f32_16x16x32_bf16 v[20:23], v[68:71], a[216:219], v[20:23]\n"
    "v_swap_b32 v43, v52\n"
    "v_mfma_f32_16x16x32_bf16 v[56:59], a[100:103], a[232:235], v[56:59]\n"
    "ds_write_b128 v5, v[40:43] offset:32768\n"
    "v_mfma_f32_16x16x32_bf16 v[60:63], a[232:235], a[104:107], v[60:63]\n"
    "v_swap_b32 v46, v49\n"
    "v_mfma_f32_16x16x32_bf16 v[8:11], a[232:235], a[232:235], v[8:11]\n"
    "v_swap_b32 v47, v53\n"
    "v_mfma_f32_16x16x32_bf16 v[20:23], v[68:71], a[232:235], v[20:23]\n"
    "ds_write_b128 v85, v[44:47] offset:32768\n"
    "v_mfma_f32_16x16x32_bf16 v[56:59], a[148:151], a[248:251], v[56:59]\n"
    "v_swap_b32 v51, v54\n"
    "v_mfma_f32_16x16x32_bf16 v[60:63], a[248:251], a[152:155], v[60:63]\n"
    "ds_write_b128 v86, v[48:51] offset:32768\n"
    "v_mfma_f32_16x16x32_bf16 v[8:11], a[248:251], a[248:251], v[8:11]\n"
    "s_sub_u32 s79, s80, 0x4000\n"
    "v_mov_b32 v24, -1\n"
    "v_mov_b32 v25, -1\n"
    "v_mfma_f32_16x16x32_bf16 v[20:23], v[68:71], a[248:251], v[20:23]\n"
    "ds_read_b128 a[200:203], v90\n"
    "v_mov_b32 v26, -1\n"
    "v_mfma_f32_16x16x32_bf16 v[56:59], a[16:19], a[204:207], v[56:59]\n"
    "ds_write_b128 v87, v[52:55] offset:32768\n"
    "v_mfma_f32_16x16x32_bf16 v[60:63], a[204:207], a[20:23], v[60:63]\n"
    "v_cndmask_b32 v8, v8, v9, s[64:65]\n"
    "v_cndmask_b32 v10, v10, v11, s[64:65]\n"
    "v_mfma_f32_16x16x32_bf16 v[56:59], a[64:67], a[220:223], v[56:59]\n"
    "ds_read_b128 a[216:219], v90 offset:256\n"
    "v_mfma_f32_16x16x32_bf16 v[60:63], a[220:223], a[68:71], v[60:63]\n"
    "ds_read_b128 a[232:235], v90 offset:512\n"
    "v_mfma_f32_16x16x32_bf16 v[56:59], a[112:115], a[236:239], v[56:59]\n"
    "ds_read_b128 a[248:251], v90 offset:768\n"
    "v_mfma_f32_16x16x32_bf16 v[60:63], a[236:239], a[116:119], v[60:63]\n"
    "ds_read_b128 a[204:207], v91\n"
    "v_mfma_f32_16x16x32_bf16 v[56:59], a[160:163], a[252:255], v[56:59]\n"
    "ds_read_b128 a[220:223], v91 offset:256\n"
    "v_mfma_f32_16x16x32_bf16 v[60:63], a[252:255], a[164:167], v[60:63]\n"
    "s_waitcnt lgkmcnt(6)\n"
    "ds_read_b128 a[236:239], v91 offset:512\n"
    "v_mfma_f32_16x16x32_bf16 v[56:59], a[28:31], a[200:203], v[56:59]\n"
    "s_waitcnt lgkmcnt(6)\n"
    "s_barrier\n"
    "v_mfma_f32_16x16x32_bf16 v[60:63], a[200:203], a[32:35], v[60:63]\n"
    "ds_read_b128 a[192:195], v3 offset:16384\n"
    "s_waitcnt lgkmcnt(6)\n"
    "v_mfma_f32_16x16x32_bf16 v[56:59], a[76:79], a[216:219], v[56:59]\n"
    "ds_read_b128 a[196:199], v3 offset:32768\n"
    "v_cndmask_b32 v21, v8, v10, s[70:71]\n"
    "v_mfma_f32_16x16x32_bf16 v[60:63], a[216:219], a[80:83], v[60:63]\n"
    "ds_read_b128 a[208:211], v3 offset:16640\n"
    "s_waitcnt lgkmcnt(6)\n"
    "v_mfma_f32_16x16x32_bf16 v[56:59], a[124:127], a[232:235], v[56:59]\n"
    "ds_read_b128 a[212:215], v3 offset:33024\n"
    "v_mov_b32 v27, -1\n"
    "v_mfma_f32_16x16x32_bf16 v[60:63], a[232:235], a[128:131], v[60:63]\n"
    "ds_read_b128 a[224:227], v3 offset:16896\n"
    "s_add_u32 s81, s80, 0x4000\n"
    "v_mfma_f32_16x16x32_bf16 v[56:59], a[172:175], a[248:251], v[56:59]\n"
    "ds_read_b128 a[228:231], v3 offset:33280\n"
    "v_mfma_f32_16x16x32_bf16 v[60:63], a[248:251], a[176:179], v[60:63]\n"
    "ds_read_b128 a[240:243], v3 offset:17152\n"
    "s_waitcnt lgkmcnt(6)\n"
    "v_mfma_f32_16x16x32_bf16 v[12:15], a[192:195], a[192:195], 0\n"
    "ds_read_b128 a[244:247], v3 offset:33536\n"
    "v_mfma_f32_16x16x32_bf16 v[76:79], v[68:71], a[192:195], 0\n"
    "ds_read_b128 a[252:255], v91 offset:768\n"
    "s_waitcnt lgkmcnt(7)\n"
    "v_mfma_f32_16x16x32_bf16 v[16:19], a[196:199], a[196:199], 0\n"
    "s_waitcnt vmcnt(0)\n"
    "ds_write_b64 v124, v[20:21]\n"
    "v_mfma_f32_16x16x32_bf16 v[80:83], v[68:71], a[196:199], 0\n"
    "ds_read_b128 a[196:199], v89 offset:16384\n"
    "s_waitcnt lgkmcnt(6)\n"
    "v_mfma_f32_16x16x32_bf16 v[12:15], a[208:211], a[208:211], v[12:15]\n"
    "buffer_store_dwordx4 v[24:27], v4, s[4:7], s45 offen sc1\n"
    "v_mfma_f32_16x16x32_bf16 v[76:79], v[68:71], a[208:211], v[76:79]\n"
    "ds_read_b128 a[200:203], v90 offset:16384\n"
    "s_mov_b32 s45, 36864\n"
    "v_mfma_f32_16x16x32_bf16 v[16:19], a[212:215], a[212:215], v[16:19]\n"
    "ds_read_b128 a[216:219], v90 offset:16640\n"
    "v_mfma_f32_16x16x32_bf16 v[80:83], v[68:71], a[212:215], v[80:83]\n"
    "ds_read_b128 a[212:215], v89 offset:16640\n"
    "v_mfma_f32_16x16x32_bf16 v[12:15], a[224:227], a[224:227], v[12:15]\n"
    "ds_read_b128 a[232:235], v90 offset:16896\n"
    "v_mfma_f32_16x16x32_bf16 v[76:79], v[68:71], a[224:227], v[76:79]\n"
    "ds_read_b128 a[248:251], v90 offset:17152\n"
    "s_waitcnt lgkmcnt(6)\n"
    "v_mfma_f32_16x16x32_bf16 v[16:19], a[228:231], a[228:231], v[16:19]\n"
    "ds_read_b32 v100, v126 offset:640\n"
    "v_mfma_f32_16x16x32_bf16 v[80:83], v[68:71], a[228:231], v[80:83]\n"
    "ds_read_b128 a[228:231], v89 offset:16896\n"
    "v_mfma_f32_16x16x32_bf16 v[12:15], a[240:243], a[240:243], v[12:15]\n"
    "ds_read_b128 v[96:99], v127 offset:576\n"
    "s_nop 0\n"
    "v_mfma_f32_16x16x32_bf16 v[76:79], v[68:71], a[240:243], v[76:79]\n"
    "ds_read_b128 v[92:95], v127 offset:512\n"
    "s_nop 0\n"
    "v_mfma_f32_16x16x32_bf16 v[16:19], a[244:247], a[244:247], v[16:19]\n"
    "s_nop 1\n"
    "v_cndmask_b32 v12, v12, v13, s[64:65]\n"
    "v_mfma_f32_16x16x32_bf16 v[80:83], v[68:71], a[244:247], v[80:83]\n"
    "s_nop 0\n"
    "v_cndmask_b32 v14, v14, v15, s[64:65]\n"
    "v_cndmask_b32 v77, v12, v14, s[70:71]\n"
    "v_mfma_f32_16x16x32_bf16 v[56:59], a[40:43], a[204:207], v[56:59]\n"
    "v_cndmask_b32 v16, v16, v17, s[64:65]\n"
    "v_cndmask_b32 v18, v18, v19, s[64:65]\n"
    "v_mfma_f32_16x16x32_bf16 v[60:63], a[204:207], a[44:47], v[60:63]\n"
    "s_nop 0\n"
    "v_cndmask_b32 v81, v16, v18, s[70:71]\n"
    "v_mfma_f32_16x16x32_bf16 v[56:59], a[88:91], a[220:223], v[56:59]\n"
    "ds_read_b128 a[244:247], v89 offset:17152\n"
    "v_mfma_f32_16x16x32_bf16 v[60:63], a[220:223], a[92:95], v[60:63]\n"
    "ds_read_b128 a[204:207], v91 offset:16384\n"
    "buffer_load_dwordx4 v[8:11], v1, s[4:7], s79 offen sc1\n"
    "v_mfma_f32_16x16x32_bf16 v[56:59], a[136:139], a[236:239], v[56:59]\n"
    "ds_write_b64 v124, v[76:77] offset:256\n"
    "v_mfma_f32_16x16x32_bf16 v[60:63], a[236:239], a[140:143], v[60:63]\n"
    "ds_write_b64 v124, v[80:81] offset:512\n"
    "v_mfma_f32_16x16x32_bf16 v[56:59], a[184:187], a[252:255], v[56:59]\n"
    "ds_read_b128 a[220:223], v91 offset:16640\n"
    "buffer_load_dwordx4 v[12:15], v1, s[4:7], s79 offen offset:1024 sc1\n"
    "v_mfma_f32_16x16x32_bf16 v[60:63], a[252:255], a[188:191], v[60:63]\n"
    "ds_read_b128 a[236:239], v91 offset:16896\n"
    "v_mfma_f32_16x16x32_bf16 v[64:67], a[0:3], a[192:195], 0\n"
    "ds_read_b128 a[252:255], v91 offset:17152\n"
    "v_mfma_f32_16x16x32_bf16 v[68:71], a[4:7], a[192:195], 0\n"
    "v_mfma_f32_16x16x32_bf16 v[64:67], a[48:51], a[208:211], v[64:67]\n"
    "buffer_load_dwordx4 v[16:19], v1, s[4:7], s79 offen offset:2048 sc1\n"
    "v_mfma_f32_16x16x32_bf16 v[68:71], a[52:55], a[208:211], v[68:71]\n"
    "s_waitcnt lgkmcnt(3)\n"
    "s_barrier\n"
    "v_mfma_f32_16x16x32_bf16 v[64:67], a[96:99], a[224:227], v[64:67]\n"
    "ds_read_b128 v[116:119], v104\n"
    "v_mfma_f32_16x16x32_bf16 v[68:71], a[100:103], a[224:227], v[68:71]\n"
    "buffer_load_dwordx4 v[20:23], v1, s[4:7], s79 offen offset:3072 sc1\n"
    "v_mfma_f32_16x16x32_bf16 v[64:67], a[144:147], a[240:243], v[64:67]\n"
    "ds_read_b128 v[120:123], v104 offset:1024\n"
    "v_mfma_f32_16x16x32_bf16 v[68:71], a[148:151], a[240:243], v[68:71]\n"
    "v_mfma_f32_16x16x32_bf16 v[64:67], a[12:15], a[196:199], v[64:67]\n"
    "v_mfma_f32_16x16x32_bf16 v[68:71], a[16:19], a[196:199], v[68:71]\n"
    "buffer_load_dwordx4 v[24:27], v1, s[4:7], s80 offen sc1\n"
    "v_mfma_f32_16x16x32_bf16 v[64:67], a[60:63], a[212:215], v[64:67]\n"
    "s_waitcnt lgkmcnt(1)\n"
    "v_add_f32 v116, v116, v118\n"
    "v_mfma_f32_16x16x32_bf16 v[68:71], a[64:67], a[212:215], v[68:71]\n"
    "v_add_f32 v117, v117, v119\n"
    "v_mfma_f32_16x16x32_bf16 v[64:67], a[108:111], a[228:231], v[64:67]\n"
    "s_waitcnt lgkmcnt(0)\n"
    "v_add_f32 v120, v120, v122\n"
    "v_mfma_f32_16x16x32_bf16 v[68:71], a[112:115], a[228:231], v[68:71]\n"
    "buffer_load_dwordx4 v[28:31], v1, s[4:7], s80 offen offset:1024 sc1\n"
    "v_mfma_f32_16x16x32_bf16 v[64:67], a[156:159], a[244:247], v[64:67]\n"
    "v_add_f32 v116, v116, v120\n"
    "v_mul_f32 v116, 0xbb000000, v116\n"
    "v_mfma_f32_16x16x32_bf16 v[68:71], a[160:163], a[244:247], v[68:71]\n"
    "ds_write_b32 v125, v116 offset:256\n"
    "v_mfma_f32_16x16x32_bf16 v[64:67], a[24:27], a[200:203], v[64:67]\n"
    "ds_read_b128 v[72:75], v127 offset:320\n"
    "v_mfma_f32_16x16x32_bf16 v[68:71], a[28:31], a[200:203], v[68:71]\n"
    "buffer_load_dwordx4 v[32:35], v1, s[4:7], s80 offen offset:2048 sc1\n"
    "v_mfma_f32_16x16x32_bf16 v[64:67], a[72:75], a[216:219], v[64:67]\n"
    "ds_read_b32 v101, v126 offset:384\n"
    "v_add_f32 v121, v121, v123\n"
    "v_mfma_f32_16x16x32_bf16 v[68:71], a[76:79], a[216:219], v[68:71]\n"
    "ds_read2_b32 v[102:103], v126 offset0:64 offset1:80\n"
    "v_add_f32 v117, v117, v121\n"
    "v_mfma_f32_16x16x32_bf16 v[64:67], a[120:123], a[232:235], v[64:67]\n"
    "ds_read_b128 v[120:123], v127 offset:256\n"
    "v_mfma_f32_16x16x32_bf16 v[68:71], a[124:127], a[232:235], v[68:71]\n"
    "buffer_load_dwordx4 v[36:39], v1, s[4:7], s80 offen offset:3072 sc1\n"
    "v_mfma_f32_16x16x32_bf16 v[64:67], a[168:171], a[248:251], v[64:67]\n"
    "s_waitcnt lgkmcnt(3)\n"
    "v_mul_f32 v72, v72, v100\n"
    "v_mfma_f32_16x16x32_bf16 v[68:71], a[172:175], a[248:251], v[68:71]\n"
    "v_mul_f32 v73, v73, v100\n"
    "v_mul_f32 v74, v74, v100\n"
    "v_mfma_f32_16x16x32_bf16 v[64:67], a[36:39], a[204:207], v[64:67]\n"
    "v_mul_f32 v75, v75, v100\n"
    "s_waitcnt lgkmcnt(2)\n"
    "v_mfma_f32_16x16x32_bf16 v[68:71], a[40:43], a[204:207], v[68:71]\n"
    "buffer_load_dwordx4 v[40:43], v1, s[4:7], s81 offen sc1\n"
    "v_mfma_f32_16x16x32_bf16 v[64:67], a[84:87], a[220:223], v[64:67]\n"
    "s_waitcnt lgkmcnt(1)\n"
    "v_mul_f32 v80, v101, v96\n"
    "v_mul_f32 v81, v101, v97\n"
    "v_mfma_f32_16x16x32_bf16 v[68:71], a[88:91], a[220:223], v[68:71]\n"
    "s_waitcnt lgkmcnt(0)\n"
    "v_mul_f32 v82, v101, v98\n"
    "v_mul_f32 v83, v101, v99\n"
    "v_mfma_f32_16x16x32_bf16 v[64:67], a[132:135], a[236:239], v[64:67]\n"
    "v_mul_f32 v117, 0x3b000000, v117\n"
    "v_mfma_f32_16x16x32_bf16 v[68:71], a[136:139], a[236:239], v[68:71]\n"
    "buffer_load_dwordx4 v[44:47], v1, s[4:7], s81 offen offset:1024 sc1\n"
    "v_mfma_f32_16x16x32_bf16 v[64:67], a[180:183], a[252:255], v[64:67]\n"
    "s_nop 0\n"
    "v_fma_f32 v117, -v116, v116, v117\n"
    "v_max_f32 v117, 0, v117\n"
    "v_mfma_f32_16x16x32_bf16 v[68:71], a[184:187], a[252:255], v[68:71]\n"
    "s_nop 0\n"
    "v_add_f32 v117, 0x3727c5ac, v117\n"
    "v_fmac_f32 v56, v102, v96\n"
    "v_mfma_f32_16x16x32_bf16 v[72:75], a[192:195], a[8:11], v[72:75]\n"
    "ds_read_b128 a[192:195], v3 offset:32768\n"
    "s_nop 0\n"
    "v_mfma_f32_16x16x32_bf16 v[72:75], a[208:211], a[56:59], v[72:75]\n"
    "buffer_load_dwordx4 v[48:51], v1, s[4:7], s81 offen offset:2048 sc1\n"
    "v_mfma_f32_16x16x32_bf16 v[72:75], a[224:227], a[104:107], v[72:75]\n"
    "ds_read_b128 a[208:211], v3 offset:33024\n"
    "v_fmac_f32 v64, v103, v92\n"
    "v_mfma_f32_16x16x32_bf16 v[72:75], a[240:243], a[152:155], v[72:75]\n"
    "ds_read_b128 a[224:227], v3 offset:33280\n"
    "v_fmac_f32 v68, v103, v96\n"
    "v_mfma_f32_16x16x32_bf16 v[72:75], a[196:199], a[20:23], v[72:75]\n"
    "ds_read_b128 a[240:243], v3 offset:33536\n"
    "v_mfma_f32_16x16x32_bf16 v[72:75], a[212:215], a[68:71], v[72:75]\n"
    "buffer_load_dwordx4 v[52:55], v1, s[4:7], s81 offen offset:3072 sc1\n"
    "v_mfma_f32_16x16x32_bf16 v[72:75], a[228:231], a[116:119], v[72:75]\n"
    "ds_read_b128 a[196:199], v89 offset:32768\n"
    "v_fmac_f32 v57, v102, v97\n"
    "v_mfma_f32_16x16x32_bf16 v[72:75], a[244:247], a[164:167], v[72:75]\n"
    "ds_read_b128 a[212:215], v89 offset:33024\n"
    "v_fmac_f32 v65, v103, v93\n"
    "v_mfma_f32_16x16x32_bf16 v[72:75], a[200:203], a[32:35], v[72:75]\n"
    "ds_read_b128 a[228:231], v89 offset:33280\n"
    "v_fmac_f32 v69, v103, v97\n"
    "v_mfma_f32_16x16x32_bf16 v[72:75], a[216:219], a[80:83], v[72:75]\n"
    "ds_read_b128 a[244:247], v89 offset:33536\n"
    "v_fmac_f32 v58, v102, v98\n"
    "v_mfma_f32_16x16x32_bf16 v[72:75], a[232:235], a[128:131], v[72:75]\n"
    "ds_read_b128 a[200:203], v90 offset:32768\n"
    "v_fmac_f32 v66, v103, v94\n"
    "v_mfma_f32_16x16x32_bf16 v[72:75], a[248:251], a[176:179], v[72:75]\n"
    "v_rsq_f32 v117, v117\n"
    "v_mfma_f32_16x16x32_bf16 v[72:75], a[204:207], a[44:47], v[72:75]\n"
    "ds_read_b128 a[216:219], v90 offset:33024\n"
    "v_fmac_f32 v70, v103, v98\n"
    "v_mfma_f32_16x16x32_bf16 v[72:75], a[220:223], a[92:95], v[72:75]\n"
    "ds_write_b32 v125, v117\n"
    "v_mfma_f32_16x16x32_bf16 v[72:75], a[236:239], a[140:143], v[72:75]\n"
    "ds_read2_b32 v[104:105], v126 offset0:16 offset1:32\n"
    "v_fmac_f32 v59, v102, v99\n"
    "v_mfma_f32_16x16x32_bf16 v[72:75], a[252:255], a[188:191], v[72:75]\n"
    "ds_read_b128 a[232:235], v90 offset:33280\n"
    "s_waitcnt lgkmcnt(6)\n"
    "v_mfma_f32_16x16x32_bf16 v[80:83], a[4:7], a[192:195], v[80:83]\n"
    "ds_read2_b32 v[106:107], v6 offset0:0 offset1:16\n"
    "v_fmac_f32 v67, v103, v95\n"
    "v_mfma_f32_16x16x32_bf16 v[80:83], a[52:55], a[208:211], v[80:83]\n"
    "ds_read2_b32 v[108:109], v7 offset0:2 offset1:18\n"
    "v_fmac_f32 v71, v103, v99\n"
    "v_mfma_f32_16x16x32_bf16 v[80:83], a[100:103], a[224:227], v[80:83]\n"
    "ds_read_b128 a[248:251], v90 offset:33536\n"
    "v_cvt_pk_bf16_f32 v56, v56, v57\n"
    "v_mfma_f32_16x16x32_bf16 v[80:83], a[148:151], a[240:243], v[80:83]\n"
    "ds_read_b128 a[204:207], v91 offset:32768\n"
    "v_cvt_pk_bf16_f32 v64, v64, v65\n"
    "v_mfma_f32_16x16x32_bf16 v[80:83], a[16:19], a[196:199], v[80:83]\n"
    "ds_read_b128 a[220:223], v91 offset:33024\n"
    "v_cvt_pk_bf16_f32 v68, v68, v69\n"
    "v_mfma_f32_16x16x32_bf16 v[80:83], a[64:67], a[212:215], v[80:83]\n"
    "s_waitcnt lgkmcnt(6)\n"
    "v_cvt_pk_bf16_f32 v57, v58, v59\n"
    "v_cvt_pk_bf16_f32 v65, v66, v67\n"
    "v_mfma_f32_16x16x32_bf16 v[80:83], a[112:115], a[228:231], v[80:83]\n"
    "v_cvt_pk_bf16_f32 v69, v70, v71\n"
    "v_fmac_f32 v60, v120, v100\n"
    "v_mfma_f32_16x16x16_bf16 v[116:119], v[56:57], v[64:65], 0\n"
    "s_nop 0\n"
    "v_fmac_f32 v61, v121, v100\n"
    "v_fmac_f32 v62, v122, v100\n"
    "v_mfma_f32_16x16x32_bf16 v[80:83], a[160:163], a[244:247], v[80:83]\n"
    "s_nop 0\n"
    "v_fmac_f32 v63, v123, v100\n"
    "v_mul_f32 v104, 0x3eb8aa3b, v104\n"
    "v_mfma_f32_16x16x16_bf16 v[120:123], v[68:69], v[64:65], 0\n"
    "ds_read_b128 a[236:239], v91 offset:33280\n"
    "v_cndmask_b32 v116, v116, v117, s[64:65]\n"
    "v_mfma_f32_16x16x32_bf16 v[80:83], a[28:31], a[200:203], v[80:83]\n"
    "ds_read_b128 a[252:255], v91 offset:33536\n"
    "v_cndmask_b32 v118, v118, v119, s[64:65]\n"
    "v_mfma_f32_16x16x32_bf16 v[80:83], a[76:79], a[216:219], v[80:83]\n"
    "s_waitcnt lgkmcnt(2)\n"
    "v_mul_f32 v70, v104, v106\n"
    "v_cndmask_b32 v120, v120, v121, s[64:65]\n"
    "v_cndmask_b32 v122, v122, v123, s[64:65]\n"
    "v_mfma_f32_16x16x32_bf16 v[80:83], a[124:127], a[232:235], v[80:83]\n"
    "v_mul_f32 v71, v104, v108\n"
    "v_cndmask_b32 v116, v116, v120, s[66:67]\n"
    "v_cndmask_b32 v118, v118, v122, s[68:69]\n"
    "v_mul_f32 v70, v70, v116\n"
    "v_mul_f32 v71, v71, v118\n"
    "v_mov_b32 v103, 0xceaba950\n"
    "v_cndmask_b32 v70, v103, v70, s[72:73]\n"
    "v_cndmask_b32 v71, v103, v71, s[74:75]\n"
    "v_max_f32 v120, v70, v71\n"
    "v_max_f32 v121, v70, v71\n"
    "v_mfma_f32_16x16x32_bf16 v[80:83], a[172:175], a[248:251], v[80:83]\n"
    "s_nop 0\n"
    "v_permlane16_swap_b32 v120, v121\n"
    "v_mfma_f32_16x16x32_bf16 v[80:83], a[40:43], a[204:207], v[80:83]\n"
    "v_max_f32 v122, v120, v121\n"
    "v_max_f32 v123, v120, v121\n"
    "v_mfma_f32_16x16x32_bf16 v[80:83], a[88:91], a[220:223], v[80:83]\n"
    "s_waitcnt lgkmcnt(0)\n"
    "v_permlane32_swap_b32 v122, v123\n"
    "v_max_f32 v120, v122, v123\n"
    "v_sub_f32 v70, v70, v120\n"
    "v_sub_f32 v71, v71, v120\n"
    "v_exp_f32 v70, v70\n"
    "v_exp_f32 v71, v71\n"
    "v_mfma_f32_16x16x32_bf16 v[80:83], a[136:139], a[236:239], v[80:83]\n"
    "v_mul_f32 v116, v70, v106\n"
    "v_mul_f32 v117, v71, v108\n"
    "v_cvt_pk_bf16_f32 v103, v116, v117\n"
    "v_mfma_f32_16x16x32_bf16 v[80:83], a[184:187], a[252:255], v[80:83]\n"
    "s_nop 0\n"
    "v_perm_b32 v116, v103, v103, v112\n"
    "v_perm_b32 v117, v103, v103, v113\n"
    "v_perm_b32 v118, v103, v103, v114\n"
    "v_perm_b32 v119, v103, v103, v115\n"
    "v_mul_f32 v76, v101, v92\n"
    "v_mul_f32 v77, v101, v93\n"
    "v_mul_f32 v78, v101, v94\n"
    "v_mul_f32 v79, v101, v95\n"
    "v_add_f32 v103, v70, v71\n"
    "v_cvt_pk_bf16_f32 v60, v60, v61\n"
    "v_add_f32 v71, v70, v71\n"
    "v_cvt_pk_bf16_f32 v61, v62, v63\n"
    "v_mfma_f32_16x16x32_bf16 v[76:79], a[0:3], a[192:195], v[76:79]\n"
    "v_cvt_pk_bf16_f32 v62, v72, v73\n"
    "v_cvt_pk_bf16_f32 v63, v74, v75\n"
    "v_permlane16_swap_b32 v103, v71\n"
    "v_add_f32 v70, v103, v71\n"
    "v_mfma_f32_16x16x32_bf16 v[120:123], v[60:63], v[116:119], 0\n"
    "v_add_f32 v71, v103, v71\n"
    "v_add_u32 v116, 49152, v5\n"
    "v_mfma_f32_16x16x32_bf16 v[76:79], a[48:51], a[208:211], v[76:79]\n"
    "v_permlane32_swap_b32 v70, v71\n"
    "v_mfma_f32_16x16x32_bf16 v[76:79], a[96:99], a[224:227], v[76:79]\n"
    "v_add_f32 v70, v70, v71\n"
    "v_rcp_f32 v70, v70\n"
    "v_mfma_f32_16x16x32_bf16 v[76:79], a[144:147], a[240:243], v[76:79]\n"
    "v_mul_f32 v120, v120, v70\n"
    "v_mul_f32 v121, v121, v70\n"
    "v_cvt_pk_bf16_f32 v64, v120, v121\n"
    "s_waitcnt vmcnt(9)\n"
    "v_max3_u32 v120, v8, v9, v10\n"
    "v_mfma_f32_16x16x32_bf16 v[76:79], a[12:15], a[196:199], v[76:79]\n"
    "v_max3_u32 v120, v120, v11, v12\n"
    "v_max3_u32 v120, v120, v13, v14\n"
    "v_max3_u32 v120, v120, v15, v16\n"
    "v_max3_u32 v120, v120, v17, v18\n"
    "v_mfma_f32_16x16x32_bf16 v[76:79], a[60:63], a[212:215], v[76:79]\n"
    "s_waitcnt vmcnt(8)\n"
    "v_max3_u32 v120, v120, v19, v20\n"
    "v_max3_u32 v120, v120, v21, v22\n"
    "v_max_u32 v120, v120, v23\n"
    "v_cmp_eq_u32 vcc, -1, v120\n"
    "v_mfma_f32_16x16x32_bf16 v[76:79], a[108:111], a[228:231], v[76:79]\n"
    "ds_read_b128 v[84:87], v127 offset:384\n"
    "v_xor_b32 v117, 1040, v116\n"
    "v_mfma_f32_16x16x32_bf16 v[76:79], a[156:159], a[244:247], v[76:79]\n"
    "s_cbranch_vccnz .R_9\n"
    ".R_9c:\n"
    "v_mfma_f32_16x16x32_bf16 v[76:79], a[24:27], a[200:203], v[76:79]\n"
    "s_and_b32 s79, s50, 0xc0\n"
    "v_swap_b32 v9, v12\n"
    "v_swap_b32 v10, v16\n"
    "v_swap_b32 v11, v20\n"
    "v_mfma_f32_16x16x32_bf16 v[76:79], a[72:75], a[216:219], v[76:79]\n"
    "ds_write_b128 v116, v[8:11]\n"
    "v_mfma_f32_16x16x32_bf16 v[76:79], a[120:123], a[232:235], v[76:79]\n"
    "v_swap_b32 v14, v17\n"
    "v_mfma_f32_16x16x32_bf16 v[76:79], a[168:171], a[248:251], v[76:79]\n"
    "s_waitcnt lgkmcnt(1)\n"
    "v_swap_b32 v15, v21\n"
    "v_mfma_f32_16x16x32_bf16 v[76:79], a[36:39], a[204:207], v[76:79]\n"
    "ds_write_b128 v117, v[12:15]\n"
    "v_mfma_f32_16x16x32_bf16 v[76:79], a[84:87], a[220:223], v[76:79]\n"
    "v_swap_b32 v19, v22\n"
    "v_mfma_f32_16x16x32_bf16 v[76:79], a[132:135], a[236:239], v[76:79]\n"
    "v_xor_b32 v118, 2080, v116\n"
    "v_mul_f32 v84, v84, v100\n"
    "v_mfma_f32_16x16x32_bf16 v[76:79], a[180:183], a[252:255], v[76:79]\n"
    "ds_write_b128 v118, v[16:19]\n"
    "v_mul_f32 v85, v85, v100\n"
    "v_mul_f32 v86, v86, v100\n"
    "v_mul_f32 v87, v87, v100\n"
    "v_xor_b32 v119, 3120, v116\n"
    "v_cvt_pk_bf16_f32 v80, v80, v81\n"
    "v_mfma_f32_16x16x32_bf16 v[84:87], a[200:203], a[32:35], v[84:87]\n"
    "ds_write_b128 v119, v[20:23]\n"
    "v_mfma_f32_16x16x32_bf16 v[84:87], a[216:219], a[80:83], v[84:87]\n"
    "s_waitcnt vmcnt(4)\n"
    "v_max3_u32 v120, v24, v25, v26\n"
    "v_max3_u32 v120, v120, v27, v28\n"
    "v_mfma_f32_16x16x32_bf16 v[84:87], a[232:235], a[128:131], v[84:87]\n"
    "v_max3_u32 v120, v120, v29, v30\n"
    "v_max3_u32 v120, v120, v31, v32\n"
    "v_mfma_f32_16x16x32_bf16 v[84:87], a[248:251], a[176:179], v[84:87]\n"
    "v_max3_u32 v120, v120, v33, v34\n"
    "v_max3_u32 v120, v120, v35, v36\n"
    "v_mfma_f32_16x16x32_bf16 v[84:87], a[204:207], a[44:47], v[84:87]\n"
    "v_max3_u32 v120, v120, v37, v38\n"
    "v_cvt_pk_bf16_f32 v76, v76, v77\n"
    "v_mfma_f32_16x16x32_bf16 v[84:87], a[220:223], a[92:95], v[84:87]\n"
    "s_waitcnt vmcnt(3)\n"
    "v_max3_u32 v120, v120, v39, v40\n"
    "v_max3_u32 v120, v120, v41, v42\n"
    "v_mfma_f32_16x16x32_bf16 v[84:87], a[236:239], a[140:143], v[84:87]\n"
    "s_waitcnt vmcnt(2)\n"
    "v_max3_u32 v120, v120, v43, v44\n"
    "v_max3_u32 v120, v120, v45, v46\n"
    "v_mfma_f32_16x16x32_bf16 v[84:87], a[252:255], a[188:191], v[84:87]\n"
    "s_waitcnt lgkmcnt(0)\n"
    "s_barrier\n"
    "v_mfma_f32_16x16x32_bf16 v[84:87], a[192:195], a[8:11], v[84:87]\n"
    "s_waitcnt vmcnt(1)\n"
    "v_max3_u32 v120, v120, v47, v48\n"
    "v_max3_u32 v120, v120, v49, v50\n"
    "v_mfma_f32_16x16x32_bf16 v[84:87], a[208:211], a[56:59], v[84:87]\n"
    "s_waitcnt vmcnt(0)\n"
    "v_max3_u32 v120, v120, v51, v52\n"
    "v_cvt_pk_bf16_f32 v77, v78, v79\n"
    "v_cvt_pk_bf16_f32 v81, v82, v83\n"
    "v_max3_u32 v120, v120, v53, v54\n"
    "v_max_u32 v120, v120, v55\n"
    "v_mfma_f32_16x16x32_bf16 v[84:87], a[224:227], a[104:107], v[84:87]\n"
    "ds_read_b128 a[200:203], v88\n"
    "v_cmp_eq_u32 vcc, -1, v120\n"
    "v_mfma_f32_16x16x16_bf16 v[92:95], v[68:69], v[76:77], 0\n"
    "s_nop 0\n"
    "v_mul_f32 v105, 0x3eb8aa3b, v105\n"
    "v_mul_f32 v100, v105, v107\n"
    "v_mfma_f32_16x16x16_bf16 v[96:99], v[80:81], v[76:77], 0\n"
    "s_nop 0\n"
    "v_mul_f32 v101, v105, v109\n"
    "v_mul_f32 v122, v122, v70\n"
    "v_mfma_f32_16x16x32_bf16 v[84:87], a[240:243], a[152:155], v[84:87]\n"
    "s_cbranch_vccnz .R_10\n"
    ".R_10c:\n"
    "v_mfma_f32_16x16x32_bf16 v[84:87], a[196:199], a[20:23], v[84:87]\n"
    "s_nop 0\n"
    "v_swap_b32 v25, v28\n"
    "v_swap_b32 v26, v32\n"
    "v_swap_b32 v27, v36\n"
    "v_cndmask_b32 v92, v92, v93, s[64:65]\n"
    "v_cndmask_b32 v96, v96, v97, s[64:65]\n"
    "v_cndmask_b32 v94, v94, v95, s[64:65]\n"
    "v_cndmask_b32 v98, v98, v99, s[64:65]\n"
    "v_cndmask_b32 v92, v92, v96, s[66:67]\n"
    "v_cndmask_b32 v94, v94, v98, s[68:69]\n"
    "v_mfma_f32_16x16x32_bf16 v[84:87], a[212:215], a[68:71], v[84:87]\n"
    "ds_write_b128 v116, v[24:27] offset:16384\n"
    "v_mul_f32 v100, v100, v92\n"
    "v_mul_f32 v101, v101, v94\n"
    "v_max_f32 v96, v100, v101\n"
    "v_max_f32 v97, v100, v101\n"
    "v_mfma_f32_16x16x32_bf16 v[84:87], a[228:231], a[116:119], v[84:87]\n"
    "ds_read_b128 a[216:219], v88 offset:256\n"
    "v_permlane16_swap_b32 v96, v97\n"
    "v_max_f32 v98, v96, v97\n"
    "v_max_f32 v99, v96, v97\n"
    "v_mfma_f32_16x16x32_bf16 v[84:87], a[244:247], a[164:167], v[84:87]\n"
    "s_waitcnt lgkmcnt(2)\n"
    "v_permlane32_swap_b32 v98, v99\n"
    "v_swap_b32 v30, v33\n"
    "v_swap_b32 v31, v37\n"
    "v_max_f32 v96, v98, v99\n"
    "v_mfma_f32_16x16x32_bf16 v[56:59], a[4:7], a[200:203], 0\n"
    "ds_write_b128 v117, v[28:31] offset:16384\n"
    "v_sub_f32 v100, v100, v96\n"
    "v_sub_f32 v101, v101, v96\n"
    "v_exp_f32 v100, v100\n"
    "v_exp_f32 v101, v101\n"
    "v_mfma_f32_16x16x32_bf16 v[60:63], a[200:203], a[8:11], 0\n"
    "v_mul_f32 v92, v100, v107\n"
    "v_mul_f32 v93, v101, v109\n"
    "v_cvt_pk_bf16_f32 v102, v92, v93\n"
    "v_perm_b32 v92, v102, v102, v112\n"
    "v_perm_b32 v93, v102, v102, v113\n"
    "v_perm_b32 v94, v102, v102, v114\n"
    "v_perm_b32 v95, v102, v102, v115\n"
    "v_mfma_f32_16x16x32_bf16 v[8:11], a[200:203], a[200:203], 0\n"
    "ds_read_b128 a[232:235], v88 offset:512\n"
    "v_add_f32 v102, v100, v101\n"
    "v_mul_f32 v123, v123, v70\n"
    "v_cvt_pk_bf16_f32 v72, v72, v73\n"
    "v_add_f32 v101, v100, v101\n"
    "v_mov_b32 v68, 0x3f803f80\n"
    "v_mov_b32 v69, 0x3f803f80\n"
    "v_mov_b32 v70, 0x3f803f80\n"
    "v_mov_b32 v71, 0x3f803f80\n"
    "v_cvt_pk_bf16_f32 v73, v74, v75\n"
    "v_swap_b32 v35, v38\n"
    "v_cvt_pk_bf16_f32 v74, v84, v85\n"
    "v_cvt_pk_bf16_f32 v75, v86, v87\n"
    "v_permlane16_swap_b32 v102, v101\n"
    "v_mfma_f32_16x16x32_bf16 v[20:23], v[68:71], a[200:203], 0\n"
    "ds_write_b128 v118, v[32:35] offset:16384\n"
    "v_mfma_f32_16x16x32_bf16 v[96:99], v[72:75], v[92:95], 0\n"
    "s_waitcnt lgkmcnt(2)\n"
    "v_add_f32 v100, v102, v101\n"
    "v_add_f32 v101, v102, v101\n"
    "v_mfma_f32_16x16x32_bf16 v[56:59], a[52:55], a[216:219], v[56:59]\n"
    "s_nop 0\n"
    "v_permlane32_swap_b32 v100, v101\n"
    "v_add_f32 v100, v100, v101\n"
    "v_mfma_f32_16x16x32_bf16 v[60:63], a[216:219], a[56:59], v[60:63]\n"
    "v_rcp_f32 v100, v100\n"
    "v_mfma_f32_16x16x32_bf16 v[8:11], a[216:219], a[216:219], v[8:11]\n"
    "ds_read_b128 a[248:251], v88 offset:768\n"
    "v_mul_f32 v96, v96, v100\n"
    "v_mul_f32 v97, v97, v100\n"
    "v_mul_f32 v98, v98, v100\n"
    "v_mul_f32 v99, v99, v100\n"
    "v_mfma_f32_16x16x32_bf16 v[20:23], v[68:71], a[216:219], v[20:23]\n"
    "ds_write_b128 v119, v[36:39] offset:16384\n"
    "v_cvt_pk_bf16_f32 v65, v122, v123\n"
    "v_cvt_pk_bf16_f32 v66, v96, v97\n"
    "v_cvt_pk_bf16_f32 v67, v98, v99\n"
    "v_min_u32 v64, -2, v64\n"
    "v_min_u32 v65, -2, v65\n"
    "v_min_u32 v66, -2, v66\n"
    "v_min_u32 v67, -2, v67\n"
    "v_xor_b32 v89, 64, v88\n"
    "v_permlane32_swap_b32 v64, v66\n"
    "v_permlane32_swap_b32 v65, v67\n"
    "buffer_store_dwordx4 v[64:67], v4, s[4:7], s76 offen sc1\n"
    "s_waitcnt lgkmcnt(1)\n"
    "v_mfma_f32_16x16x32_bf16 v[56:59], a[100:103], a[232:235], v[56:59]\n"
    "s_and_b32 s76, s50, 63\n"
    "v_swap_b32 v41, v44\n"
    "v_swap_b32 v42, v48\n"
    "v_swap_b32 v43, v52\n"
    "v_mfma_f32_16x16x32_bf16 v[60:63], a[232:235], a[104:107], v[60:63]\n"
    "ds_write_b128 v116, v[40:43] offset:32768\n"
    "v_mfma_f32_16x16x32_bf16 v[8:11], a[232:235], a[232:235], v[8:11]\n"
    "ds_read_b128 a[204:207], v89\n"
    "v_xor_b32 v90, 128, v88\n"
    "v_mfma_f32_16x16x32_bf16 v[20:23], v[68:71], a[232:235], v[20:23]\n"
    "s_lshl_b32 s76, s76, 14\n"
    "v_swap_b32 v46, v49\n"
    "v_swap_b32 v47, v53\n"
    "v_mfma_f32_16x16x32_bf16 v[56:59], a[148:151], a[248:251], v[56:59]\n"
    "ds_read_b128 a[220:223], v89 offset:256\n"
    "ds_write_b128 v117, v[44:47] offset:32768\n"
    "buffer_load_dwordx4 v[28:31], v0, s[96:99], s77 offen\n"
    "v_mfma_f32_16x16x32_bf16 v[60:63], a[248:251], a[152:155], v[60:63]\n"
    "ds_read_b128 a[236:239], v89 offset:512\n"
    "v_xor_b32 v91, 192, v88\n"
    "v_mfma_f32_16x16x32_bf16 v[8:11], a[248:251], a[248:251], v[8:11]\n"
    "s_lshl2_add_u32 s80, s79, s76\n"
    "v_swap_b32 v51, v54\n"
    "v_mfma_f32_16x16x32_bf16 v[20:23], v[68:71], a[248:251], v[20:23]\n"
    "ds_write_b128 v118, v[48:51] offset:32768\n"
    "s_waitcnt lgkmcnt(4)\n"
    "v_mfma_f32_16x16x32_bf16 v[56:59], a[16:19], a[204:207], v[56:59]\n"
    "ds_read_b128 a[252:255], v89 offset:768\n"
    "v_mfma_f32_16x16x32_bf16 v[60:63], a[204:207], a[20:23], v[60:63]\n"
    "ds_read_b128 a[200:203], v90\n"
    "s_waitcnt lgkmcnt(4)\n"
    "v_mfma_f32_16x16x32_bf16 v[56:59], a[64:67], a[220:223], v[56:59]\n"
    "ds_write_b128 v119, v[52:55] offset:32768\n"
    "v_mfma_f32_16x16x32_bf16 v[60:63], a[220:223], a[68:71], v[60:63]\n"
    "s_waitcnt lgkmcnt(4)\n"
    "v_cndmask_b32 v8, v8, v9, s[64:65]\n"
    "v_mfma_f32_16x16x32_bf16 v[56:59], a[112:115], a[236:239], v[56:59]\n"
    "s_lshl_b32 s79, s79, 6\n"
    "ds_read_b128 a[216:219], v90 offset:256\n"
    "v_mfma_f32_16x16x32_bf16 v[60:63], a[236:239], a[116:119], v[60:63]\n"
    "s_add_u32 s79, s79, s76\n"
    "ds_read_b128 a[232:235], v90 offset:512\n"
    "v_cndmask_b32 v10, v10, v11, s[64:65]\n"
    "ds_read_b128 a[248:251], v90 offset:768\n"
    "s_waitcnt lgkmcnt(5)\n"
    "v_mfma_f32_16x16x32_bf16 v[56:59], a[160:163], a[252:255], v[56:59]\n"
    "ds_read_b128 a[204:207], v91\n"
    "v_cndmask_b32 v21, v8, v10, s[70:71]\n"
    "v_mfma_f32_16x16x32_bf16 v[60:63], a[252:255], a[164:167], v[60:63]\n"
    "ds_read_b128 a[220:223], v91 offset:256\n"
    "s_waitcnt lgkmcnt(6)\n"
    "v_mfma_f32_16x16x32_bf16 v[56:59], a[28:31], a[200:203], v[56:59]\n"
    "s_add_u32 s79, s79, 0xa32000\n"
    "s_waitcnt lgkmcnt(5)\n"
    "s_barrier\n"
    "v_mfma_f32_16x16x32_bf16 v[60:63], a[200:203], a[32:35], v[60:63]\n"
    "ds_read_b128 a[192:195], v88 offset:16384\n"
    "s_waitcnt lgkmcnt(5)\n"
    "v_mfma_f32_16x16x32_bf16 v[56:59], a[76:79], a[216:219], v[56:59]\n"
    "ds_read_b128 a[196:199], v88 offset:32768\n"
    "v_add_u32 v104, 147456, v1\n"
    "v_mfma_f32_16x16x32_bf16 v[60:63], a[216:219], a[80:83], v[60:63]\n"
    "ds_read_b128 a[208:211], v88 offset:16640\n"
    "s_waitcnt lgkmcnt(6)\n"
    "v_mfma_f32_16x16x32_bf16 v[56:59], a[124:127], a[232:235], v[56:59]\n"
    "ds_read_b128 a[212:215], v88 offset:33024\n"
    "v_mov_b32 v24, -1\n"
    "v_mfma_f32_16x16x32_bf16 v[60:63], a[232:235], a[128:131], v[60:63]\n"
    "ds_read_b128 a[224:227], v88 offset:16896\n"
    "s_waitcnt lgkmcnt(6)\n"
    "v_mfma_f32_16x16x32_bf16 v[56:59], a[172:175], a[248:251], v[56:59]\n"
    "ds_read_b128 a[228:231], v88 offset:33280\n"
    "v_mov_b32 v25, -1\n"
    "v_mfma_f32_16x16x32_bf16 v[60:63], a[248:251], a[176:179], v[60:63]\n"
    "ds_read_b128 a[240:243], v88 offset:17152\n"
    "s_waitcnt lgkmcnt(6)\n"
    "v_mfma_f32_16x16x32_bf16 v[12:15], a[192:195], a[192:195], 0\n"
    "ds_read_b128 a[244:247], v88 offset:33536\n"
    "v_mov_b32 v26, -1\n"
    "v_mfma_f32_16x16x32_bf16 v[76:79], v[68:71], a[192:195], 0\n"
    "ds_read_b128 a[236:239], v91 offset:512\n"
    "s_waitcnt lgkmcnt(7)\n"
    "v_mfma_f32_16x16x32_bf16 v[16:19], a[196:199], a[196:199], 0\n"
    "ds_read_b128 a[252:255], v91 offset:768\n"
    "v_mov_b32 v27, -1\n"
    "v_mfma_f32_16x16x32_bf16 v[80:83], v[68:71], a[196:199], 0\n"
    "ds_read_b128 a[196:199], v89 offset:16384\n"
    "s_waitcnt lgkmcnt(7)\n"
    "v_mfma_f32_16x16x32_bf16 v[12:15], a[208:211], a[208:211], v[12:15]\n"
    "ds_write_b64 v124, v[20:21]\n"
    "v_mfma_f32_16x16x32_bf16 v[76:79], v[68:71], a[208:211], v[76:79]\n"
    "ds_read_b128 a[200:203], v90 offset:16384\n"
    "s_add_u32 s77, s37, 0x100000\n"
    "v_mfma_f32_16x16x32_bf16 v[16:19], a[212:215], a[212:215], v[16:19]\n"
    "ds_read_b128 a[216:219], v90 offset:16640\n"
    "v_mfma_f32_16x16x32_bf16 v[80:83], v[68:71], a[212:215], v[80:83]\n"
    "ds_read_b128 a[212:215], v89 offset:16640\n"
    "s_waitcnt lgkmcnt(6)\n"
    "v_mfma_f32_16x16x32_bf16 v[12:15], a[224:227], a[224:227], v[12:15]\n"
    "ds_read_b128 a[232:235], v90 offset:16896\n"
    "v_mfma_f32_16x16x32_bf16 v[76:79], v[68:71], a[224:227], v[76:79]\n"
    "ds_read_b128 a[248:251], v90 offset:17152\n"
    "v_mfma_f32_16x16x32_bf16 v[16:19], a[228:231], a[228:231], v[16:19]\n"
    "ds_read_b32 v100, v126 offset:640\n"
    "v_mfma_f32_16x16x32_bf16 v[80:83], v[68:71], a[228:231], v[80:83]\n"
    "ds_read_b128 a[228:231], v89 offset:16896\n"
    "v_mfma_f32_16x16x32_bf16 v[12:15], a[240:243], a[240:243], v[12:15]\n"
    "ds_read_b128 v[96:99], v127 offset:576\n"
    "s_nop 0\n"
    "v_mfma_f32_16x16x32_bf16 v[76:79], v[68:71], a[240:243], v[76:79]\n"
    "ds_read_b128 v[92:95], v127 offset:512\n"
    "s_nop 0\n"
    "v_mfma_f32_16x16x32_bf16 v[16:19], a[244:247], a[244:247], v[16:19]\n"
    "s_nop 1\n"
    "v_cndmask_b32 v12, v12, v13, s[64:65]\n"
    "v_mfma_f32_16x16x32_bf16 v[80:83], v[68:71], a[244:247], v[80:83]\n"
    "ds_read_b128 a[244:247], v89 offset:17152\n"
    "v_cndmask_b32 v14, v14, v15, s[64:65]\n"
    "v_mfma_f32_16x16x32_bf16 v[56:59], a[40:43], a[204:207], v[56:59]\n"
    "v_cndmask_b32 v77, v12, v14, s[70:71]\n"
    "v_cndmask_b32 v16, v16, v17, s[64:65]\n"
    "v_mfma_f32_16x16x32_bf16 v[60:63], a[204:207], a[44:47], v[60:63]\n"
    "ds_write_b64 v124, v[76:77] offset:256\n"
    "v_mfma_f32_16x16x32_bf16 v[56:59], a[88:91], a[220:223], v[56:59]\n"
    "v_cndmask_b32 v18, v18, v19, s[64:65]\n"
    "v_cndmask_b32 v81, v16, v18, s[70:71]\n"
    "v_mfma_f32_16x16x32_bf16 v[60:63], a[220:223], a[92:95], v[60:63]\n"
    "ds_write_b64 v124, v[80:81] offset:512\n"
    "v_mfma_f32_16x16x32_bf16 v[56:59], a[136:139], a[236:239], v[56:59]\n"
    "ds_read_b128 a[204:207], v91 offset:16384\n"
    "v_mfma_f32_16x16x32_bf16 v[60:63], a[236:239], a[140:143], v[60:63]\n"
    "ds_read_b128 a[220:223], v91 offset:16640\n"
    "s_waitcnt lgkmcnt(4)\n"
    "v_mfma_f32_16x16x32_bf16 v[56:59], a[184:187], a[252:255], v[56:59]\n"
    "ds_read_b128 a[236:239], v91 offset:16896\n"
    "v_mfma_f32_16x16x32_bf16 v[60:63], a[252:255], a[188:191], v[60:63]\n"
    "ds_read_b128 a[252:255], v91 offset:17152\n"
    "v_mfma_f32_16x16x32_bf16 v[64:67], a[0:3], a[192:195], 0\n"
    "s_waitcnt lgkmcnt(4)\n"
    "s_barrier\n"
    "v_mfma_f32_16x16x32_bf16 v[68:71], a[4:7], a[192:195], 0\n"
    "ds_read_b128 v[116:119], v104\n"
    "v_mfma_f32_16x16x32_bf16 v[64:67], a[48:51], a[208:211], v[64:67]\n"
    "ds_read_b128 v[120:123], v104 offset:1024\n"
    "v_mfma_f32_16x16x32_bf16 v[68:71], a[52:55], a[208:211], v[68:71]\n"
    "s_waitcnt vmcnt(0)\n"
    "buffer_store_dwordx4 v[24:27], v30, s[4:7], s88 offen sc1\n"
    "v_mfma_f32_16x16x32_bf16 v[64:67], a[96:99], a[224:227], v[64:67]\n"
    "s_cmp_lt_u32 s53, 2\n"
    "s_cselect_b32 s88, s77, 0x40000000\n"
    "s_cselect_b32 s37, 0x40000000, s77\n"
    "v_mfma_f32_16x16x32_bf16 v[68:71], a[100:103], a[224:227], v[68:71]\n"
    "v_mfma_f32_16x16x32_bf16 v[64:67], a[144:147], a[240:243], v[64:67]\n"
    "v_mfma_f32_16x16x32_bf16 v[68:71], a[148:151], a[240:243], v[68:71]\n"
    "s_waitcnt lgkmcnt(1)\n"
    "v_add_f32 v116, v116, v118\n"
    "v_add_f32 v117, v117, v119\n"
    "v_mfma_f32_16x16x32_bf16 v[64:67], a[12:15], a[196:199], v[64:67]\n"
    "s_waitcnt lgkmcnt(0)\n"
    "v_add_f32 v120, v120, v122\n"
    "v_add_f32 v116, v116, v120\n"
    "v_mfma_f32_16x16x32_bf16 v[68:71], a[16:19], a[196:199], v[68:71]\n"
    "v_mul_f32 v116, 0xbb000000, v116\n"
    "v_add_f32 v121, v121, v123\n"
    "v_mfma_f32_16x16x32_bf16 v[64:67], a[60:63], a[212:215], v[64:67]\n"
    "ds_write_b32 v125, v116 offset:256\n"
    "v_mfma_f32_16x16x32_bf16 v[68:71], a[64:67], a[212:215], v[68:71]\n"
    "ds_read_b128 v[72:75], v127 offset:320\n"
    "v_add_f32 v117, v117, v121\n"
    "v_mfma_f32_16x16x32_bf16 v[64:67], a[108:111], a[228:231], v[64:67]\n"
    "ds_read_b32 v101, v126 offset:384\n"
    "v_mul_f32 v117, 0x3b000000, v117\n"
    "v_mfma_f32_16x16x32_bf16 v[68:71], a[112:115], a[228:231], v[68:71]\n"
    "ds_read2_b32 v[102:103], v126 offset0:64 offset1:80\n"
    "v_fma_f32 v117, -v116, v116, v117\n"
    "v_mfma_f32_16x16x32_bf16 v[64:67], a[156:159], a[244:247], v[64:67]\n"
    "v_max_f32 v117, 0, v117\n"
    "v_add_f32 v117, 0x3727c5ac, v117\n"
    "v_mfma_f32_16x16x32_bf16 v[68:71], a[160:163], a[244:247], v[68:71]\n"
    "v_rsq_f32 v117, v117\n"
    "v_mfma_f32_16x16x32_bf16 v[64:67], a[24:27], a[200:203], v[64:67]\n"
    "ds_write_b32 v125, v117\n"
    "v_mfma_f32_16x16x32_bf16 v[68:71], a[28:31], a[200:203], v[68:71]\n"
    "s_waitcnt lgkmcnt(3)\n"
    "v_mul_f32 v72, v72, v100\n"
    "v_mfma_f32_16x16x32_bf16 v[64:67], a[72:75], a[216:219], v[64:67]\n"
    "v_mul_f32 v73, v73, v100\n"
    "v_mul_f32 v74, v74, v100\n"
    "v_mfma_f32_16x16x32_bf16 v[68:71], a[76:79], a[216:219], v[68:71]\n"
    "buffer_load_dwordx4 v[8:11], v1, s[4:7], s46 offen sc1\n"
    "v_mul_f32 v75, v75, v100\n"
    "v_mfma_f32_16x16x32_bf16 v[64:67], a[120:123], a[232:235], v[64:67]\n"
    "s_waitcnt lgkmcnt(1)\n"
    "v_mul_f32 v80, v101, v96\n"
    "v_mul_f32 v81, v101, v97\n"
    "v_mfma_f32_16x16x32_bf16 v[68:71], a[124:127], a[232:235], v[68:71]\n"
    "v_mul_f32 v82, v101, v98\n"
    "v_mul_f32 v83, v101, v99\n"
    "v_mfma_f32_16x16x32_bf16 v[64:67], a[168:171], a[248:251], v[64:67]\n"
    "v_fmac_f32 v56, v102, v96\n"
    "v_fmac_f32 v57, v102, v97\n"
    "v_mfma_f32_16x16x32_bf16 v[68:71], a[172:175], a[248:251], v[68:71]\n"
    "buffer_load_dwordx4 v[12:15], v1, s[4:7], s47 offen sc1\n"
    "v_fmac_f32 v58, v102, v98\n"
    "v_mfma_f32_16x16x32_bf16 v[64:67], a[36:39], a[204:207], v[64:67]\n"
    "v_fmac_f32 v59, v102, v99\n"
    "v_cvt_pk_bf16_f32 v56, v56, v57\n"
    "v_mfma_f32_16x16x32_bf16 v[68:71], a[40:43], a[204:207], v[68:71]\n"
    "v_cvt_pk_bf16_f32 v57, v58, v59\n"
    "v_mul_f32 v76, v101, v92\n"
    "v_mfma_f32_16x16x32_bf16 v[64:67], a[84:87], a[220:223], v[64:67]\n"
    "v_mul_f32 v77, v101, v93\n"
    "v_mul_f32 v78, v101, v94\n"
    "v_mfma_f32_16x16x32_bf16 v[68:71], a[88:91], a[220:223], v[68:71]\n"
    "buffer_load_dwordx4 v[16:19], v1, s[4:7], s48 offen sc1\n"
    "v_mul_f32 v79, v101, v95\n"
    "v_mfma_f32_16x16x32_bf16 v[64:67], a[132:135], a[236:239], v[64:67]\n"
    "ds_read_b128 v[84:87], v127 offset:384\n"
    "v_mfma_f32_16x16x32_bf16 v[68:71], a[136:139], a[236:239], v[68:71]\n"
    "ds_read_b128 v[120:123], v127 offset:256\n"
    "v_mfma_f32_16x16x32_bf16 v[64:67], a[180:183], a[252:255], v[64:67]\n"
    "ds_read2_b32 v[104:105], v126 offset0:16 offset1:32\n"
    "s_nop 0\n"
    "v_mfma_f32_16x16x32_bf16 v[68:71], a[184:187], a[252:255], v[68:71]\n"
    "buffer_load_dwordx4 v[20:23], v1, s[4:7], s49 offen sc1\n"
    "s_nop 0\n"
    "v_mfma_f32_16x16x32_bf16 v[72:75], a[192:195], a[8:11], v[72:75]\n"
    "ds_read_b128 a[192:195], v88 offset:32768\n"
    "s_nop 0\n"
    "v_mfma_f32_16x16x32_bf16 v[72:75], a[208:211], a[56:59], v[72:75]\n"
    "ds_read_b128 a[208:211], v88 offset:33024\n"
    "v_fmac_f32 v64, v103, v92\n"
    "v_mfma_f32_16x16x32_bf16 v[72:75], a[224:227], a[104:107], v[72:75]\n"
    "ds_read_b128 a[224:227], v88 offset:33280\n"
    "v_fmac_f32 v65, v103, v93\n"
    "v_mfma_f32_16x16x32_bf16 v[72:75], a[240:243], a[152:155], v[72:75]\n"
    "ds_read_b128 a[240:243], v88 offset:33536\n"
    "v_fmac_f32 v66, v103, v94\n"
    "v_mfma_f32_16x16x32_bf16 v[72:75], a[196:199], a[20:23], v[72:75]\n"
    "ds_read_b128 a[196:199], v89 offset:32768\n"
    "v_fmac_f32 v67, v103, v95\n"
    "v_mfma_f32_16x16x32_bf16 v[72:75], a[212:215], a[68:71], v[72:75]\n"
    "ds_read_b128 a[212:215], v89 offset:33024\n"
    "v_cvt_pk_bf16_f32 v64, v64, v65\n"
    "v_mfma_f32_16x16x32_bf16 v[72:75], a[228:231], a[116:119], v[72:75]\n"
    "ds_read_b128 a[228:231], v89 offset:33280\n"
    "v_cvt_pk_bf16_f32 v65, v66, v67\n"
    "v_mfma_f32_16x16x32_bf16 v[72:75], a[244:247], a[164:167], v[72:75]\n"
    "ds_read_b128 a[244:247], v89 offset:33536\n"
    "v_fmac_f32 v68, v103, v96\n"
    "v_mfma_f32_16x16x16_bf16 v[116:119], v[56:57], v[64:65], 0\n"
    "buffer_load_dwordx4 v[56:59], v0, s[96:99], s91 offen\n"
    "v_fmac_f32 v69, v103, v97\n"
    "v_mfma_f32_16x16x32_bf16 v[72:75], a[200:203], a[32:35], v[72:75]\n"
    "ds_read_b128 a[200:203], v90 offset:32768\n"
    "v_fmac_f32 v70, v103, v98\n"
    "v_mfma_f32_16x16x32_bf16 v[72:75], a[216:219], a[80:83], v[72:75]\n"
    "ds_read_b128 a[216:219], v90 offset:33024\n"
    "v_fmac_f32 v71, v103, v99\n"
    "v_mfma_f32_16x16x32_bf16 v[72:75], a[232:235], a[128:131], v[72:75]\n"
    "ds_read_b128 a[232:235], v90 offset:33280\n"
    "v_cvt_pk_bf16_f32 v68, v68, v69\n"
    "v_mfma_f32_16x16x32_bf16 v[72:75], a[248:251], a[176:179], v[72:75]\n"
    "ds_read_b128 a[248:251], v90 offset:33536\n"
    "v_cvt_pk_bf16_f32 v69, v70, v71\n"
    "v_mfma_f32_16x16x32_bf16 v[72:75], a[204:207], a[44:47], v[72:75]\n"
    "ds_read_b128 a[204:207], v91 offset:32768\n"
    "v_cndmask_b32 v116, v116, v117, s[64:65]\n"
    "v_mfma_f32_16x16x32_bf16 v[72:75], a[220:223], a[92:95], v[72:75]\n"
    "ds_read_b128 a[220:223], v91 offset:33024\n"
    "v_cndmask_b32 v118, v118, v119, s[64:65]\n"
    "v_mfma_f32_16x16x32_bf16 v[72:75], a[236:239], a[140:143], v[72:75]\n"
    "ds_read_b128 a[236:239], v91 offset:33280\n"
    "s_waitcnt lgkmcnt(7)\n"
    "v_mfma_f32_16x16x32_bf16 v[72:75], a[252:255], a[188:191], v[72:75]\n"
    "ds_read_b128 a[252:255], v91 offset:33536\n"
    "v_mul_f32 v84, v84, v100\n"
    "v_mfma_f32_16x16x32_bf16 v[80:83], a[4:7], a[192:195], v[80:83]\n"
    "buffer_store_dwordx4 v[24:27], v31, s[4:7], s89 offen sc1\n"
    "v_mfma_f32_16x16x32_bf16 v[80:83], a[52:55], a[208:211], v[80:83]\n"
    "ds_read2_b32 v[106:107], v6 offset0:0 offset1:16\n"
    "v_mul_f32 v85, v85, v100\n"
    "v_mfma_f32_16x16x32_bf16 v[80:83], a[100:103], a[224:227], v[80:83]\n"
    "ds_read2_b32 v[108:109], v7 offset0:2 offset1:18\n"
    "v_mul_f32 v86, v86, v100\n"
    "v_mfma_f32_16x16x32_bf16 v[80:83], a[148:151], a[240:243], v[80:83]\n"
    "v_mul_f32 v87, v87, v100\n"
    "v_fmac_f32 v60, v120, v100\n"
    "v_mfma_f32_16x16x32_bf16 v[80:83], a[16:19], a[196:199], v[80:83]\n"
    "v_fmac_f32 v61, v121, v100\n"
    "v_fmac_f32 v62, v122, v100\n"
    "v_mfma_f32_16x16x32_bf16 v[80:83], a[64:67], a[212:215], v[80:83]\n"
    "v_fmac_f32 v63, v123, v100\n"
    "v_mul_f32 v104, 0x3eb8aa3b, v104\n"
    "v_mfma_f32_16x16x16_bf16 v[120:123], v[68:69], v[64:65], 0\n"
    "s_nop 0\n"
    "v_cvt_pk_bf16_f32 v60, v60, v61\n"
    "v_cvt_pk_bf16_f32 v61, v62, v63\n"
    "v_mfma_f32_16x16x32_bf16 v[80:83], a[112:115], a[228:231], v[80:83]\n"
    "s_nop 0\n"
    "s_waitcnt lgkmcnt(1)\n"
    "v_mul_f32 v70, v104, v106\n"
    "v_mfma_f32_16x16x32_bf16 v[80:83], a[160:163], a[244:247], v[80:83]\n"
    "v_cndmask_b32 v120, v120, v121, s[64:65]\n"
    "v_cndmask_b32 v122, v122, v123, s[64:65]\n"
    "v_mfma_f32_16x16x32_bf16 v[80:83], a[28:31], a[200:203], v[80:83]\n"
    "s_waitcnt lgkmcnt(0)\n"
    "v_mul_f32 v71, v104, v108\n"
    "v_cndmask_b32 v116, v116, v120, s[66:67]\n"
    "v_mfma_f32_16x16x32_bf16 v[80:83], a[76:79], a[216:219], v[80:83]\n"
    "v_cndmask_b32 v118, v118, v122, s[68:69]\n"
    "v_mul_f32 v70, v70, v116\n"
    "v_mfma_f32_16x16x32_bf16 v[80:83], a[124:127], a[232:235], v[80:83]\n"
    "v_mul_f32 v71, v71, v118\n"
    "v_max_f32 v120, v70, v71\n"
    "v_mfma_f32_16x16x32_bf16 v[80:83], a[172:175], a[248:251], v[80:83]\n"
    "v_max_f32 v121, v70, v71\n"
    "v_cvt_pk_bf16_f32 v62, v72, v73\n"
    "v_mfma_f32_16x16x32_bf16 v[80:83], a[40:43], a[204:207], v[80:83]\n"
    "v_permlane16_swap_b32 v120, v121\n"
    "v_mfma_f32_16x16x32_bf16 v[80:83], a[88:91], a[220:223], v[80:83]\n"
    "v_max_f32 v122, v120, v121\n"
    "v_max_f32 v123, v120, v121\n"
    "v_mfma_f32_16x16x32_bf16 v[80:83], a[136:139], a[236:239], v[80:83]\n"
    "s_nop 0\n"
    "v_permlane32_swap_b32 v122, v123\n"
    "v_mfma_f32_16x16x32_bf16 v[80:83], a[184:187], a[252:255], v[80:83]\n"
    "s_nop 0\n"
    "v_max_f32 v120, v122, v123\n"
    "v_sub_f32 v70, v70, v120\n"
    "v_mfma_f32_16x16x32_bf16 v[76:79], a[0:3], a[192:195], v[76:79]\n"
    "s_nop 0\n"
    "v_sub_f32 v71, v71, v120\n"
    "v_cvt_pk_bf16_f32 v63, v74, v75\n"
    "v_mfma_f32_16x16x32_bf16 v[76:79], a[48:51], a[208:211], v[76:79]\n"
    "v_exp_f32 v70, v70\n"
    "v_mfma_f32_16x16x32_bf16 v[76:79], a[96:99], a[224:227], v[76:79]\n"
    "v_exp_f32 v71, v71\n"
    "v_mfma_f32_16x16x32_bf16 v[76:79], a[144:147], a[240:243], v[76:79]\n"
    "v_mul_f32 v116, v70, v106\n"
    "v_mul_f32 v117, v71, v108\n"
    "v_mfma_f32_16x16x32_bf16 v[76:79], a[12:15], a[196:199], v[76:79]\n"
    "v_cvt_pk_bf16_f32 v103, v116, v117\n"
    "v_perm_b32 v116, v103, v103, v112\n"
    "v_mfma_f32_16x16x32_bf16 v[76:79], a[60:63], a[212:215], v[76:79]\n"
    "v_perm_b32 v117, v103, v103, v113\n"
    "v_perm_b32 v118, v103, v103, v114\n"
    "v_mfma_f32_16x16x32_bf16 v[76:79], a[108:111], a[228:231], v[76:79]\n"
    "v_perm_b32 v119, v103, v103, v115\n"
    "v_cvt_pk_bf16_f32 v80, v80, v81\n"
    "v_mfma_f32_16x16x32_bf16 v[76:79], a[156:159], a[244:247], v[76:79]\n"
    "v_cvt_pk_bf16_f32 v81, v82, v83\n"
    "v_mul_f32 v105, 0x3eb8aa3b, v105\n"
    "v_mfma_f32_16x16x32_bf16 v[120:123], v[60:63], v[116:119], 0\n"
    "buffer_load_dwordx4 v[60:63], v0, s[96:99], s92 offen\n"
    "v_mul_f32 v100, v105, v107\n"
    "v_mfma_f32_16x16x32_bf16 v[76:79], a[24:27], a[200:203], v[76:79]\n"
    "v_mul_f32 v101, v105, v109\n"
    "v_add_f32 v103, v70, v71\n"
    "v_mfma_f32_16x16x32_bf16 v[76:79], a[72:75], a[216:219], v[76:79]\n"
    "v_cvt_pk_bf16_f32 v72, v72, v73\n"
    "v_add_f32 v71, v70, v71\n"
    "v_mfma_f32_16x16x32_bf16 v[76:79], a[120:123], a[232:235], v[76:79]\n"
    "v_cvt_pk_bf16_f32 v73, v74, v75\n"
    "v_mfma_f32_16x16x32_bf16 v[76:79], a[168:171], a[248:251], v[76:79]\n"
    "s_waitcnt vmcnt(6)\n"
    "v_max3_u32 v90, v8, v9, v10\n"
    "v_mfma_f32_16x16x32_bf16 v[76:79], a[36:39], a[204:207], v[76:79]\n"
    "s_waitcnt vmcnt(5)\n"
    "v_max3_u32 v90, v90, v11, v12\n"
    "v_mfma_f32_16x16x32_bf16 v[76:79], a[84:87], a[220:223], v[76:79]\n"
    "v_max3_u32 v90, v90, v13, v14\n"
    "v_mfma_f32_16x16x32_bf16 v[76:79], a[132:135], a[236:239], v[76:79]\n"
    "v_permlane16_swap_b32 v103, v71\n"
    "v_mfma_f32_16x16x32_bf16 v[76:79], a[180:183], a[252:255], v[76:79]\n"
    "s_nop 0\n"
    "v_add_f32 v70, v103, v71\n"
    "v_add_f32 v71, v103, v71\n"
    "v_mfma_f32_16x16x32_bf16 v[84:87], a[200:203], a[32:35], v[84:87]\n"
    "s_nop 0\n"
    "s_waitcnt vmcnt(4)\n"
    "v_max3_u32 v90, v90, v15, v16\n"
    "v_mfma_f32_16x16x32_bf16 v[84:87], a[216:219], a[80:83], v[84:87]\n"
    "v_cvt_pk_bf16_f32 v76, v76, v77\n"
    "v_cvt_pk_bf16_f32 v77, v78, v79\n"
    "v_mfma_f32_16x16x32_bf16 v[84:87], a[232:235], a[128:131], v[84:87]\n"
    "v_max3_u32 v90, v90, v17, v18\n"
    "v_mfma_f32_16x16x16_bf16 v[92:95], v[68:69], v[76:77], 0\n"
    "s_nop 0\n"
    "v_permlane32_swap_b32 v70, v71\n"
    "v_mfma_f32_16x16x16_bf16 v[96:99], v[80:81], v[76:77], 0\n"
    "s_nop 0\n"
    "s_waitcnt vmcnt(3)\n"
    "v_max3_u32 v90, v90, v19, v20\n"
    "v_mfma_f32_16x16x32_bf16 v[84:87], a[248:251], a[176:179], v[84:87]\n"
    "s_nop 0\n"
    "v_max3_u32 v90, v90, v21, v22\n"
    "v_max_u32 v90, v90, v23\n"
    "v_mfma_f32_16x16x32_bf16 v[84:87], a[204:207], a[44:47], v[84:87]\n"
    "v_cmp_eq_u32 vcc, -1, v90\n"
    "v_cndmask_b32 v92, v92, v93, s[64:65]\n"
    "v_mfma_f32_16x16x32_bf16 v[84:87], a[220:223], a[92:95], v[84:87]\n"
    "v_cndmask_b32 v96, v96, v97, s[64:65]\n"
    "v_cndmask_b32 v94, v94, v95, s[64:65]\n"
    "v_mfma_f32_16x16x32_bf16 v[84:87], a[236:239], a[140:143], v[84:87]\n"
    "s_cbranch_vccnz .R_11\n"
    ".R_11c:\n"
    "v_mfma_f32_16x16x32_bf16 v[84:87], a[252:255], a[188:191], v[84:87]\n"
    "v_cndmask_b32 v98, v98, v99, s[64:65]\n"
    "v_cndmask_b32 v92, v92, v96, s[66:67]\n"
    "v_mfma_f32_16x16x32_bf16 v[84:87], a[192:195], a[8:11], v[84:87]\n"
    "v_cndmask_b32 v94, v94, v98, s[68:69]\n"
    "v_mul_f32 v100, v100, v92\n"
    "v_mfma_f32_16x16x32_bf16 v[84:87], a[208:211], a[56:59], v[84:87]\n"
    "v_mul_f32 v101, v101, v94\n"
    "v_max_f32 v96, v100, v101\n"
    "v_mfma_f32_16x16x32_bf16 v[84:87], a[224:227], a[104:107], v[84:87]\n"
    "v_max_f32 v97, v100, v101\n"
    "v_add_f32 v70, v70, v71\n"
    "v_mfma_f32_16x16x32_bf16 v[84:87], a[240:243], a[152:155], v[84:87]\n"
    "v_permlane16_swap_b32 v96, v97\n"
    "v_mfma_f32_16x16x32_bf16 v[84:87], a[196:199], a[20:23], v[84:87]\n"
    "v_max_f32 v98, v96, v97\n"
    "v_max_f32 v99, v96, v97\n"
    "v_mfma_f32_16x16x32_bf16 v[84:87], a[212:215], a[68:71], v[84:87]\n"
    "s_nop 0\n"
    "v_permlane32_swap_b32 v98, v99\n"
    "v_mfma_f32_16x16x32_bf16 v[84:87], a[228:231], a[116:119], v[84:87]\n"
    "v_max_f32 v96, v98, v99\n"
    "v_sub_f32 v100, v100, v96\n"
    "v_mfma_f32_16x16x32_bf16 v[84:87], a[244:247], a[164:167], v[84:87]\n"
    "s_nop 0\n"
    "v_sub_f32 v101, v101, v96\n"
    "s_add_u32 s46, s82, 0x300000\n"
    "v_mfma_f32_16x16x32_bf16 v[24:27], v[8:11], v[128:131], 0\n"
    "s_nop 0\n"
    "v_exp_f32 v100, v100\n"
    "v_mfma_f32_16x16x32_bf16 v[24:27], v[12:15], v[160:163], v[24:27]\n"
    "v_exp_f32 v101, v101\n"
    "v_mfma_f32_16x16x32_bf16 v[24:27], v[16:19], v[192:195], v[24:27]\n"
    "v_mul_f32 v92, v100, v107\n"
    "v_mul_f32 v93, v101, v109\n"
    "v_mfma_f32_16x16x32_bf16 v[24:27], v[20:23], v[224:227], v[24:27]\n"
    "s_xor_b32 s47, s46, 1024\n"
    "v_cvt_pk_bf16_f32 v102, v92, v93\n"
    "v_perm_b32 v92, v102, v102, v112\n"
    "v_perm_b32 v93, v102, v102, v113\n"
    "v_perm_b32 v94, v102, v102, v114\n"
    "v_perm_b32 v95, v102, v102, v115\n"
    "v_mfma_f32_16x16x32_bf16 v[28:31], v[8:11], v[132:135], 0\n"
    "s_xor_b32 s48, s46, 2048\n"
    "v_add_f32 v102, v100, v101\n"
    "v_add_f32 v101, v100, v101\n"
    "v_cvt_pk_bf16_f32 v74, v84, v85\n"
    "v_cvt_pk_bf16_f32 v75, v86, v87\n"
    "v_mfma_f32_16x16x32_bf16 v[28:31], v[12:15], v[164:167], v[28:31]\n"
    "s_xor_b32 s49, s46, 3072\n"
    "v_permlane16_swap_b32 v102, v101\n"
    "v_mfma_f32_16x16x32_bf16 v[96:99], v[72:75], v[92:95], 0\n"
    "v_add_f32 v100, v102, v101\n"
    "v_add_f32 v101, v102, v101\n"
    "v_mfma_f32_16x16x32_bf16 v[28:31], v[16:19], v[196:199], v[28:31]\n"
    "s_nop 0\n"
    "v_permlane32_swap_b32 v100, v101\n"
    "v_mfma_f32_16x16x32_bf16 v[28:31], v[20:23], v[228:231], v[28:31]\n"
    "v_add_f32 v100, v100, v101\n"
    "v_rcp_f32 v70, v70\n"
    "v_rcp_f32 v100, v100\n"
    "v_mfma_f32_16x16x32_bf16 v[32:35], v[8:11], v[136:139], 0\n"
    "v_mul_f32 v120, v120, v70\n"
    "v_mul_f32 v121, v121, v70\n"
    "v_mul_f32 v122, v122, v70\n"
    "v_mul_f32 v123, v123, v70\n"
    "v_mul_f32 v96, v96, v100\n"
    "v_mul_f32 v97, v97, v100\n"
    "v_mul_f32 v98, v98, v100\n"
    "v_mul_f32 v99, v99, v100\n"
    "v_cvt_pk_bf16_f32 v64, v120, v121\n"
    "v_cvt_pk_bf16_f32 v65, v122, v123\n"
    "v_cvt_pk_bf16_f32 v66, v96, v97\n"
    "v_cvt_pk_bf16_f32 v67, v98, v99\n"
    "v_min_u32 v64, -2, v64\n"
    "v_min_u32 v65, -2, v65\n"
    "v_min_u32 v66, -2, v66\n"
    "v_min_u32 v67, -2, v67\n"
    "v_mfma_f32_16x16x32_bf16 v[32:35], v[12:15], v[168:171], v[32:35]\n"
    "v_permlane32_swap_b32 v64, v66\n"
    "v_permlane32_swap_b32 v65, v67\n"
    "buffer_store_dwordx4 v[64:67], v4, s[4:7], s90 offen sc1\n"
    "v_mfma_f32_16x16x32_bf16 v[32:35], v[16:19], v[200:203], v[32:35]\n"
    "s_waitcnt vmcnt(3)\n"
    "ds_write_b128 v56, v[24:27]\n"
    "v_mfma_f32_16x16x32_bf16 v[32:35], v[20:23], v[232:235], v[32:35]\n"
    "ds_write_b128 v56, v[28:31] offset:1024\n"
    "v_mfma_f32_16x16x32_bf16 v[36:39], v[8:11], v[140:143], 0\n"
    "v_xor_b32 v116, 64, v56\n"
    "v_xor_b32 v117, 128, v56\n"
    "v_mfma_f32_16x16x32_bf16 v[36:39], v[12:15], v[172:175], v[36:39]\n"
    "s_nop 0\n"
    "v_xor_b32 v118, 192, v56\n"
    "v_mfma_f32_16x16x32_bf16 v[36:39], v[16:19], v[204:207], v[36:39]\n"
    "ds_write_b128 v116, v[32:35] offset:2048\n"
    "v_mfma_f32_16x16x32_bf16 v[36:39], v[20:23], v[236:239], v[36:39]\n"
    "s_waitcnt vmcnt(1)\n"
    "v_add_u32 v61, s59, v61\n"
    "v_xor_b32 v121, 1040, v61\n"
    "v_mfma_f32_16x16x32_bf16 v[40:43], v[8:11], v[144:147], 0\n"
    "ds_read_b32 v120, v61\n"
    "v_xor_b32 v122, 2080, v61\n"
    "v_mfma_f32_16x16x32_bf16 v[40:43], v[12:15], v[176:179], v[40:43]\n"
    "ds_read_b32 v121, v121\n"
    "v_xor_b32 v123, 3120, v61\n"
    "v_mfma_f32_16x16x32_bf16 v[40:43], v[16:19], v[208:211], v[40:43]\n"
    "ds_write_b128 v116, v[36:39] offset:3072\n"
    "v_mfma_f32_16x16x32_bf16 v[40:43], v[20:23], v[240:243], v[40:43]\n"
    "s_nop 0\n"
    "ds_read_b32 v122, v122\n"
    "v_mfma_f32_16x16x32_bf16 v[44:47], v[8:11], v[148:151], 0\n"
    "ds_read_b32 v123, v123\n"
    "s_nop 0\n"
    "v_mfma_f32_16x16x32_bf16 v[44:47], v[12:15], v[180:183], v[44:47]\n"
    "buffer_load_dwordx4 v[112:115], v0, s[96:99], s41 offen\n"
    "v_mfma_f32_16x16x32_bf16 v[44:47], v[16:19], v[212:215], v[44:47]\n"
    "ds_write_b128 v117, v[40:43] offset:4096\n"
    "v_mfma_f32_16x16x32_bf16 v[44:47], v[20:23], v[244:247], v[44:47]\n"
    "s_waitcnt lgkmcnt(4)\n"
    "v_lshlrev_b32 v88, 16, v120\n"
    "v_lshlrev_b32 v89, 16, v121\n"
    "v_mfma_f32_16x16x32_bf16 v[48:51], v[8:11], v[152:155], 0\n"
    "s_nop 0\n"
    "v_and_b32 v92, 0xffff0000, v120\n"
    "v_and_b32 v93, 0xffff0000, v121\n"
    "v_mfma_f32_16x16x32_bf16 v[48:51], v[12:15], v[184:187], v[48:51]\n"
    "ds_write_b128 v117, v[44:47] offset:5120\n"
    "v_mfma_f32_16x16x32_bf16 v[48:51], v[16:19], v[216:219], v[48:51]\n"
    "s_waitcnt lgkmcnt(3)\n"
    "v_lshlrev_b32 v90, 16, v122\n"
    "v_and_b32 v94, 0xffff0000, v122\n"
    "v_mfma_f32_16x16x32_bf16 v[48:51], v[20:23], v[248:251], v[48:51]\n"
    "s_waitcnt lgkmcnt(2)\n"
    "v_lshlrev_b32 v91, 16, v123\n"
    "v_and_b32 v95, 0xffff0000, v123\n"
    "v_mfma_f32_16x16x32_bf16 v[52:55], v[8:11], v[156:159], 0\n"
    "buffer_load_dwordx4 v[124:127], v0, s[96:99], s40 offen\n"
    "s_nop 0\n"
    "v_mfma_f32_16x16x32_bf16 v[52:55], v[12:15], v[188:191], v[52:55]\n"
    "v_mfma_f32_16x16x32_bf16 v[52:55], v[16:19], v[220:223], v[52:55]\n"
    "ds_write_b128 v118, v[48:51] offset:6144\n"
    "v_mfma_f32_16x16x32_bf16 v[52:55], v[20:23], v[252:255], v[52:55]\n"
    "s_nop 7\n"
    "ds_write_b128 v118, v[52:55] offset:7168\n"
    "s_waitcnt lgkmcnt(0)\n"
    "s_barrier\n"
    "ds_read_b128 v[24:27], v57\n"
    "ds_read_b128 v[28:31], v57 offset:1024\n"
    "ds_read_b128 v[32:35], v57 offset:8192\n"
    "ds_read_b128 v[36:39], v57 offset:9216\n"
    "ds_read_b128 v[40:43], v57 offset:49152\n"
    "ds_read_b128 v[44:47], v57 offset:50176\n"
    "ds_read_b128 v[48:51], v57 offset:57344\n"
    "ds_read_b128 v[52:55], v57 offset:58368\n"
    "s_waitcnt lgkmcnt(7)\n"
    "v_pk_add_f32 v[88:89], v[88:89], v[24:25]\n"
    "v_pk_add_f32 v[90:91], v[90:91], v[26:27]\n"
    "s_waitcnt lgkmcnt(6)\n"
    "v_pk_add_f32 v[92:93], v[92:93], v[28:29]\n"
    "v_pk_add_f32 v[94:95], v[94:95], v[30:31]\n"
    "s_waitcnt lgkmcnt(5)\n"
    "v_pk_add_f32 v[88:89], v[88:89], v[32:33]\n"
    "v_pk_add_f32 v[90:91], v[90:91], v[34:35]\n"
    "s_waitcnt lgkmcnt(4)\n"
    "v_pk_add_f32 v[92:93], v[92:93], v[36:37]\n"
    "v_pk_add_f32 v[94:95], v[94:95], v[38:39]\n"
    "s_waitcnt lgkmcnt(3)\n"
    "v_pk_add_f32 v[88:89], v[88:89], v[40:41]\n"
    "v_pk_add_f32 v[90:91], v[90:91], v[42:43]\n"
    "s_waitcnt lgkmcnt(2)\n"
    "v_pk_add_f32 v[92:93], v[92:93], v[44:45]\n"
    "v_pk_add_f32 v[94:95], v[94:95], v[46:47]\n"
    "s_waitcnt lgkmcnt(1)\n"
    "v_pk_add_f32 v[88:89], v[88:89], v[48:49]\n"
    "v_pk_add_f32 v[90:91], v[90:91], v[50:51]\n"
    "v_pk_mul_f32 v[36:37], v[88:89], v[88:89]\n"
    "v_pk_mul_f32 v[38:39], v[90:91], v[90:91]\n"
    "s_waitcnt lgkmcnt(0)\n"
    "v_pk_add_f32 v[92:93], v[92:93], v[52:53]\n"
    "v_pk_add_f32 v[94:95], v[94:95], v[54:55]\n"
    "v_pk_add_f32 v[32:33], v[88:89], v[92:93]\n"
    "v_pk_fma_f32 v[36:37], v[92:93], v[92:93], v[36:37]\n"
    "v_pk_add_f32 v[34:35], v[90:91], v[94:95]\n"
    "v_pk_fma_f32 v[38:39], v[94:95], v[94:95], v[38:39]\n"
    "v_cvt_pk_bf16_f32 v120, v88, v92\n"
    "v_permlane32_swap_b32 v32, v34\n"
    "v_permlane32_swap_b32 v33, v35\n"
    "v_permlane32_swap_b32 v36, v38\n"
    "v_permlane32_swap_b32 v37, v39\n"
    "v_pk_add_f32 v[32:33], v[32:33], v[34:35]\n"
    "v_pk_add_f32 v[36:37], v[36:37], v[38:39]\n"
    "v_cvt_pk_bf16_f32 v121, v89, v93\n"
    "v_permlane16_swap_b32 v32, v33\n"
    "v_permlane16_swap_b32 v36, v37\n"
    "v_add_f32 v32, v32, v33\n"
    "v_add_f32 v36, v36, v37\n"
    "v_cvt_pk_bf16_f32 v122, v90, v94\n"
    "v_add_f32_dpp v32, v32, v32 row_ror:8 row_mask:0xf bank_mask:0xf\n"
    "v_add_f32_dpp v36, v36, v36 row_ror:8 row_mask:0xf bank_mask:0xf\n"
    "v_cvt_pk_bf16_f32 v123, v91, v95\n"
    "v_add_f32_dpp v34, v32, v32 row_ror:4 row_mask:0xf bank_mask:0xf\n"
    "v_add_f32_dpp v35, v36, v36 row_ror:4 row_mask:0xf bank_mask:0xf\n"
    "ds_write_b64 v58, v[34:35]\n"
    "v_min_u32 v120, -2, v120\n"
    "v_min_u32 v121, -2, v121\n"
    "v_min_u32 v122, -2, v122\n"
    "v_min_u32 v123, -2, v123\n"
    "buffer_store_dwordx4 v[120:123], v62, s[4:7], s38 offen sc1\n"
    "s_nop 1\n"
    "v_xor_b32 v121, 1040, v61\n"
    "v_xor_b32 v122, 2080, v61\n"
    "v_xor_b32 v123, 3120, v61\n"
    "ds_read_b32 v120, v61 offset:49152\n"
    "s_waitcnt lgkmcnt(1)\n"
    "s_barrier\n"
    "ds_read_b128 v[24:27], v59\n"
    "ds_read_b128 v[28:31], v59 offset:16\n"
    "ds_read_b32 v121, v121 offset:49152\n"
    "ds_read_b32 v122, v122 offset:49152\n"
    "ds_read_b32 v123, v123 offset:49152\n"
    "s_waitcnt lgkmcnt(5)\n"
    "v_lshlrev_b32 v88, 16, v120\n"
    "v_and_b32 v92, 0xffff0000, v120\n"
    "s_waitcnt vmcnt(1)\n"
    "ds_read_b64 a[204:205], v124 offset:3584\n"
    "ds_read_b64 a[206:207], v124 offset:3592\n"
    "s_waitcnt lgkmcnt(6)\n"
    "v_pk_add_f32 v[24:25], v[24:25], v[26:27]\n"
    "s_waitcnt lgkmcnt(5)\n"
    "v_pk_add_f32 v[28:29], v[28:29], v[30:31]\n"
    "s_nop 0\n"
    "v_pk_add_f32 v[24:25], v[24:25], v[28:29]\n"
    "s_waitcnt lgkmcnt(4)\n"
    "v_mul_f32 v24, 0x3c000000, v24\n"
    "v_mul_f32 v25, 0x3c000000, v25\n"
    "v_fma_f32 v25, -v24, v24, v25\n"
    "v_max_f32 v25, 0, v25\n"
    "v_sqrt_f32 v25, v25\n"
    "v_lshlrev_b32 v89, 16, v121\n"
    "v_cvt_pk_bf16_f32 v24, v24, v25\n"
    "v_min_u32 v24, -2, v24\n"
    "buffer_load_dwordx4 v[8:11], v1, s[4:7], s46 offen sc1\n"
    "v_and_b32 v93, 0xffff0000, v121\n"
    "v_mov_b32_dpp v25, v24 row_ror:12 row_mask:0xf bank_mask:0xf\n"
    "v_mov_b32_dpp v26, v24 row_ror:8 row_mask:0xf bank_mask:0xf\n"
    "v_mov_b32_dpp v27, v24 row_ror:4 row_mask:0xf bank_mask:0xf\n"
    "buffer_store_dwordx4 v[24:27], v60, s[4:7], s39 offen sc1\n"
    "buffer_load_dwordx4 v[12:15], v1, s[4:7], s47 offen sc1\n"
    "s_waitcnt lgkmcnt(0)\n"
    "v_lshlrev_b32 v91, 16, v123\n"
    "v_and_b32 v94, 0xffff0000, v122\n"
    "v_and_b32 v95, 0xffff0000, v123\n"
    "ds_read_b64 a[220:221], v124 offset:3072\n"
    "ds_read_b64 a[222:223], v124 offset:3080\n"
    "ds_read_b64 a[236:237], v124 offset:2560\n"
    "ds_read_b64 a[238:239], v124 offset:2568\n"
    "buffer_load_dwordx4 v[16:19], v1, s[4:7], s48 offen sc1\n"
    "ds_read_b64 a[252:253], v124 offset:2048\n"
    "ds_read_b64 a[254:255], v124 offset:2056\n"
    "ds_read_b64 a[200:201], v124 offset:3712\n"
    "ds_read_b64 a[202:203], v124 offset:3720\n"
    "ds_read_b64 a[216:217], v124 offset:3200\n"
    "ds_read_b64 a[218:219], v124 offset:3208\n"
    "ds_read_b64 a[232:233], v124 offset:2688\n"
    "buffer_load_dwordx4 v[20:23], v1, s[4:7], s49 offen sc1\n"
    "ds_read_b64 a[234:235], v124 offset:2696\n"
    "ds_read_b64 a[248:249], v124 offset:2176\n"
    "ds_read_b64 a[250:251], v124 offset:2184\n"
    "ds_read_b64 a[196:197], v124 offset:3840\n"
    "ds_read_b64 a[198:199], v124 offset:3848\n"
    "ds_read_b64 a[212:213], v124 offset:3328\n"
    "ds_read_b64 a[214:215], v124 offset:3336\n"
    "ds_read_b64 a[228:229], v124 offset:2816\n"
    "ds_read_b64 a[230:231], v124 offset:2824\n"
    "ds_read_b64 a[244:245], v124 offset:2304\n"
    "ds_read_b64 a[246:247], v124 offset:2312\n"
    "ds_read_b64 a[192:193], v124 offset:3968\n"
    "ds_read_b64 a[194:195], v124 offset:3976\n"
    "ds_read_b64 a[208:209], v124 offset:3456\n"
    "ds_read_b64 a[210:211], v124 offset:3464\n"
    "ds_read_b64 a[224:225], v124 offset:2944\n"
    "ds_read_b64 a[226:227], v124 offset:2952\n"
    "ds_read_b64 a[240:241], v124 offset:2432\n"
    "ds_read_b64 a[242:243], v124 offset:2440\n"
    "s_waitcnt vmcnt(4)\n"
    "v_max3_u32 v90, v8, v9, v10\n"
    "s_waitcnt vmcnt(2)\n"
    "v_max3_u32 v90, v90, v11, v12\n"
    "v_max3_u32 v90, v90, v13, v14\n"
    "s_waitcnt vmcnt(1)\n"
    "v_max3_u32 v90, v90, v15, v16\n"
    "v_max3_u32 v90, v90, v17, v18\n"
    "s_waitcnt vmcnt(0)\n"
    "v_max3_u32 v90, v90, v19, v20\n"
    "v_max3_u32 v90, v90, v21, v22\n"
    "v_max_u32 v90, v90, v23\n"
    "v_cmp_eq_u32 vcc, -1, v90\n"
    "s_cbranch_vccnz .R_12\n"
    ".R_12c:\n"
    "v_mfma_f32_16x16x32_bf16 v[24:27], v[8:11], v[128:131], 0\n"
    "v_lshlrev_b32 v90, 16, v122\n"
    "v_mfma_f32_16x16x32_bf16 v[24:27], v[12:15], v[160:163], v[24:27]\n"
    "buffer_load_dwordx4 v[128:131], v0, s[96:99], s45 offen\n"
    "v_mfma_f32_16x16x32_bf16 v[24:27], v[16:19], v[192:195], v[24:27]\n"
    "v_mfma_f32_16x16x32_bf16 v[24:27], v[20:23], v[224:227], v[24:27]\n"
    "s_nop 2\n"
    "v_mfma_f32_16x16x32_bf16 v[28:31], v[8:11], v[132:135], 0\n"
    "s_nop 2\n"
    "v_mfma_f32_16x16x32_bf16 v[28:31], v[12:15], v[164:167], v[28:31]\n"
    "ds_write_b128 v56, v[24:27]\n"
    "v_mfma_f32_16x16x32_bf16 v[28:31], v[16:19], v[196:199], v[28:31]\n"
    "v_mfma_f32_16x16x32_bf16 v[28:31], v[20:23], v[228:231], v[28:31]\n"
    "s_nop 2\n"
    "v_mfma_f32_16x16x32_bf16 v[32:35], v[8:11], v[136:139], 0\n"
    "s_nop 2\n"
    "v_mfma_f32_16x16x32_bf16 v[32:35], v[12:15], v[168:171], v[32:35]\n"
    "ds_write_b128 v56, v[28:31] offset:1024\n"
    "v_mfma_f32_16x16x32_bf16 v[32:35], v[16:19], v[200:203], v[32:35]\n"
    "v_mfma_f32_16x16x32_bf16 v[32:35], v[20:23], v[232:235], v[32:35]\n"
    "s_nop 2\n"
    "v_mfma_f32_16x16x32_bf16 v[36:39], v[8:11], v[140:143], 0\n"
    "s_nop 2\n"
    "v_mfma_f32_16x16x32_bf16 v[36:39], v[12:15], v[172:175], v[36:39]\n"
    "ds_write_b128 v116, v[32:35] offset:2048\n"
    "v_mfma_f32_16x16x32_bf16 v[36:39], v[16:19], v[204:207], v[36:39]\n"
    "v_mfma_f32_16x16x32_bf16 v[36:39], v[20:23], v[236:239], v[36:39]\n"
    "s_nop 2\n"
    "v_mfma_f32_16x16x32_bf16 v[40:43], v[8:11], v[144:147], 0\n"
    "s_nop 2\n"
    "v_mfma_f32_16x16x32_bf16 v[40:43], v[12:15], v[176:179], v[40:43]\n"
    "ds_write_b128 v116, v[36:39] offset:3072\n"
    "v_mfma_f32_16x16x32_bf16 v[40:43], v[16:19], v[208:211], v[40:43]\n"
    "v_mfma_f32_16x16x32_bf16 v[40:43], v[20:23], v[240:243], v[40:43]\n"
    "s_nop 2\n"
    "v_mfma_f32_16x16x32_bf16 v[44:47], v[8:11], v[148:151], 0\n"
    "s_nop 2\n"
    "v_mfma_f32_16x16x32_bf16 v[44:47], v[12:15], v[180:183], v[44:47]\n"
    "ds_write_b128 v117, v[40:43] offset:4096\n"
    "v_mfma_f32_16x16x32_bf16 v[44:47], v[16:19], v[212:215], v[44:47]\n"
    "buffer_load_dwordx4 v[64:67], v0, s[4:7], s28 offen sc1\n"
    "v_mfma_f32_16x16x32_bf16 v[44:47], v[20:23], v[244:247], v[44:47]\n"
    "s_nop 2\n"
    "v_mfma_f32_16x16x32_bf16 v[48:51], v[8:11], v[152:155], 0\n"
    "s_nop 2\n"
    "v_mfma_f32_16x16x32_bf16 v[48:51], v[12:15], v[184:187], v[48:51]\n"
    "ds_write_b128 v117, v[44:47] offset:5120\n"
    "v_mfma_f32_16x16x32_bf16 v[48:51], v[16:19], v[216:219], v[48:51]\n"
    "v_mfma_f32_16x16x32_bf16 v[48:51], v[20:23], v[248:251], v[48:51]\n"
    "s_nop 2\n"
    "v_mfma_f32_16x16x32_bf16 v[52:55], v[8:11], v[156:159], 0\n"
    "s_nop 2\n"
    "v_mfma_f32_16x16x32_bf16 v[52:55], v[12:15], v[188:191], v[52:55]\n"
    "ds_write_b128 v118, v[48:51] offset:6144\n"
    "v_mfma_f32_16x16x32_bf16 v[52:55], v[16:19], v[220:223], v[52:55]\n"
    "ds_read_b64 v[16:17], v124 offset:512\n"
    "v_mfma_f32_16x16x32_bf16 v[52:55], v[20:23], v[252:255], v[52:55]\n"
    "ds_read_b64 v[18:19], v124 offset:520\n"
    "ds_read_b64 v[20:21], v124\n"
    "ds_read_b64 v[22:23], v124 offset:8\n"
    "s_nop 4\n"
    "ds_write_b128 v118, v[52:55] offset:7168\n"
    "buffer_load_dwordx4 v[68:71], v0, s[4:7], s29 offen sc1\n"
    "buffer_load_dwordx4 v[72:75], v0, s[4:7], s30 offen sc1\n"
    "s_waitcnt lgkmcnt(0)\n"
    "s_barrier\n"
    "buffer_load_dwordx4 v[76:79], v0, s[4:7], s31 offen sc1\n"
    "ds_read_b128 v[24:27], v57\n"
    "ds_read_b128 v[28:31], v57 offset:1024\n"
    "ds_read_b128 v[32:35], v57 offset:8192\n"
    "ds_read_b128 v[36:39], v57 offset:9216\n"
    "ds_read_b128 v[40:43], v57 offset:49152\n"
    "buffer_load_dwordx4 v[80:83], v0, s[4:7], s34 offen sc1\n"
    "ds_read_b128 v[44:47], v57 offset:50176\n"
    "ds_read_b128 v[48:51], v57 offset:57344\n"
    "ds_read_b128 v[52:55], v57 offset:58368\n"
    "s_waitcnt lgkmcnt(7)\n"
    "v_pk_add_f32 v[88:89], v[88:89], v[24:25]\n"
    "v_pk_add_f32 v[90:91], v[90:91], v[26:27]\n"
    "s_waitcnt lgkmcnt(6)\n"
    "v_pk_add_f32 v[92:93], v[92:93], v[28:29]\n"
    "v_pk_add_f32 v[94:95], v[94:95], v[30:31]\n"
    "s_waitcnt lgkmcnt(5)\n"
    "v_pk_add_f32 v[88:89], v[88:89], v[32:33]\n"
    "v_pk_add_f32 v[90:91], v[90:91], v[34:35]\n"
    "s_waitcnt lgkmcnt(4)\n"
    "v_pk_add_f32 v[92:93], v[92:93], v[36:37]\n"
    "v_pk_add_f32 v[94:95], v[94:95], v[38:39]\n"
    "s_waitcnt lgkmcnt(3)\n"
    "v_pk_add_f32 v[88:89], v[88:89], v[40:41]\n"
    "v_pk_add_f32 v[90:91], v[90:91], v[42:43]\n"
    "s_waitcnt lgkmcnt(2)\n"
    "v_pk_add_f32 v[92:93], v[92:93], v[44:45]\n"
    "v_pk_add_f32 v[94:95], v[94:95], v[46:47]\n"
    "s_waitcnt lgkmcnt(1)\n"
    "v_pk_add_f32 v[88:89], v[88:89], v[48:49]\n"
    "v_pk_add_f32 v[90:91], v[90:91], v[50:51]\n"
    "v_pk_mul_f32 v[36:37], v[88:89], v[88:89]\n"
    "v_pk_mul_f32 v[38:39], v[90:91], v[90:91]\n"
    "s_waitcnt lgkmcnt(0)\n"
    "v_pk_add_f32 v[92:93], v[92:93], v[52:53]\n"
    "v_pk_add_f32 v[94:95], v[94:95], v[54:55]\n"
    "v_pk_add_f32 v[32:33], v[88:89], v[92:93]\n"
    "v_pk_fma_f32 v[36:37], v[92:93], v[92:93], v[36:37]\n"
    "v_pk_add_f32 v[34:35], v[90:91], v[94:95]\n"
    "v_pk_fma_f32 v[38:39], v[94:95], v[94:95], v[38:39]\n"
    "v_cvt_pk_bf16_f32 v120, v88, v92\n"
    "v_permlane32_swap_b32 v32, v34\n"
    "v_permlane32_swap_b32 v33, v35\n"
    "v_permlane32_swap_b32 v36, v38\n"
    "v_permlane32_swap_b32 v37, v39\n"
    "v_pk_add_f32 v[32:33], v[32:33], v[34:35]\n"
    "v_pk_add_f32 v[36:37], v[36:37], v[38:39]\n"
    "v_cvt_pk_bf16_f32 v121, v89, v93\n"
    "v_permlane16_swap_b32 v32, v33\n"
    "v_permlane16_swap_b32 v36, v37\n"
    "v_add_f32 v32, v32, v33\n"
    "v_add_f32 v36, v36, v37\n"
    "v_cvt_pk_bf16_f32 v122, v90, v94\n"
    "v_add_f32_dpp v32, v32, v32 row_ror:8 row_mask:0xf bank_mask:0xf\n"
    "v_add_f32_dpp v36, v36, v36 row_ror:8 row_mask:0xf bank_mask:0xf\n"
    "v_cvt_pk_bf16_f32 v123, v91, v95\n"
    "v_add_f32_dpp v34, v32, v32 row_ror:4 row_mask:0xf bank_mask:0xf\n"
    "v_add_f32_dpp v35, v36, v36 row_ror:4 row_mask:0xf bank_mask:0xf\n"
    "ds_write_b64 v58, v[34:35]\n"
    "v_min_u32 v120, -2, v120\n"
    "v_min_u32 v121, -2, v121\n"
    "v_min_u32 v122, -2, v122\n"
    "v_min_u32 v123, -2, v123\n"
    "buffer_store_dwordx4 v[120:123], v62, s[4:7], s42 offen sc1\n"
    "s_waitcnt lgkmcnt(0)\n"
    "s_barrier\n"
    "ds_read_b128 v[24:27], v59\n"
    "ds_read_b128 v[28:31], v59 offset:16\n"
    "s_waitcnt vmcnt(5)\n"
    "v_max3_u32 v108, v64, v65, v66\n"
    "s_waitcnt lgkmcnt(1)\n"
    "v_pk_add_f32 v[24:25], v[24:25], v[26:27]\n"
    "s_waitcnt lgkmcnt(0)\n"
    "v_pk_add_f32 v[28:29], v[28:29], v[30:31]\n"
    "s_nop 0\n"
    "v_pk_add_f32 v[24:25], v[24:25], v[28:29]\n"
    "s_nop 0\n"
    "v_mul_f32 v24, 0x3c000000, v24\n"
    "v_mul_f32 v25, 0x3c000000, v25\n"
    "v_fma_f32 v25, -v24, v24, v25\n"
    "v_max_f32 v25, 0, v25\n"
    "v_sqrt_f32 v25, v25\n"
    "s_nop 0\n"
    "v_cvt_pk_bf16_f32 v24, v24, v25\n"
    "v_min_u32 v24, -2, v24\n"
    "s_nop 1\n"
    "v_mov_b32_dpp v25, v24 row_ror:12 row_mask:0xf bank_mask:0xf\n"
    "v_mov_b32_dpp v26, v24 row_ror:8 row_mask:0xf bank_mask:0xf\n"
    "v_mov_b32_dpp v27, v24 row_ror:4 row_mask:0xf bank_mask:0xf\n"
    "buffer_store_dwordx4 v[24:27], v60, s[4:7], s43 offen sc1\n"
    "s_waitcnt vmcnt(5)\n"
    "v_max3_u32 v108, v108, v67, v68\n"
    "v_max3_u32 v108, v108, v69, v70\n"
    "v_dot2_f32_bf16 v100, v68, s35, 0\n"
    "v_dot2_f32_bf16 v104, v68, v68, s36\n"
    "v_dot2_f32_bf16 v101, v69, s35, 0\n"
    "v_dot2_f32_bf16 v105, v69, v69, s36\n"
    "v_dot2_f32_bf16 v102, v70, s35, 0\n"
    "v_dot2_f32_bf16 v106, v70, v70, s36\n"
    "v_dot2_f32_bf16 v103, v71, s35, 0\n"
    "v_dot2_f32_bf16 v107, v71, v71, s36\n"
    "s_waitcnt vmcnt(4)\n"
    "v_max3_u32 v108, v108, v71, v72\n"
    "v_max3_u32 v108, v108, v73, v74\n"
    "v_dot2_f32_bf16 v100, v72, s35, v100\n"
    "v_dot2_f32_bf16 v104, v72, v72, v104\n"
    "v_dot2_f32_bf16 v101, v73, s35, v101\n"
    "v_dot2_f32_bf16 v105, v73, v73, v105\n"
    "v_dot2_f32_bf16 v102, v74, s35, v102\n"
    "v_dot2_f32_bf16 v106, v74, v74, v106\n"
    "v_dot2_f32_bf16 v103, v75, s35, v103\n"
    "v_dot2_f32_bf16 v107, v75, v75, v107\n"
    "s_waitcnt vmcnt(3)\n"
    "v_max3_u32 v108, v108, v75, v76\n"
    "v_max3_u32 v108, v108, v77, v78\n"
    "v_dot2_f32_bf16 v100, v76, s35, v100\n"
    "v_dot2_f32_bf16 v104, v76, v76, v104\n"
    "v_dot2_f32_bf16 v101, v77, s35, v101\n"
    "v_dot2_f32_bf16 v105, v77, v77, v105\n"
    "v_dot2_f32_bf16 v102, v78, s35, v102\n"
    "v_dot2_f32_bf16 v106, v78, v78, v106\n"
    "v_dot2_f32_bf16 v103, v79, s35, v103\n"
    "v_dot2_f32_bf16 v107, v79, v79, v107\n"
    "s_waitcnt vmcnt(2)\n"
    "v_max3_u32 v108, v108, v79, v80\n"
    "v_max3_u32 v108, v108, v81, v82\n"
    "v_max_u32 v108, v108, v83\n"
    "v_cmp_eq_u32 vcc, -1, v108\n"
    "v_dot2_f32_bf16 v100, v80, s35, v100\n"
    "v_dot2_f32_bf16 v104, v80, v80, v104\n"
    "v_dot2_f32_bf16 v101, v81, s35, v101\n"
    "v_dot2_f32_bf16 v105, v81, v81, v105\n"
    "v_dot2_f32_bf16 v102, v82, s35, v102\n"
    "v_dot2_f32_bf16 v106, v82, v82, v106\n"
    "v_dot2_f32_bf16 v103, v83, s35, v103\n"
    "v_dot2_f32_bf16 v107, v83, v83, v107\n"
    "s_nop 2\n"
    "s_cbranch_vccnz .R_13\n"
    ".R_13c:\n"
    "v_pk_fma_f32 v[104:105], v[100:101], v[100:101], v[104:105] neg_lo:[1,0,0] neg_hi:[1,0,0]\n"
    "v_pk_fma_f32 v[106:107], v[102:103], v[102:103], v[106:107] neg_lo:[1,0,0] neg_hi:[1,0,0]\n"
    "v_max_f32 v104, s36, v104\n"
    "v_max_f32 v105, s36, v105\n"
    "v_max_f32 v106, s36, v106\n"
    "v_max_f32 v107, s36, v107\n"
    "v_lshlrev_b32 v116, 16, v64\n"
    "v_lshlrev_b32 v117, 16, v65\n"
    "v_lshlrev_b32 v118, 16, v66\n"
    "v_lshlrev_b32 v119, 16, v67\n"
    "v_rsq_f32 v104, v104\n"
    "v_rsq_f32 v105, v105\n"
    "v_rsq_f32 v106, v106\n"
    "v_rsq_f32 v107, v107\n"
    "v_pk_fma_f32 v[8:9], v[116:117], 2.0, v[100:101] op_sel_hi:[1,0,1]\n"
    "v_pk_fma_f32 v[10:11], v[118:119], 2.0, v[102:103] op_sel_hi:[1,0,1]\n"
    "v_and_b32 v120, 0xffff0000, v64\n"
    "v_and_b32 v121, 0xffff0000, v65\n"
    "v_pk_mul_f32 v[8:9], v[8:9], v[104:105]\n"
    "v_pk_mul_f32 v[10:11], v[10:11], v[106:107]\n"
    "v_pk_fma_f32 v[12:13], v[120:121], 2.0, v[100:101] op_sel_hi:[1,0,1]\n"
    "v_and_b32 v122, 0xffff0000, v66\n"
    "v_and_b32 v123, 0xffff0000, v67\n"
    "v_cvt_pk_bf16_f32 v100, v8, v9\n"
    "v_cvt_pk_bf16_f32 v101, v10, v11\n"
    "v_pk_fma_f32 v[14:15], v[122:123], 2.0, v[102:103] op_sel_hi:[1,0,1]\n"
    "s_add_u32 s28, s86, 0x601000\n"
    "ds_write_b64 v112, v[100:101]\n"
    "v_pk_mul_f32 v[12:13], v[12:13], v[104:105]\n"
    "v_pk_mul_f32 v[14:15], v[14:15], v[106:107]\n"
    "v_cvt_pk_bf16_f32 v102, v12, v13\n"
    "v_cvt_pk_bf16_f32 v103, v14, v15\n"
    "s_mov_b32 s29, 0xa09000\n"
    "ds_write_b64 v112, v[102:103] offset:4608\n"
    "v_fmac_f32 v116, s61, v8\n"
    "v_fmac_f32 v120, s62, v12\n"
    "v_fmac_f32 v117, s61, v9\n"
    "v_fmac_f32 v121, s62, v13\n"
    "v_fmac_f32 v118, s61, v10\n"
    "v_fmac_f32 v122, s62, v14\n"
    "v_fmac_f32 v119, s61, v11\n"
    "v_fmac_f32 v123, s62, v15\n"
    "s_mov_b32 s30, 0xa0b000\n"
    "s_mov_b32 s31, 0xa0d000\n"
    "s_mov_b32 s34, 0xa0f000\n"
    "ds_read_b64 v[8:9], v124 offset:1536\n"
    "ds_read_b64 v[10:11], v124 offset:1544\n"
    "ds_read_b64 v[12:13], v124 offset:1024\n"
    "s_waitcnt lgkmcnt(3)\n"
    "s_barrier\n"
    "ds_read_b128 v[40:43], v113\n"
    "ds_read_b128 v[44:47], v113 offset:128\n"
    "ds_read_b128 v[48:51], v113 offset:256\n"
    "ds_read_b128 v[52:55], v113 offset:384\n"
    "ds_read_b128 v[24:27], v113 offset:512\n"
    "ds_read_b128 v[28:31], v113 offset:640\n"
    "ds_read_b128 v[32:35], v113 offset:768\n"
    "ds_read_b128 v[36:39], v113 offset:896\n"
    "s_waitcnt lgkmcnt(7)\n"
    "v_mfma_f32_16x16x32_bf16 v[84:87], a[204:207], v[40:43], 0\n"
    "ds_read_b64 v[14:15], v124 offset:1032\n"
    "v_mfma_f32_16x16x32_bf16 v[88:91], a[220:223], v[40:43], 0\n"
    "v_mfma_f32_16x16x32_bf16 v[92:95], a[236:239], v[40:43], 0\n"
    "v_mfma_f32_16x16x32_bf16 v[96:99], a[252:255], v[40:43], 0\n"
    "ds_read_b128 v[40:43], v113 offset:1024\n"
    "s_waitcnt lgkmcnt(6)\n"
    "v_mfma_f32_16x16x32_bf16 v[84:87], a[200:203], v[44:47], v[84:87]\n"
    "v_mfma_f32_16x16x32_bf16 v[88:91], a[216:219], v[44:47], v[88:91]\n"
    "v_mfma_f32_16x16x32_bf16 v[92:95], a[232:235], v[44:47], v[92:95]\n"
    "v_mfma_f32_16x16x32_bf16 v[96:99], a[248:251], v[44:47], v[96:99]\n"
    "ds_read_b128 v[44:47], v113 offset:1152\n"
    "v_mfma_f32_16x16x32_bf16 v[84:87], a[196:199], v[48:51], v[84:87]\n"
    "v_mfma_f32_16x16x32_bf16 v[88:91], a[212:215], v[48:51], v[88:91]\n"
    "v_mfma_f32_16x16x32_bf16 v[92:95], a[228:231], v[48:51], v[92:95]\n"
    "v_mfma_f32_16x16x32_bf16 v[96:99], a[244:247], v[48:51], v[96:99]\n"
    "ds_read_b128 v[48:51], v113 offset:1280\n"
    "v_mfma_f32_16x16x32_bf16 v[84:87], a[192:195], v[52:55], v[84:87]\n"
    "s_nop 2\n"
    "v_mfma_f32_16x16x32_bf16 v[88:91], a[208:211], v[52:55], v[88:91]\n"
    "s_nop 2\n"
    "v_mfma_f32_16x16x32_bf16 v[92:95], a[224:227], v[52:55], v[92:95]\n"
    "ds_write_b128 v114, v[84:87]\n"
    "v_mfma_f32_16x16x32_bf16 v[96:99], a[240:243], v[52:55], v[96:99]\n"
    "ds_read_b128 v[52:55], v113 offset:1408\n"
    "s_waitcnt lgkmcnt(3)\n"
    "v_mfma_f32_16x16x32_bf16 v[88:91], a[204:207], v[24:27], v[88:91]\n"
    "v_mfma_f32_16x16x32_bf16 v[92:95], a[220:223], v[24:27], v[92:95]\n"
    "v_mfma_f32_16x16x32_bf16 v[96:99], a[236:239], v[24:27], v[96:99]\n"
    "ds_read_b128 v[24:27], v113 offset:1536\n"
    "v_mfma_f32_16x16x32_bf16 v[88:91], a[200:203], v[28:31], v[88:91]\n"
    "v_mfma_f32_16x16x32_bf16 v[92:95], a[216:219], v[28:31], v[92:95]\n"
    "v_mfma_f32_16x16x32_bf16 v[96:99], a[232:235], v[28:31], v[96:99]\n"
    "ds_read_b128 v[28:31], v113 offset:1664\n"
    "v_mfma_f32_16x16x32_bf16 v[88:91], a[196:199], v[32:35], v[88:91]\n"
    "v_mfma_f32_16x16x32_bf16 v[92:95], a[212:215], v[32:35], v[92:95]\n"
    "v_mfma_f32_16x16x32_bf16 v[96:99], a[228:231], v[32:35], v[96:99]\n"
    "ds_read_b128 v[32:35], v113 offset:1792\n"
    "v_mfma_f32_16x16x32_bf16 v[88:91], a[192:195], v[36:39], v[88:91]\n"
    "s_nop 2\n"
    "v_mfma_f32_16x16x32_bf16 v[92:95], a[208:211], v[36:39], v[92:95]\n"
    "s_nop 2\n"
    "v_mfma_f32_16x16x32_bf16 v[96:99], a[224:227], v[36:39], v[96:99]\n"
    "ds_read_b128 v[36:39], v113 offset:1920\n"
    "v_mfma_f32_16x16x32_bf16 v[92:95], a[204:207], v[40:43], v[92:95]\n"
    "ds_write_b128 v114, v[88:91] offset:1024\n"
    "v_mfma_f32_16x16x32_bf16 v[96:99], a[220:223], v[40:43], v[96:99]\n"
    "ds_read_b128 v[40:43], v113 offset:1536\n"
    "v_mfma_f32_16x16x32_bf16 v[92:95], a[200:203], v[44:47], v[92:95]\n"
    "v_mfma_f32_16x16x32_bf16 v[96:99], a[216:219], v[44:47], v[96:99]\n"
    "ds_read_b128 v[44:47], v113 offset:1024\n"
    "s_waitcnt lgkmcnt(4)\n"
    "v_mfma_f32_16x16x32_bf16 v[92:95], a[196:199], v[48:51], v[92:95]\n"
    "v_mfma_f32_16x16x32_bf16 v[96:99], a[212:215], v[48:51], v[96:99]\n"
    "ds_read_b128 v[48:51], v113 offset:512\n"
    "v_mfma_f32_16x16x32_bf16 v[92:95], a[192:195], v[52:55], v[92:95]\n"
    "s_nop 2\n"
    "v_mfma_f32_16x16x32_bf16 v[96:99], a[208:211], v[52:55], v[96:99]\n"
    "ds_read_b128 v[52:55], v113\n"
    "s_nop 0\n"
    "v_mfma_f32_16x16x32_bf16 v[96:99], a[204:207], v[24:27], v[96:99]\n"
    "ds_read_b64 v[24:25], v124 offset:1664\n"
    "v_mfma_f32_16x16x32_bf16 v[96:99], a[200:203], v[28:31], v[96:99]\n"
    "ds_write_b128 v114, v[92:95] offset:2048\n"
    "v_mfma_f32_16x16x32_bf16 v[96:99], a[196:199], v[32:35], v[96:99]\n"
    "ds_read_b64 v[26:27], v124 offset:1672\n"
    "s_waitcnt lgkmcnt(5)\n"
    "v_mfma_f32_16x16x32_bf16 v[96:99], a[192:195], v[36:39], v[96:99]\n"
    "ds_read_b64 v[28:29], v124 offset:1152\n"
    "s_nop 0\n"
    "v_mfma_f32_16x16x32_bf16 v[84:87], a[220:223], v[40:43], 0\n"
    "ds_read_b64 v[30:31], v124 offset:1160\n"
    "s_nop 0\n"
    "v_mfma_f32_16x16x32_bf16 v[88:91], a[236:239], v[40:43], 0\n"
    "ds_read_b64 v[32:33], v124 offset:640\n"
    "s_nop 0\n"
    "ds_write_b128 v114, v[96:99] offset:3072\n"
    "v_mfma_f32_16x16x32_bf16 v[92:95], a[252:255], v[40:43], 0\n"
    "ds_read_b64 v[34:35], v124 offset:648\n"
    "v_mfma_f32_16x16x32_bf16 v[84:87], a[236:239], v[44:47], v[84:87]\n"
    "buffer_load_dwordx4 v[64:67], v0, s[4:7], s28 offen sc1\n"
    "v_mfma_f32_16x16x32_bf16 v[88:91], a[252:255], v[44:47], v[88:91]\n"
    "ds_read_b64 v[36:37], v124 offset:128\n"
    "v_mfma_f32_16x16x32_bf16 v[92:95], v[8:11], v[44:47], v[92:95]\n"
    "ds_read_b64 v[38:39], v124 offset:136\n"
    "v_mfma_f32_16x16x32_bf16 v[96:99], v[12:15], v[44:47], 0\n"
    "ds_read_b128 v[44:47], v113 offset:1152\n"
    "v_mfma_f32_16x16x32_bf16 v[96:99], v[8:11], v[40:43], v[96:99]\n"
    "s_waitcnt lgkmcnt(4)\n"
    "s_barrier\n"
    "ds_read_b128 v[100:103], v115\n"
    "ds_read_b128 v[56:59], v115 offset:8192\n"
    "ds_read_b128 v[104:107], v115 offset:4096\n"
    "ds_read_b128 v[60:63], v115 offset:12288\n"
    "v_mfma_f32_16x16x32_bf16 v[84:87], a[252:255], v[48:51], v[84:87]\n"
    "ds_read_b128 v[40:43], v113 offset:1664\n"
    "v_mfma_f32_16x16x32_bf16 v[88:91], v[8:11], v[48:51], v[88:91]\n"
    "v_mfma_f32_16x16x32_bf16 v[92:95], v[12:15], v[48:51], v[92:95]\n"
    "v_mfma_f32_16x16x32_bf16 v[96:99], v[16:19], v[48:51], v[96:99]\n"
    "ds_read_b128 v[48:51], v113 offset:640\n"
    "v_mfma_f32_16x16x32_bf16 v[84:87], v[8:11], v[52:55], v[84:87]\n"
    "s_waitcnt lgkmcnt(5)\n"
    "v_add_f32 v116, v116, v100\n"
    "v_add_f32 v117, v117, v101\n"
    "v_add_f32 v118, v118, v102\n"
    "v_add_f32 v119, v119, v103\n"
    "s_waitcnt lgkmcnt(4)\n"
    "v_add_f32 v120, v120, v56\n"
    "v_add_f32 v121, v121, v57\n"
    "v_add_f32 v122, v122, v58\n"
    "v_add_f32 v123, v123, v59\n"
    "s_waitcnt lgkmcnt(3)\n"
    "v_add_f32 v116, v116, v104\n"
    "v_add_f32 v117, v117, v105\n"
    "v_add_f32 v118, v118, v106\n"
    "v_add_f32 v119, v119, v107\n"
    "s_waitcnt lgkmcnt(2)\n"
    "v_add_f32 v120, v120, v60\n"
    "v_add_f32 v121, v121, v61\n"
    "v_add_f32 v122, v122, v62\n"
    "v_add_f32 v123, v123, v63\n"
    "v_cvt_pk_bf16_f32 v108, v116, v120\n"
    "v_cvt_pk_bf16_f32 v109, v117, v121\n"
    "v_cvt_pk_bf16_f32 v110, v118, v122\n"
    "v_cvt_pk_bf16_f32 v111, v119, v123\n"
    "v_min_u32 v108, -2, v108\n"
    "v_min_u32 v109, -2, v109\n"
    "v_min_u32 v110, -2, v110\n"
    "v_min_u32 v111, -2, v111\n"
    "buffer_store_dwordx4 v[108:111], v125, s[4:7], s44 offen sc1\n"
    "buffer_load_dwordx4 v[68:71], v0, s[4:7], s29 offen sc1\n"
    "v_mfma_f32_16x16x32_bf16 v[88:91], v[12:15], v[52:55], v[88:91]\n"
    "ds_read_b64 v[8:9], v124 offset:1792\n"
    "v_mfma_f32_16x16x32_bf16 v[92:95], v[16:19], v[52:55], v[92:95]\n"
    "ds_read_b64 v[10:11], v124 offset:1800\n"
    "v_mfma_f32_16x16x32_bf16 v[96:99], v[20:23], v[52:55], v[96:99]\n"
    "ds_read_b128 v[52:55], v113 offset:128\n"
    "s_waitcnt lgkmcnt(3)\n"
    "v_mfma_f32_16x16x32_bf16 v[84:87], a[216:219], v[40:43], v[84:87]\n"
    "buffer_load_dwordx4 v[72:75], v0, s[4:7], s30 offen sc1\n"
    "v_mfma_f32_16x16x32_bf16 v[88:91], a[232:235], v[40:43], v[88:91]\n"
    "ds_read_b64 v[12:13], v124 offset:1280\n"
    "v_mfma_f32_16x16x32_bf16 v[92:95], a[248:251], v[40:43], v[92:95]\n"
    "ds_read_b64 v[14:15], v124 offset:1288\n"
    "v_mfma_f32_16x16x32_bf16 v[96:99], v[24:27], v[40:43], v[96:99]\n"
    "ds_read_b128 v[40:43], v113 offset:1792\n"
    "v_mfma_f32_16x16x32_bf16 v[84:87], a[232:235], v[44:47], v[84:87]\n"
    "buffer_load_dwordx4 v[76:79], v0, s[4:7], s31 offen sc1\n"
    "v_mfma_f32_16x16x32_bf16 v[88:91], a[248:251], v[44:47], v[88:91]\n"
    "ds_read_b64 v[16:17], v124 offset:768\n"
    "v_mfma_f32_16x16x32_bf16 v[92:95], v[24:27], v[44:47], v[92:95]\n"
    "ds_read_b64 v[18:19], v124 offset:776\n"
    "v_mfma_f32_16x16x32_bf16 v[96:99], v[28:31], v[44:47], v[96:99]\n"
    "ds_read_b128 v[44:47], v113 offset:1280\n"
    "v_mfma_f32_16x16x32_bf16 v[84:87], a[248:251], v[48:51], v[84:87]\n"
    "buffer_load_dwordx4 v[80:83], v0, s[4:7], s34 offen sc1\n"
    "v_mfma_f32_16x16x32_bf16 v[88:91], v[24:27], v[48:51], v[88:91]\n"
    "ds_read_b64 v[20:21], v124 offset:256\n"
    "v_mfma_f32_16x16x32_bf16 v[92:95], v[28:31], v[48:51], v[92:95]\n"
    "ds_read_b64 v[22:23], v124 offset:264\n"
    "v_mfma_f32_16x16x32_bf16 v[96:99], v[32:35], v[48:51], v[96:99]\n"
    "ds_read_b128 v[48:51], v113 offset:768\n"
    "s_waitcnt lgkmcnt(6)\n"
    "v_mfma_f32_16x16x32_bf16 v[84:87], v[24:27], v[52:55], v[84:87]\n"
    "ds_read_b64 v[24:25], v124 offset:1920\n"
    "v_mfma_f32_16x16x32_bf16 v[88:91], v[28:31], v[52:55], v[88:91]\n"
    "ds_read_b64 v[26:27], v124 offset:1928\n"
    "v_mfma_f32_16x16x32_bf16 v[92:95], v[32:35], v[52:55], v[92:95]\n"
    "ds_read_b64 v[28:29], v124 offset:1408\n"
    "v_mfma_f32_16x16x32_bf16 v[96:99], v[36:39], v[52:55], v[96:99]\n"
    "ds_read_b128 v[52:55], v113 offset:256\n"
    "v_mfma_f32_16x16x32_bf16 v[84:87], a[212:215], v[40:43], v[84:87]\n"
    "ds_read_b64 v[30:31], v124 offset:1416\n"
    "v_mfma_f32_16x16x32_bf16 v[88:91], a[228:231], v[40:43], v[88:91]\n"
    "ds_read_b64 v[32:33], v124 offset:896\n"
    "v_mfma_f32_16x16x32_bf16 v[92:95], a[244:247], v[40:43], v[92:95]\n"
    "ds_read_b64 v[34:35], v124 offset:904\n"
    "v_mfma_f32_16x16x32_bf16 v[96:99], v[8:11], v[40:43], v[96:99]\n"
    "ds_read_b128 v[40:43], v113 offset:1920\n"
    "s_waitcnt lgkmcnt(7)\n"
    "v_mfma_f32_16x16x32_bf16 v[84:87], a[228:231], v[44:47], v[84:87]\n"
    "ds_read_b64 v[36:37], v124 offset:384\n"
    "v_mfma_f32_16x16x32_bf16 v[88:91], a[244:247], v[44:47], v[88:91]\n"
    "ds_read_b64 v[38:39], v124 offset:392\n"
    "v_mfma_f32_16x16x32_bf16 v[92:95], v[8:11], v[44:47], v[92:95]\n"
    "v_mfma_f32_16x16x32_bf16 v[96:99], v[12:15], v[44:47], v[96:99]\n"
    "ds_read_b128 v[44:47], v113 offset:1408\n"
    "v_mfma_f32_16x16x32_bf16 v[84:87], a[244:247], v[48:51], v[84:87]\n"
    "v_mfma_f32_16x16x32_bf16 v[88:91], v[8:11], v[48:51], v[88:91]\n"
    "v_mfma_f32_16x16x32_bf16 v[92:95], v[12:15], v[48:51], v[92:95]\n"
    "v_mfma_f32_16x16x32_bf16 v[96:99], v[16:19], v[48:51], v[96:99]\n"
    "ds_read_b128 v[48:51], v113 offset:896\n"
    "s_waitcnt lgkmcnt(3)\n"
    "v_mfma_f32_16x16x32_bf16 v[84:87], v[8:11], v[52:55], v[84:87]\n"
    "v_mfma_f32_16x16x32_bf16 v[88:91], v[12:15], v[52:55], v[88:91]\n"
    "v_mfma_f32_16x16x32_bf16 v[92:95], v[16:19], v[52:55], v[92:95]\n"
    "s_waitcnt vmcnt(5)\n"
    "v_max3_u32 v108, v64, v65, v66\n"
    "v_mfma_f32_16x16x32_bf16 v[96:99], v[20:23], v[52:55], v[96:99]\n"
    "ds_read_b128 v[52:55], v113 offset:384\n"
    "v_mfma_f32_16x16x32_bf16 v[84:87], a[208:211], v[40:43], v[84:87]\n"
    "v_mfma_f32_16x16x32_bf16 v[88:91], a[224:227], v[40:43], v[88:91]\n"
    "v_mfma_f32_16x16x32_bf16 v[92:95], a[240:243], v[40:43], v[92:95]\n"
    "v_mfma_f32_16x16x32_bf16 v[96:99], v[24:27], v[40:43], v[96:99]\n"
    "s_waitcnt lgkmcnt(1)\n"
    "v_mfma_f32_16x16x32_bf16 v[84:87], a[224:227], v[44:47], v[84:87]\n"
    "v_mfma_f32_16x16x32_bf16 v[88:91], a[240:243], v[44:47], v[88:91]\n"
    "v_mfma_f32_16x16x32_bf16 v[92:95], v[24:27], v[44:47], v[92:95]\n"
    "v_mfma_f32_16x16x32_bf16 v[96:99], v[28:31], v[44:47], v[96:99]\n"
    "v_mfma_f32_16x16x32_bf16 v[84:87], a[240:243], v[48:51], v[84:87]\n"
    "v_mfma_f32_16x16x32_bf16 v[88:91], v[24:27], v[48:51], v[88:91]\n"
    "v_mfma_f32_16x16x32_bf16 v[92:95], v[28:31], v[48:51], v[92:95]\n"
    "v_mfma_f32_16x16x32_bf16 v[96:99], v[32:35], v[48:51], v[96:99]\n"
    "s_waitcnt lgkmcnt(0)\n"
    "v_mfma_f32_16x16x32_bf16 v[84:87], v[24:27], v[52:55], v[84:87]\n"
    "v_mfma_f32_16x16x32_bf16 v[88:91], v[28:31], v[52:55], v[88:91]\n"
    "v_mfma_f32_16x16x32_bf16 v[92:95], v[32:35], v[52:55], v[92:95]\n"
    "v_mfma_f32_16x16x32_bf16 v[96:99], v[36:39], v[52:55], v[96:99]\n"
    "s_waitcnt vmcnt(3)\n"
    "v_max3_u32 v108, v108, v67, v68\n"
    "v_max3_u32 v108, v108, v69, v70\n"
    "v_dot2_f32_bf16 v100, v68, s35, 0\n"
    "v_dot2_f32_bf16 v104, v68, v68, s36\n"
    "v_dot2_f32_bf16 v101, v69, s35, 0\n"
    "v_dot2_f32_bf16 v105, v69, v69, s36\n"
    "s_waitcnt vmcnt(2)\n"
    "v_max3_u32 v108, v108, v71, v72\n"
    "v_max3_u32 v108, v108, v73, v74\n"
    "v_dot2_f32_bf16 v102, v70, s35, 0\n"
    "v_dot2_f32_bf16 v106, v70, v70, s36\n"
    "v_dot2_f32_bf16 v103, v71, s35, 0\n"
    "v_dot2_f32_bf16 v107, v71, v71, s36\n"
    "v_dot2_f32_bf16 v100, v72, s35, v100\n"
    "v_dot2_f32_bf16 v104, v72, v72, v104\n"
    "v_dot2_f32_bf16 v101, v73, s35, v101\n"
    "v_dot2_f32_bf16 v105, v73, v73, v105\n"
    "v_dot2_f32_bf16 v102, v74, s35, v102\n"
    "v_dot2_f32_bf16 v106, v74, v74, v106\n"
    "v_dot2_f32_bf16 v103, v75, s35, v103\n"
    "v_dot2_f32_bf16 v107, v75, v75, v107\n"
    "s_waitcnt vmcnt(1)\n"
    "v_max3_u32 v108, v108, v75, v76\n"
    "v_max3_u32 v108, v108, v77, v78\n"
    "v_dot2_f32_bf16 v100, v76, s35, v100\n"
    "v_dot2_f32_bf16 v104, v76, v76, v104\n"
    "v_dot2_f32_bf16 v101, v77, s35, v101\n"
    "v_dot2_f32_bf16 v105, v77, v77, v105\n"
    "v_dot2_f32_bf16 v102, v78, s35, v102\n"
    "v_dot2_f32_bf16 v106, v78, v78, v106\n"
    "v_dot2_f32_bf16 v103, v79, s35, v103\n"
    "v_dot2_f32_bf16 v107, v79, v79, v107\n"
    "s_waitcnt vmcnt(0)\n"
    "v_max3_u32 v108, v108, v79, v80\n"
    "v_max3_u32 v108, v108, v81, v82\n"
    "v_max_u32 v108, v108, v83\n"
    "v_cmp_eq_u32 vcc, -1, v108\n"
    "v_dot2_f32_bf16 v100, v80, s35, v100\n"
    "v_dot2_f32_bf16 v104, v80, v80, v104\n"
    "v_dot2_f32_bf16 v101, v81, s35, v101\n"
    "v_dot2_f32_bf16 v105, v81, v81, v105\n"
    "v_dot2_f32_bf16 v102, v82, s35, v102\n"
    "v_dot2_f32_bf16 v106, v82, v82, v106\n"
    "v_dot2_f32_bf16 v103, v83, s35, v103\n"
    "v_dot2_f32_bf16 v107, v83, v83, v107\n"
    "s_nop 2\n"
    "s_cbranch_vccnz .R_14\n"
    ".R_14c:\n"
    "v_pk_fma_f32 v[104:105], v[100:101], v[100:101], v[104:105] neg_lo:[1,0,0] neg_hi:[1,0,0]\n"
    "v_pk_fma_f32 v[106:107], v[102:103], v[102:103], v[106:107] neg_lo:[1,0,0] neg_hi:[1,0,0]\n"
    "v_max_f32 v104, s36, v104\n"
    "v_max_f32 v105, s36, v105\n"
    "v_max_f32 v106, s36, v106\n"
    "v_max_f32 v107, s36, v107\n"
    "v_lshlrev_b32 v116, 16, v64\n"
    "v_lshlrev_b32 v117, 16, v65\n"
    "v_lshlrev_b32 v118, 16, v66\n"
    "v_lshlrev_b32 v119, 16, v67\n"
    "v_rsq_f32 v104, v104\n"
    "v_rsq_f32 v105, v105\n"
    "v_rsq_f32 v106, v106\n"
    "v_rsq_f32 v107, v107\n"
    "v_pk_fma_f32 v[56:57], v[116:117], 2.0, v[100:101] op_sel_hi:[1,0,1]\n"
    "v_pk_fma_f32 v[58:59], v[118:119], 2.0, v[102:103] op_sel_hi:[1,0,1]\n"
    "v_and_b32 v120, 0xffff0000, v64\n"
    "v_and_b32 v121, 0xffff0000, v65\n"
    "v_pk_mul_f32 v[56:57], v[56:57], v[104:105]\n"
    "v_pk_mul_f32 v[58:59], v[58:59], v[106:107]\n"
    "v_pk_fma_f32 v[60:61], v[120:121], 2.0, v[100:101] op_sel_hi:[1,0,1]\n"
    "v_and_b32 v122, 0xffff0000, v66\n"
    "v_and_b32 v123, 0xffff0000, v67\n"
    "v_cvt_pk_bf16_f32 v100, v56, v57\n"
    "v_cvt_pk_bf16_f32 v101, v58, v59\n"
    "v_pk_fma_f32 v[62:63], v[122:123], 2.0, v[102:103] op_sel_hi:[1,0,1]\n"
    "v_pk_mul_f32 v[60:61], v[60:61], v[104:105]\n"
    "v_pk_mul_f32 v[62:63], v[62:63], v[106:107]\n"
    "ds_write_b64 v112, v[100:101] offset:2048\n"
    "v_cvt_pk_bf16_f32 v102, v60, v61\n"
    "v_cvt_pk_bf16_f32 v103, v62, v63\n"
    "ds_write_b64 v112, v[102:103] offset:6656\n"
    "v_fmac_f32 v116, s61, v56\n"
    "v_fmac_f32 v120, s62, v60\n"
    "v_fmac_f32 v117, s61, v57\n"
    "v_fmac_f32 v121, s62, v61\n"
    "v_fmac_f32 v118, s61, v58\n"
    "v_fmac_f32 v122, s62, v62\n"
    "v_fmac_f32 v119, s61, v59\n"
    "v_fmac_f32 v123, s62, v63\n"
    "s_waitcnt lgkmcnt(0)\n"
    "s_barrier\n"
    "ds_read_b128 v[8:11], v113 offset:2048\n"
    "ds_read_b128 v[12:15], v113 offset:2176\n"
    "ds_read_b128 v[16:19], v113 offset:2304\n"
    "ds_read_b128 v[20:23], v113 offset:2432\n"
    "ds_read_b128 v[24:27], v113 offset:2560\n"
    "ds_read_b128 v[28:31], v113 offset:2688\n"
    "ds_read_b128 v[32:35], v113 offset:2816\n"
    "ds_read_b128 v[36:39], v113 offset:2944\n"
    "s_waitcnt lgkmcnt(7)\n"
    "v_mfma_f32_16x16x32_bf16 v[84:87], a[204:207], v[8:11], v[84:87]\n"
    "ds_read_b128 v[40:43], v113 offset:3072\n"
    "v_mfma_f32_16x16x32_bf16 v[88:91], a[220:223], v[8:11], v[88:91]\n"
    "ds_read_b128 v[44:47], v113 offset:3200\n"
    "s_waitcnt lgkmcnt(8)\n"
    "v_mfma_f32_16x16x32_bf16 v[84:87], a[200:203], v[12:15], v[84:87]\n"
    "ds_read_b128 v[48:51], v113 offset:3328\n"
    "v_mfma_f32_16x16x32_bf16 v[88:91], a[216:219], v[12:15], v[88:91]\n"
    "ds_read_b128 v[52:55], v113 offset:3456\n"
    "s_waitcnt lgkmcnt(8)\n"
    "v_mfma_f32_16x16x32_bf16 v[84:87], a[196:199], v[16:19], v[84:87]\n"
    "ds_read_b128 v[64:67], v113 offset:3584\n"
    "v_mfma_f32_16x16x32_bf16 v[88:91], a[212:215], v[16:19], v[88:91]\n"
    "ds_read_b128 v[68:71], v113 offset:3712\n"
    "v_mfma_f32_16x16x32_bf16 v[84:87], a[192:195], v[20:23], v[84:87]\n"
    "ds_read_b128 v[72:75], v113 offset:3840\n"
    "s_nop 0\n"
    "v_mfma_f32_16x16x32_bf16 v[88:91], a[208:211], v[20:23], v[88:91]\n"
    "ds_read_b128 v[76:79], v113 offset:3968\n"
    "s_waitcnt lgkmcnt(8)\n"
    "v_mfma_f32_16x16x32_bf16 v[88:91], a[204:207], v[24:27], v[88:91]\n"
    "s_nop 0\n"
    "v_mfma_f32_16x16x32_bf16 v[88:91], a[200:203], v[28:31], v[88:91]\n"
    "ds_write_b128 v114, v[84:87]\n"
    "v_mfma_f32_16x16x32_bf16 v[88:91], a[196:199], v[32:35], v[88:91]\n"
    "v_mfma_f32_16x16x32_bf16 v[88:91], a[192:195], v[36:39], v[88:91]\n"
    "s_nop 2\n"
    "v_mfma_f32_16x16x32_bf16 v[92:95], a[236:239], v[8:11], v[92:95]\n"
    "s_nop 2\n"
    "v_mfma_f32_16x16x32_bf16 v[96:99], a[252:255], v[8:11], v[96:99]\n"
    "ds_write_b128 v114, v[88:91] offset:1024\n"
    "buffer_load_dwordx4 v[132:135], v128, s[4:7], s79 offen sc1\n"
    "buffer_load_dwordx4 v[136:139], v128, s[4:7], s79 offen offset:1024 sc1\n"
    "buffer_load_dwordx4 v[140:143], v128, s[4:7], s79 offen offset:2048 sc1\n"
    "v_mfma_f32_16x16x32_bf16 v[92:95], a[232:235], v[12:15], v[92:95]\n"
    "buffer_load_dwordx4 v[144:147], v128, s[4:7], s79 offen offset:3072 sc1\n"
    "v_mfma_f32_16x16x32_bf16 v[96:99], a[248:251], v[12:15], v[96:99]\n"
    "v_mfma_f32_16x16x32_bf16 v[92:95], a[228:231], v[16:19], v[92:95]\n"
    "v_mfma_f32_16x16x32_bf16 v[96:99], a[244:247], v[16:19], v[96:99]\n"
    "s_waitcnt lgkmcnt(0)\n"
    "s_barrier\n"
    "ds_read_b128 v[100:103], v115\n"
    "ds_read_b128 v[104:107], v115 offset:4096\n"
    "ds_read_b128 v[56:59], v115 offset:8192\n"
    "ds_read_b128 v[60:63], v115 offset:12288\n"
    "v_mfma_f32_16x16x32_bf16 v[92:95], a[224:227], v[20:23], v[92:95]\n"
    "v_mfma_f32_16x16x32_bf16 v[96:99], a[240:243], v[20:23], v[96:99]\n"
    "v_mfma_f32_16x16x32_bf16 v[92:95], a[220:223], v[24:27], v[92:95]\n"
    "v_mfma_f32_16x16x32_bf16 v[96:99], a[236:239], v[24:27], v[96:99]\n"
    "v_mfma_f32_16x16x32_bf16 v[92:95], a[216:219], v[28:31], v[92:95]\n"
    "v_mfma_f32_16x16x32_bf16 v[96:99], a[232:235], v[28:31], v[96:99]\n"
    "s_waitcnt lgkmcnt(2)\n"
    "v_add_f32 v100, v100, v104\n"
    "v_add_f32 v100, v100, v116\n"
    "v_add_f32 v101, v101, v105\n"
    "v_add_f32 v101, v101, v117\n"
    "v_mfma_f32_16x16x32_bf16 v[92:95], a[212:215], v[32:35], v[92:95]\n"
    "v_add_f32 v102, v102, v106\n"
    "s_waitcnt lgkmcnt(0)\n"
    "v_add_f32 v56, v56, v60\n"
    "v_add_f32 v56, v56, v120\n"
    "v_cvt_pk_bf16_f32 v80, v100, v56\n"
    "buffer_store_dword v80, v126, s[12:15], s88 offen\n"
    "v_add_f32 v57, v57, v61\n"
    "v_add_f32 v57, v57, v121\n"
    "v_cvt_pk_bf16_f32 v81, v101, v57\n"
    "v_mfma_f32_16x16x32_bf16 v[96:99], a[228:231], v[32:35], v[96:99]\n"
    "v_add_f32 v58, v58, v62\n"
    "v_add_f32 v102, v102, v118\n"
    "v_mfma_f32_16x16x32_bf16 v[92:95], a[208:211], v[36:39], v[92:95]\n"
    "v_add_f32 v58, v58, v122\n"
    "v_cvt_pk_bf16_f32 v82, v102, v58\n"
    "v_mfma_f32_16x16x32_bf16 v[96:99], a[224:227], v[36:39], v[96:99]\n"
    "v_add_f32 v103, v103, v107\n"
    "v_add_f32 v59, v59, v63\n"
    "v_mfma_f32_16x16x32_bf16 v[92:95], a[204:207], v[40:43], v[92:95]\n"
    "v_add_f32 v103, v103, v119\n"
    "v_add_f32 v59, v59, v123\n"
    "v_mfma_f32_16x16x32_bf16 v[96:99], a[220:223], v[40:43], v[96:99]\n"
    "v_cvt_pk_bf16_f32 v83, v103, v59\n"
    "v_mfma_f32_16x16x32_bf16 v[92:95], a[200:203], v[44:47], v[92:95]\n"
    "v_mfma_f32_16x16x32_bf16 v[96:99], a[216:219], v[44:47], v[96:99]\n"
    "v_mfma_f32_16x16x32_bf16 v[92:95], a[196:199], v[48:51], v[92:95]\n"
    "v_mfma_f32_16x16x32_bf16 v[96:99], a[212:215], v[48:51], v[96:99]\n"
    "v_mfma_f32_16x16x32_bf16 v[92:95], a[192:195], v[52:55], v[92:95]\n"
    "s_nop 2\n"
    "v_mfma_f32_16x16x32_bf16 v[96:99], a[208:211], v[52:55], v[96:99]\n"
    "s_nop 2\n"
    "v_mfma_f32_16x16x32_bf16 v[96:99], a[204:207], v[64:67], v[96:99]\n"
    "buffer_store_dword v81, v126, s[12:15], s88 offen offset:1024\n"
    "v_mfma_f32_16x16x32_bf16 v[96:99], a[200:203], v[68:71], v[96:99]\n"
    "ds_write_b128 v114, v[92:95] offset:2048\n"
    "v_mfma_f32_16x16x32_bf16 v[96:99], a[196:199], v[72:75], v[96:99]\n"
    "v_mfma_f32_16x16x32_bf16 v[96:99], a[192:195], v[76:79], v[96:99]\n"
    "s_nop 7\n"
    "ds_write_b128 v114, v[96:99] offset:3072\n"
    "s_waitcnt lgkmcnt(0)\n"
    "s_barrier\n"
    "ds_read_b128 v[100:103], v115\n"
    "ds_read_b128 v[56:59], v115 offset:8192\n"
    "buffer_store_dword v82, v126, s[12:15], s88 offen offset:2048\n"
    "ds_read_b128 v[104:107], v115 offset:4096\n"
    "ds_read_b128 v[60:63], v115 offset:12288\n"
    "s_waitcnt lgkmcnt(3)\n"
    "v_pk_add_f32 v[116:117], v[116:117], v[100:101]\n"
    "v_pk_add_f32 v[118:119], v[118:119], v[102:103]\n"
    "s_waitcnt vmcnt(6)\n"
    "v_max3_u32 v148, v132, v133, v134\n"
    "s_waitcnt lgkmcnt(2)\n"
    "v_pk_add_f32 v[120:121], v[120:121], v[56:57]\n"
    "s_waitcnt lgkmcnt(1)\n"
    "v_pk_add_f32 v[116:117], v[116:117], v[104:105]\n"
    "v_pk_add_f32 v[122:123], v[122:123], v[58:59]\n"
    "s_waitcnt vmcnt(5)\n"
    "v_max3_u32 v148, v148, v135, v136\n"
    "v_max3_u32 v148, v148, v137, v138\n"
    "s_waitcnt lgkmcnt(0)\n"
    "v_pk_add_f32 v[120:121], v[120:121], v[60:61]\n"
    "s_nop 0\n"
    "v_cvt_pk_bf16_f32 v108, v116, v120\n"
    "s_waitcnt vmcnt(4)\n"
    "v_max3_u32 v148, v148, v139, v140\n"
    "v_max3_u32 v148, v148, v141, v142\n"
    "v_cvt_pk_bf16_f32 v109, v117, v121\n"
    "v_pk_add_f32 v[118:119], v[118:119], v[106:107]\n"
    "v_pk_add_f32 v[122:123], v[122:123], v[62:63]\n"
    "s_waitcnt vmcnt(3)\n"
    "v_max3_u32 v148, v148, v143, v144\n"
    "v_max3_u32 v148, v148, v145, v146\n"
    "v_max_u32 v148, v148, v147\n"
    "v_cmp_eq_u32 vcc, -1, v148\n"
    "v_cvt_pk_bf16_f32 v110, v118, v122\n"
    "v_cvt_pk_bf16_f32 v111, v119, v123\n"
    "s_cbranch_vccnz .R_15\n"
    ".R_15c:\n"
    "v_swap_b32 v133, v136\n"
    "v_swap_b32 v134, v140\n"
    "v_swap_b32 v135, v144\n"
    "buffer_store_dword v83, v126, s[12:15], s88 offen offset:3072\n"
    "v_swap_b32 v138, v141\n"
    "v_swap_b32 v139, v145\n"
    "v_swap_b32 v143, v146\n"
    "buffer_store_dwordx4 v[132:135], v129, s[12:15], s80 offen\n"
    "buffer_store_dwordx4 v[136:139], v129, s[12:15], s80 offen offset:1024\n"
    "buffer_store_dwordx4 v[140:143], v129, s[12:15], s80 offen offset:2048\n"
    "buffer_store_dwordx4 v[144:147], v129, s[12:15], s80 offen offset:3072\n"
    "buffer_store_dword v108, v126, s[12:15], s37 offen\n"
    "buffer_store_dword v109, v126, s[12:15], s37 offen offset:1024\n"
    "buffer_store_dword v110, v126, s[12:15], s37 offen offset:2048\n"
    "buffer_store_dword v111, v126, s[12:15], s37 offen offset:3072\n"
    "s_or_b32 s89, s50, s53\n"
    "s_cbranch_scc1 .L_ep\n"
    "v_mov_b32 v12, s56\n"
    "v_mov_b32 v13, 0xa11000\n"
    "s_mov_b64 exec, 1\n"
    "buffer_store_dword v12, v13, s[4:7], 0 offen sc1\n"
    "s_mov_b64 exec, -1\n"
    ".L_ep:\n"
    "s_endpgm\n"
    ".R_1:\n"
    "s_cmp_eq_u32 s57, 0\n"
    "s_cbranch_scc1 .R_1c\n"
    "s_sub_u32 s57, s57, 1\n"
    "buffer_load_dword v11, v110, s[4:7], s37 offen sc1\n"
    "s_waitcnt vmcnt(0)\n"
    "v_cmp_ne_u32 vcc, s56, v11\n"
    "s_nop 2\n"
    "s_cbranch_vccnz .R_1\n"
    "s_branch .R_1c\n"
    ".R_2:\n"
    "s_cmp_eq_u32 s57, 0\n"
    "s_cbranch_scc1 .R_2c\n"
    "s_sub_u32 s57, s57, 1\n"
    "buffer_load_dwordx4 v[8:11], v1, s[4:7], s46 offen sc1\n"
    "buffer_load_dwordx4 v[12:15], v1, s[4:7], s47 offen sc1\n"
    "buffer_load_dwordx4 v[16:19], v1, s[4:7], s48 offen sc1\n"
    "buffer_load_dwordx4 v[20:23], v1, s[4:7], s49 offen sc1\n"
    "s_waitcnt vmcnt(0)\n"
    "v_max3_u32 v90, v8, v9, v10\n"
    "v_max3_u32 v90, v90, v11, v12\n"
    "v_max3_u32 v90, v90, v13, v14\n"
    "v_max3_u32 v90, v90, v15, v16\n"
    "v_max3_u32 v90, v90, v17, v18\n"
    "v_max3_u32 v90, v90, v19, v20\n"
    "v_max3_u32 v90, v90, v21, v22\n"
    "v_max_u32 v90, v90, v23\n"
    "v_cmp_eq_u32 vcc, -1, v90\n"
    "s_nop 2\n"
    "s_cbranch_vccnz .R_2\n"
    "s_branch .R_2c\n"
    ".R_3:\n"
    "s_cmp_eq_u32 s57, 0\n"
    "s_cbranch_scc1 .R_3c\n"
    "s_sub_u32 s57, s57, 1\n"
    "buffer_load_dwordx4 v[8:11], v1, s[4:7], s46 offen sc1\n"
    "buffer_load_dwordx4 v[12:15], v1, s[4:7], s47 offen sc1\n"
    "buffer_load_dwordx4 v[16:19], v1, s[4:7], s48 offen sc1\n"
    "buffer_load_dwordx4 v[20:23], v1, s[4:7], s49 offen sc1\n"
    "s_waitcnt vmcnt(0)\n"
    "v_max3_u32 v90, v8, v9, v10\n"
    "v_max3_u32 v90, v90, v11, v12\n"
    "v_max3_u32 v90, v90, v13, v14\n"
    "v_max3_u32 v90, v90, v15, v16\n"
    "v_max3_u32 v90, v90, v17, v18\n"
    "v_max3_u32 v90, v90, v19, v20\n"
    "v_max3_u32 v90, v90, v21, v22\n"
    "v_max_u32 v90, v90, v23\n"
    "v_cmp_eq_u32 vcc, -1, v90\n"
    "s_nop 2\n"
    "s_cbranch_vccnz .R_3\n"
    "s_branch .R_3c\n"
    ".R_4:\n"
    "s_cmp_eq_u32 s57, 0\n"
    "s_cbranch_scc1 .R_4c\n"
    "s_sub_u32 s57, s57, 1\n"
    "buffer_load_dword v109, v108, s[4:7], s37 offen sc1\n"
    "s_waitcnt vmcnt(0)\n"
    "v_cmp_ne_u32 vcc, s56, v109\n"
    "s_nop 2\n"
    "s_cbranch_vccnz .R_4\n"
    "s_branch .R_4c\n"
    ".R_5:\n"
    "s_cmp_eq_u32 s57, 0\n"
    "s_cbranch_scc1 .R_5c\n"
    "s_sub_u32 s57, s57, 1\n"
    "buffer_load_dwordx4 v[64:67], v0, s[4:7], s28 offen sc1\n"
    "buffer_load_dwordx4 v[68:71], v0, s[4:7], s29 offen sc1\n"
    "buffer_load_dwordx4 v[72:75], v0, s[4:7], s30 offen sc1\n"
    "buffer_load_dwordx4 v[76:79], v0, s[4:7], s31 offen sc1\n"
    "buffer_load_dwordx4 v[80:83], v0, s[4:7], s34 offen sc1\n"
    "s_waitcnt vmcnt(0)\n"
    "v_max3_u32 v108, v64, v65, v66\n"
    "v_max3_u32 v108, v108, v67, v68\n"
    "v_max3_u32 v108, v108, v69, v70\n"
    "v_max3_u32 v108, v108, v71, v72\n"
    "v_max3_u32 v108, v108, v73, v74\n"
    "v_max3_u32 v108, v108, v75, v76\n"
    "v_max3_u32 v108, v108, v77, v78\n"
    "v_max3_u32 v108, v108, v79, v80\n"
    "v_max3_u32 v108, v108, v81, v82\n"
    "v_max_u32 v108, v108, v83\n"
    "v_cmp_eq_u32 vcc, -1, v108\n"
    "v_dot2_f32_bf16 v100, v68, s35, 0\n"
    "v_dot2_f32_bf16 v104, v68, v68, s36\n"
    "v_dot2_f32_bf16 v101, v69, s35, 0\n"
    "v_dot2_f32_bf16 v105, v69, v69, s36\n"
    "v_dot2_f32_bf16 v102, v70, s35, 0\n"
    "v_dot2_f32_bf16 v106, v70, v70, s36\n"
    "v_dot2_f32_bf16 v103, v71, s35, 0\n"
    "v_dot2_f32_bf16 v107, v71, v71, s36\n"
    "v_dot2_f32_bf16 v100, v72, s35, v100\n"
    "v_dot2_f32_bf16 v104, v72, v72, v104\n"
    "v_dot2_f32_bf16 v101, v73, s35, v101\n"
    "v_dot2_f32_bf16 v105, v73, v73, v105\n"
    "v_dot2_f32_bf16 v102, v74, s35, v102\n"
    "v_dot2_f32_bf16 v106, v74, v74, v106\n"
    "v_dot2_f32_bf16 v103, v75, s35, v103\n"
    "v_dot2_f32_bf16 v107, v75, v75, v107\n"
    "v_dot2_f32_bf16 v100, v76, s35, v100\n"
    "v_dot2_f32_bf16 v104, v76, v76, v104\n"
    "v_dot2_f32_bf16 v101, v77, s35, v101\n"
    "v_dot2_f32_bf16 v105, v77, v77, v105\n"
    "v_dot2_f32_bf16 v102, v78, s35, v102\n"
    "v_dot2_f32_bf16 v106, v78, v78, v106\n"
    "v_dot2_f32_bf16 v103, v79, s35, v103\n"
    "v_dot2_f32_bf16 v107, v79, v79, v107\n"
    "v_dot2_f32_bf16 v100, v80, s35, v100\n"
    "v_dot2_f32_bf16 v104, v80, v80, v104\n"
    "v_dot2_f32_bf16 v101, v81, s35, v101\n"
    "v_dot2_f32_bf16 v105, v81, v81, v105\n"
    "v_dot2_f32_bf16 v102, v82, s35, v102\n"
    "v_dot2_f32_bf16 v106, v82, v82, v106\n"
    "v_dot2_f32_bf16 v103, v83, s35, v103\n"
    "v_dot2_f32_bf16 v107, v83, v83, v107\n"
    "s_nop 2\n"
    "s_cbranch_vccnz .R_5\n"
    "s_branch .R_5c\n"
    ".R_6:\n"
    "s_cmp_eq_u32 s57, 0\n"
    "s_cbranch_scc1 .R_6c\n"
    "s_sub_u32 s57, s57, 1\n"
    "buffer_load_dwordx4 v[64:67], v0, s[4:7], s28 offen sc1\n"
    "buffer_load_dwordx4 v[68:71], v0, s[4:7], s29 offen sc1\n"
    "buffer_load_dwordx4 v[72:75], v0, s[4:7], s30 offen sc1\n"
    "buffer_load_dwordx4 v[76:79], v0, s[4:7], s31 offen sc1\n"
    "buffer_load_dwordx4 v[80:83], v0, s[4:7], s34 offen sc1\n"
    "s_waitcnt vmcnt(0)\n"
    "v_max3_u32 v108, v64, v65, v66\n"
    "v_max3_u32 v108, v108, v67, v68\n"
    "v_max3_u32 v108, v108, v69, v70\n"
    "v_max3_u32 v108, v108, v71, v72\n"
    "v_max3_u32 v108, v108, v73, v74\n"
    "v_max3_u32 v108, v108, v75, v76\n"
    "v_max3_u32 v108, v108, v77, v78\n"
    "v_max3_u32 v108, v108, v79, v80\n"
    "v_max3_u32 v108, v108, v81, v82\n"
    "v_max_u32 v108, v108, v83\n"
    "v_cmp_eq_u32 vcc, -1, v108\n"
    "v_dot2_f32_bf16 v100, v68, s35, 0\n"
    "v_dot2_f32_bf16 v104, v68, v68, s36\n"
    "v_dot2_f32_bf16 v101, v69, s35, 0\n"
    "v_dot2_f32_bf16 v105, v69, v69, s36\n"
    "v_dot2_f32_bf16 v102, v70, s35, 0\n"
    "v_dot2_f32_bf16 v106, v70, v70, s36\n"
    "v_dot2_f32_bf16 v103, v71, s35, 0\n"
    "v_dot2_f32_bf16 v107, v71, v71, s36\n"
    "v_dot2_f32_bf16 v100, v72, s35, v100\n"
    "v_dot2_f32_bf16 v104, v72, v72, v104\n"
    "v_dot2_f32_bf16 v101, v73, s35, v101\n"
    "v_dot2_f32_bf16 v105, v73, v73, v105\n"
    "v_dot2_f32_bf16 v102, v74, s35, v102\n"
    "v_dot2_f32_bf16 v106, v74, v74, v106\n"
    "v_dot2_f32_bf16 v103, v75, s35, v103\n"
    "v_dot2_f32_bf16 v107, v75, v75, v107\n"
    "v_dot2_f32_bf16 v100, v76, s35, v100\n"
    "v_dot2_f32_bf16 v104, v76, v76, v104\n"
    "v_dot2_f32_bf16 v101, v77, s35, v101\n"
    "v_dot2_f32_bf16 v105, v77, v77, v105\n"
    "v_dot2_f32_bf16 v102, v78, s35, v102\n"
    "v_dot2_f32_bf16 v106, v78, v78, v106\n"
    "v_dot2_f32_bf16 v103, v79, s35, v103\n"
    "v_dot2_f32_bf16 v107, v79, v79, v107\n"
    "v_dot2_f32_bf16 v100, v80, s35, v100\n"
    "v_dot2_f32_bf16 v104, v80, v80, v104\n"
    "v_dot2_f32_bf16 v101, v81, s35, v101\n"
    "v_dot2_f32_bf16 v105, v81, v81, v105\n"
    "v_dot2_f32_bf16 v102, v82, s35, v102\n"
    "v_dot2_f32_bf16 v106, v82, v82, v106\n"
    "v_dot2_f32_bf16 v103, v83, s35, v103\n"
    "v_dot2_f32_bf16 v107, v83, v83, v107\n"
    "s_nop 2\n"
    "s_cbranch_vccnz .R_6\n"
    "s_branch .R_6c\n"
    ".R_7:\n"
    "s_cmp_eq_u32 s57, 0\n"
    "s_cbranch_scc1 .R_7c\n"
    "s_sub_u32 s57, s57, 1\n"
    "buffer_load_dwordx4 v[8:11], v1, s[4:7], s79 offen sc1\n"
    "buffer_load_dwordx4 v[12:15], v1, s[4:7], s79 offen offset:1024 sc1\n"
    "buffer_load_dwordx4 v[16:19], v1, s[4:7], s79 offen offset:2048 sc1\n"
    "buffer_load_dwordx4 v[20:23], v1, s[4:7], s79 offen offset:3072 sc1\n"
    "buffer_load_dwordx4 v[24:27], v1, s[4:7], s80 offen sc1\n"
    "buffer_load_dwordx4 v[28:31], v1, s[4:7], s80 offen offset:1024 sc1\n"
    "buffer_load_dwordx4 v[32:35], v1, s[4:7], s80 offen offset:2048 sc1\n"
    "buffer_load_dwordx4 v[36:39], v1, s[4:7], s80 offen offset:3072 sc1\n"
    "buffer_load_dwordx4 v[40:43], v1, s[4:7], s81 offen sc1\n"
    "buffer_load_dwordx4 v[44:47], v1, s[4:7], s81 offen offset:1024 sc1\n"
    "buffer_load_dwordx4 v[48:51], v1, s[4:7], s81 offen offset:2048 sc1\n"
    "buffer_load_dwordx4 v[52:55], v1, s[4:7], s81 offen offset:3072 sc1\n"
    "s_waitcnt vmcnt(0)\n"
    "v_max3_u32 v92, v8, v9, v10\n"
    "v_max3_u32 v92, v92, v11, v12\n"
    "v_max3_u32 v92, v92, v13, v14\n"
    "v_max3_u32 v92, v92, v15, v16\n"
    "v_max3_u32 v92, v92, v17, v18\n"
    "v_max3_u32 v92, v92, v19, v20\n"
    "v_max3_u32 v92, v92, v21, v22\n"
    "v_max_u32 v92, v92, v23\n"
    "v_cmp_eq_u32 vcc, -1, v92\n"
    "s_nop 2\n"
    "s_cbranch_vccnz .R_7\n"
    "s_branch .R_7c\n"
    ".R_8:\n"
    "s_cmp_eq_u32 s57, 0\n"
    "s_cbranch_scc1 .R_8c\n"
    "s_sub_u32 s57, s57, 1\n"
    "buffer_load_dwordx4 v[24:27], v1, s[4:7], s80 offen sc1\n"
    "buffer_load_dwordx4 v[28:31], v1, s[4:7], s80 offen offset:1024 sc1\n"
    "buffer_load_dwordx4 v[32:35], v1, s[4:7], s80 offen offset:2048 sc1\n"
    "buffer_load_dwordx4 v[36:39], v1, s[4:7], s80 offen offset:3072 sc1\n"
    "buffer_load_dwordx4 v[40:43], v1, s[4:7], s81 offen sc1\n"
    "buffer_load_dwordx4 v[44:47], v1, s[4:7], s81 offen offset:1024 sc1\n"
    "buffer_load_dwordx4 v[48:51], v1, s[4:7], s81 offen offset:2048 sc1\n"
    "buffer_load_dwordx4 v[52:55], v1, s[4:7], s81 offen offset:3072 sc1\n"
    "s_waitcnt vmcnt(0)\n"
    "v_max3_u32 v92, v24, v25, v26\n"
    "v_max3_u32 v92, v92, v27, v28\n"
    "v_max3_u32 v92, v92, v29, v30\n"
    "v_max3_u32 v92, v92, v31, v32\n"
    "v_max3_u32 v92, v92, v33, v34\n"
    "v_max3_u32 v92, v92, v35, v36\n"
    "v_max3_u32 v92, v92, v37, v38\n"
    "v_max3_u32 v92, v92, v39, v40\n"
    "v_max3_u32 v92, v92, v41, v42\n"
    "v_max3_u32 v92, v92, v43, v44\n"
    "v_max3_u32 v92, v92, v45, v46\n"
    "v_max3_u32 v92, v92, v47, v48\n"
    "v_max3_u32 v92, v92, v49, v50\n"
    "v_max3_u32 v92, v92, v51, v52\n"
    "v_max3_u32 v92, v92, v53, v54\n"
    "v_max_u32 v92, v92, v55\n"
    "v_cmp_eq_u32 vcc, -1, v92\n"
    "s_nop 2\n"
    "s_cbranch_vccnz .R_8\n"
    "s_branch .R_8c\n"
    ".R_9:\n"
    "s_cmp_eq_u32 s57, 0\n"
    "s_cbranch_scc1 .R_9c\n"
    "s_sub_u32 s57, s57, 1\n"
    "buffer_load_dwordx4 v[8:11], v1, s[4:7], s79 offen sc1\n"
    "buffer_load_dwordx4 v[12:15], v1, s[4:7], s79 offen offset:1024 sc1\n"
    "buffer_load_dwordx4 v[16:19], v1, s[4:7], s79 offen offset:2048 sc1\n"
    "buffer_load_dwordx4 v[20:23], v1, s[4:7], s79 offen offset:3072 sc1\n"
    "buffer_load_dwordx4 v[24:27], v1, s[4:7], s80 offen sc1\n"
    "buffer_load_dwordx4 v[28:31], v1, s[4:7], s80 offen offset:1024 sc1\n"
    "buffer_load_dwordx4 v[32:35], v1, s[4:7], s80 offen offset:2048 sc1\n"
    "buffer_load_dwordx4 v[36:39], v1, s[4:7], s80 offen offset:3072 sc1\n"
    "buffer_load_dwordx4 v[40:43], v1, s[4:7], s81 offen sc1\n"
    "buffer_load_dwordx4 v[44:47], v1, s[4:7], s81 offen offset:1024 sc1\n"
    "buffer_load_dwordx4 v[48:51], v1, s[4:7], s81 offen offset:2048 sc1\n"
    "buffer_load_dwordx4 v[52:55], v1, s[4:7], s81 offen offset:3072 sc1\n"
    "s_waitcnt vmcnt(0)\n"
    "v_max3_u32 v120, v8, v9, v10\n"
    "v_max3_u32 v120, v120, v11, v12\n"
    "v_max3_u32 v120, v120, v13, v14\n"
    "v_max3_u32 v120, v120, v15, v16\n"
    "v_max3_u32 v120, v120, v17, v18\n"
    "v_max3_u32 v120, v120, v19, v20\n"
    "v_max3_u32 v120, v120, v21, v22\n"
    "v_max_u32 v120, v120, v23\n"
    "v_cmp_eq_u32 vcc, -1, v120\n"
    "s_nop 2\n"
    "s_cbranch_vccnz .R_9\n"
    "s_branch .R_9c\n"
    ".R_10:\n"
    "s_cmp_eq_u32 s57, 0\n"
    "s_cbranch_scc1 .R_10c\n"
    "s_sub_u32 s57, s57, 1\n"
    "buffer_load_dwordx4 v[24:27], v1, s[4:7], s80 offen sc1\n"
    "buffer_load_dwordx4 v[28:31], v1, s[4:7], s80 offen offset:1024 sc1\n"
    "buffer_load_dwordx4 v[32:35], v1, s[4:7], s80 offen offset:2048 sc1\n"
    "buffer_load_dwordx4 v[36:39], v1, s[4:7], s80 offen offset:3072 sc1\n"
    "buffer_load_dwordx4 v[40:43], v1, s[4:7], s81 offen sc1\n"
    "buffer_load_dwordx4 v[44:47], v1, s[4:7], s81 offen offset:1024 sc1\n"
    "buffer_load_dwordx4 v[48:51], v1, s[4:7], s81 offen offset:2048 sc1\n"
    "buffer_load_dwordx4 v[52:55], v1, s[4:7], s81 offen offset:3072 sc1\n"
    "s_waitcnt vmcnt(0)\n"
    "v_max3_u32 v120, v24, v25, v26\n"
    "v_max3_u32 v120, v120, v27, v28\n"
    "v_max3_u32 v120, v120, v29, v30\n"
    "v_max3_u32 v120, v120, v31, v32\n"
    "v_max3_u32 v120, v120, v33, v34\n"
    "v_max3_u32 v120, v120, v35, v36\n"
    "v_max3_u32 v120, v120, v37, v38\n"
    "v_max3_u32 v120, v120, v39, v40\n"
    "v_max3_u32 v120, v120, v41, v42\n"
    "v_max3_u32 v120, v120, v43, v44\n"
    "v_max3_u32 v120, v120, v45, v46\n"
    "v_max3_u32 v120, v120, v47, v48\n"
    "v_max3_u32 v120, v120, v49, v50\n"
    "v_max3_u32 v120, v120, v51, v52\n"
    "v_max3_u32 v120, v120, v53, v54\n"
    "v_max_u32 v120, v120, v55\n"
    "v_cmp_eq_u32 vcc, -1, v120\n"
    "s_nop 2\n"
    "s_cbranch_vccnz .R_10\n"
    "s_branch .R_10c\n"
    ".R_11:\n"
    "s_cmp_eq_u32 s57, 0\n"
    "s_cbranch_scc1 .R_11c\n"
    "s_sub_u32 s57, s57, 1\n"
    "buffer_load_dwordx4 v[8:11], v1, s[4:7], s46 offen sc1\n"
    "buffer_load_dwordx4 v[12:15], v1, s[4:7], s47 offen sc1\n"
    "buffer_load_dwordx4 v[16:19], v1, s[4:7], s48 offen sc1\n"
    "buffer_load_dwordx4 v[20:23], v1, s[4:7], s49 offen sc1\n"
    "s_waitcnt vmcnt(0)\n"
    "v_max3_u32 v90, v8, v9, v10\n"
    "v_max3_u32 v90, v90, v11, v12\n"
    "v_max3_u32 v90, v90, v13, v14\n"
    "v_max3_u32 v90, v90, v15, v16\n"
    "v_max3_u32 v90, v90, v17, v18\n"
    "v_max3_u32 v90, v90, v19, v20\n"
    "v_max3_u32 v90, v90, v21, v22\n"
    "v_max_u32 v90, v90, v23\n"
    "v_cmp_eq_u32 vcc, -1, v90\n"
    "s_nop 2\n"
    "s_cbranch_vccnz .R_11\n"
    "s_branch .R_11c\n"
    ".R_12:\n"
    "s_cmp_eq_u32 s57, 0\n"
    "s_cbranch_scc1 .R_12c\n"
    "s_sub_u32 s57, s57, 1\n"
    "buffer_load_dwordx4 v[8:11], v1, s[4:7], s46 offen sc1\n"
    "buffer_load_dwordx4 v[12:15], v1, s[4:7], s47 offen sc1\n"
    "buffer_load_dwordx4 v[16:19], v1, s[4:7], s48 offen sc1\n"
    "buffer_load_dwordx4 v[20:23], v1, s[4:7], s49 offen sc1\n"
    "s_waitcnt vmcnt(0)\n"
    "v_max3_u32 v90, v8, v9, v10\n"
    "v_max3_u32 v90, v90, v11, v12\n"
    "v_max3_u32 v90, v90, v13, v14\n"
    "v_max3_u32 v90, v90, v15, v16\n"
    "v_max3_u32 v90, v90, v17, v18\n"
    "v_max3_u32 v90, v90, v19, v20\n"
    "v_max3_u32 v90, v90, v21, v22\n"
    "v_max_u32 v90, v90, v23\n"
    "v_cmp_eq_u32 vcc, -1, v90\n"
    "s_nop 2\n"
    "s_cbranch_vccnz .R_12\n"
    "s_branch .R_12c\n"
    ".R_13:\n"
    "s_cmp_eq_u32 s57, 0\n"
    "s_cbranch_scc1 .R_13c\n"
    "s_sub_u32 s57, s57, 1\n"
    "buffer_load_dwordx4 v[64:67], v0, s[4:7], s28 offen sc1\n"
    "buffer_load_dwordx4 v[68:71], v0, s[4:7], s29 offen sc1\n"
    "buffer_load_dwordx4 v[72:75], v0, s[4:7], s30 offen sc1\n"
    "buffer_load_dwordx4 v[76:79], v0, s[4:7], s31 offen sc1\n"
    "buffer_load_dwordx4 v[80:83], v0, s[4:7], s34 offen sc1\n"
    "s_waitcnt vmcnt(0)\n"
    "v_max3_u32 v108, v64, v65, v66\n"
    "v_max3_u32 v108, v108, v67, v68\n"
    "v_max3_u32 v108, v108, v69, v70\n"
    "v_max3_u32 v108, v108, v71, v72\n"
    "v_max3_u32 v108, v108, v73, v74\n"
    "v_max3_u32 v108, v108, v75, v76\n"
    "v_max3_u32 v108, v108, v77, v78\n"
    "v_max3_u32 v108, v108, v79, v80\n"
    "v_max3_u32 v108, v108, v81, v82\n"
    "v_max_u32 v108, v108, v83\n"
    "v_cmp_eq_u32 vcc, -1, v108\n"
    "v_dot2_f32_bf16 v100, v68, s35, 0\n"
    "v_dot2_f32_bf16 v104, v68, v68, s36\n"
    "v_dot2_f32_bf16 v101, v69, s35, 0\n"
    "v_dot2_f32_bf16 v105, v69, v69, s36\n"
    "v_dot2_f32_bf16 v102, v70, s35, 0\n"
    "v_dot2_f32_bf16 v106, v70, v70, s36\n"
    "v_dot2_f32_bf16 v103, v71, s35, 0\n"
    "v_dot2_f32_bf16 v107, v71, v71, s36\n"
    "v_dot2_f32_bf16 v100, v72, s35, v100\n"
    "v_dot2_f32_bf16 v104, v72, v72, v104\n"
    "v_dot2_f32_bf16 v101, v73, s35, v101\n"
    "v_dot2_f32_bf16 v105, v73, v73, v105\n"
    "v_dot2_f32_bf16 v102, v74, s35, v102\n"
    "v_dot2_f32_bf16 v106, v74, v74, v106\n"
    "v_dot2_f32_bf16 v103, v75, s35, v103\n"
    "v_dot2_f32_bf16 v107, v75, v75, v107\n"
    "v_dot2_f32_bf16 v100, v76, s35, v100\n"
    "v_dot2_f32_bf16 v104, v76, v76, v104\n"
    "v_dot2_f32_bf16 v101, v77, s35, v101\n"
    "v_dot2_f32_bf16 v105, v77, v77, v105\n"
    "v_dot2_f32_bf16 v102, v78, s35, v102\n"
    "v_dot2_f32_bf16 v106, v78, v78, v106\n"
    "v_dot2_f32_bf16 v103, v79, s35, v103\n"
    "v_dot2_f32_bf16 v107, v79, v79, v107\n"
    "v_dot2_f32_bf16 v100, v80, s35, v100\n"
    "v_dot2_f32_bf16 v104, v80, v80, v104\n"
    "v_dot2_f32_bf16 v101, v81, s35, v101\n"
    "v_dot2_f32_bf16 v105, v81, v81, v105\n"
    "v_dot2_f32_bf16 v102, v82, s35, v102\n"
    "v_dot2_f32_bf16 v106, v82, v82, v106\n"
    "v_dot2_f32_bf16 v103, v83, s35, v103\n"
    "v_dot2_f32_bf16 v107, v83, v83, v107\n"
    "s_nop 2\n"
    "s_cbranch_vccnz .R_13\n"
    "s_branch .R_13c\n"
    ".R_14:\n"
    "s_cmp_eq_u32 s57, 0\n"
    "s_cbranch_scc1 .R_14c\n"
    "s_sub_u32 s57, s57, 1\n"
    "buffer_load_dwordx4 v[64:67], v0, s[4:7], s28 offen sc1\n"
    "buffer_load_dwordx4 v[68:71], v0, s[4:7], s29 offen sc1\n"
    "buffer_load_dwordx4 v[72:75], v0, s[4:7], s30 offen sc1\n"
    "buffer_load_dwordx4 v[76:79], v0, s[4:7], s31 offen sc1\n"
    "buffer_load_dwordx4 v[80:83], v0, s[4:7], s34 offen sc1\n"
    "s_waitcnt vmcnt(0)\n"
    "v_max3_u32 v108, v64, v65, v66\n"
    "v_max3_u32 v108, v108, v67, v68\n"
    "v_max3_u32 v108, v108, v69, v70\n"
    "v_max3_u32 v108, v108, v71, v72\n"
    "v_max3_u32 v108, v108, v73, v74\n"
    "v_max3_u32 v108, v108, v75, v76\n"
    "v_max3_u32 v108, v108, v77, v78\n"
    "v_max3_u32 v108, v108, v79, v80\n"
    "v_max3_u32 v108, v108, v81, v82\n"
    "v_max_u32 v108, v108, v83\n"
    "v_cmp_eq_u32 vcc, -1, v108\n"
    "v_dot2_f32_bf16 v100, v68, s35, 0\n"
    "v_dot2_f32_bf16 v104, v68, v68, s36\n"
    "v_dot2_f32_bf16 v101, v69, s35, 0\n"
    "v_dot2_f32_bf16 v105, v69, v69, s36\n"
    "v_dot2_f32_bf16 v102, v70, s35, 0\n"
    "v_dot2_f32_bf16 v106, v70, v70, s36\n"
    "v_dot2_f32_bf16 v103, v71, s35, 0\n"
    "v_dot2_f32_bf16 v107, v71, v71, s36\n"
    "v_dot2_f32_bf16 v100, v72, s35, v100\n"
    "v_dot2_f32_bf16 v104, v72, v72, v104\n"
    "v_dot2_f32_bf16 v101, v73, s35, v101\n"
    "v_dot2_f32_bf16 v105, v73, v73, v105\n"
    "v_dot2_f32_bf16 v102, v74, s35, v102\n"
    "v_dot2_f32_bf16 v106, v74, v74, v106\n"
    "v_dot2_f32_bf16 v103, v75, s35, v103\n"
    "v_dot2_f32_bf16 v107, v75, v75, v107\n"
    "v_dot2_f32_bf16 v100, v76, s35, v100\n"
    "v_dot2_f32_bf16 v104, v76, v76, v104\n"
    "v_dot2_f32_bf16 v101, v77, s35, v101\n"
    "v_dot2_f32_bf16 v105, v77, v77, v105\n"
    "v_dot2_f32_bf16 v102, v78, s35, v102\n"
    "v_dot2_f32_bf16 v106, v78, v78, v106\n"
    "v_dot2_f32_bf16 v103, v79, s35, v103\n"
    "v_dot2_f32_bf16 v107, v79, v79, v107\n"
    "v_dot2_f32_bf16 v100, v80, s35, v100\n"
    "v_dot2_f32_bf16 v104, v80, v80, v104\n"
    "v_dot2_f32_bf16 v101, v81, s35, v101\n"
    "v_dot2_f32_bf16 v105, v81, v81, v105\n"
    "v_dot2_f32_bf16 v102, v82, s35, v102\n"
    "v_dot2_f32_bf16 v106, v82, v82, v106\n"
    "v_dot2_f32_bf16 v103, v83, s35, v103\n"
    "v_dot2_f32_bf16 v107, v83, v83, v107\n"
    "s_nop 2\n"
    "s_cbranch_vccnz .R_14\n"
    "s_branch .R_14c\n"
    ".R_15:\n"
    "s_cmp_eq_u32 s57, 0\n"
    "s_cbranch_scc1 .R_15c\n"
    "s_sub_u32 s57, s57, 1\n"
    "buffer_load_dwordx4 v[132:135], v128, s[4:7], s79 offen sc1\n"
    "buffer_load_dwordx4 v[136:139], v128, s[4:7], s79 offen offset:1024 sc1\n"
    "buffer_load_dwordx4 v[140:143], v128, s[4:7], s79 offen offset:2048 sc1\n"
    "buffer_load_dwordx4 v[144:147], v128, s[4:7], s79 offen offset:3072 sc1\n"
    "s_waitcnt vmcnt(0)\n"
    "v_max3_u32 v148, v132, v133, v134\n"
    "v_max3_u32 v148, v148, v135, v136\n"
    "v_max3_u32 v148, v148, v137, v138\n"
    "v_max3_u32 v148, v148, v139, v140\n"
    "v_max3_u32 v148, v148, v141, v142\n"
    "v_max3_u32 v148, v148, v143, v144\n"
    "v_max3_u32 v148, v148, v145, v146\n"
    "v_max_u32 v148, v148, v147\n"
    "v_cmp_eq_u32 vcc, -1, v148\n"
    "s_nop 2\n"
    "s_cbranch_vccnz .R_15\n"
    "s_branch .R_15c\n"
    ".L_end:\n"
    :
    : "s"(__builtin_amdgcn_kernarg_segment_ptr()), "s"(blockIdx.x), "s"(thr_tab), "v"(threadIdx.x)
    : "v1", "v2", "v3", "v4", "v5", "v6", "v7", "v8", "v9", "v10", "v11", "v12", "v13", "v14", "v15", "v16", "v17", "v18", "v19", "v20", "v21", "v22", "v23", "v24", "v25", "v26", "v27", "v28", "v29", "v30", "v31", "v32", "v33", "v34", "v35", "v36", "v37", "v38", "v39", "v40", "v41", "v42", "v43", "v44", "v45", "v46", "v47", "v48", "v49", "v50", "v51", "v52", "v53", "v54", "v55", "v56", "v57", "v58", "v59", "v60", "v61", "v62", "v63", "v64", "v65", "v66", "v67", "v68", "v69", "v70", "v71", "v72", "v73", "v74", "v75", "v76", "v77", "v78", "v79", "v80", "v81", "v82", "v83", "v84", "v85", "v86", "v87", "v88", "v89", "v90", "v91", "v92", "v93", "v94", "v95", "v96", "v97", "v98", "v99", "v100", "v101", "v102", "v103", "v104", "v105", "v106", "v107", "v108", "v109", "v110", "v111", "v112", "v113", "v114", "v115", "v116", "v117", "v118", "v119", "v120", "v121", "v122", "v123", "v124", "v125", "v126", "v127", "v128", "v129", "v130", "v131", "v132", "v133", "v134", "v135", "v136", "v137", "v138", "v139", "v140", "v141", "v142", "v143", "v144", "v145", "v146", "v147", "v148", "v149", "v150", "v151", "v152", "v153", "v154", "v155", "v156", "v157", "v158", "v159", "v160", "v161", "v162", "v163", "v164", "v165", "v166", "v167", "v168", "v169", "v170", "v171", "v172", "v173", "v174", "v175", "v176", "v177", "v178", "v179", "v180", "v181", "v182", "v183", "v184", "v185", "v186", "v187", "v188", "v189", "v190", "v191", "v192", "v193", "v194", "v195", "v196", "v197", "v198", "v199", "v200", "v201", "v202", "v203", "v204", "v205", "v206", "v207", "v208", "v209", "v210", "v211", "v212", "v213", "v214", "v215", "v216", "v217", "v218", "v219", "v220", "v221", "v222", "v223", "v224", "v225", "v226", "v227", "v228", "v229", "v230", "v231", "v232", "v233", "v234", "v235", "v236", "v237", "v238", "v239", "v240", "v241", "v242", "v243", "v244", "v245", "v246", "v247", "v248", "v249", "v250", "v251", "v252", "v253", "v254", "v255", "a0", "a1", "a2", "a3", "a4", "a5", "a6", "a7", "a8", "a9", "a10", "a11", "a12", "a13", "a14", "a15", "a16", "a17", "a18", "a19", "a20", "a21", "a22", "a23", "a24", "a25", "a26", "a27", "a28", "a29", "a30", "a31", "a32", "a33", "a34", "a35", "a36", "a37", "a38", "a39", "a40", "a41", "a42", "a43", "a44", "a45", "a46", "a47", "a48", "a49", "a50", "a51", "a52", "a53", "a54", "a55", "a56", "a57", "a58", "a59", "a60", "a61", "a62", "a63", "a64", "a65", "a66", "a67", "a68", "a69", "a70", "a71", "a72", "a73", "a74", "a75", "a76", "a77", "a78", "a79", "a80", "a81", "a82", "a83", "a84", "a85", "a86", "a87", "a88", "a89", "a90", "a91", "a92", "a93", "a94", "a95", "a96", "a97", "a98", "a99", "a100", "a101", "a102", "a103", "a104", "a105", "a106", "a107", "a108", "a109", "a110", "a111", "a112", "a113", "a114", "a115", "a116", "a117", "a118", "a119", "a120", "a121", "a122", "a123", "a124", "a125", "a126", "a127", "a128", "a129", "a130", "a131", "a132", "a133", "a134", "a135", "a136", "a137", "a138", "a139", "a140", "a141", "a142", "a143", "a144", "a145", "a146", "a147", "a148", "a149", "a150", "a151", "a152", "a153", "a154", "a155", "a156", "a157", "a158", "a159", "a160", "a161", "a162", "a163", "a164", "a165", "a166", "a167", "a168", "a169", "a170", "a171", "a172", "a173", "a174", "a175", "a176", "a177", "a178", "a179", "a180", "a181", "a182", "a183", "a184", "a185", "a186", "a187", "a188", "a189", "a190", "a191", "a192", "a193", "a194", "a195", "a196", "a197", "a198", "a199", "a200", "a201", "a202", "a203", "a204", "a205", "a206", "a207", "a208", "a209", "a210", "a211", "a212", "a213", "a214", "a215", "a216", "a217", "a218", "a219", "a220", "a221", "a222", "a223", "a224", "a225", "a226", "a227", "a228", "a229", "a230", "a231", "a232", "a233", "a234", "a235", "a236", "a237", "a238", "a239", "a240", "a241", "a242", "a243", "a244", "a245", "a246", "a247", "a248", "a249", "a250", "a251", "a252", "a253", "a254", "a255", "s6", "s7", "s8", "s9", "s10", "s11", "s12", "s13", "s14", "s15", "s16", "s17", "s18", "s19", "s20", "s21", "s22", "s23", "s24", "s25", "s26", "s27", "s28", "s29", "s30", "s31", "s33", "s34", "s35", "s36", "s37", "s38", "s39", "s40", "s41", "s42", "s43", "s44", "s45", "s46", "s47", "s48", "s49", "s50", "s51", "s52", "s53", "s54", "s55", "s56", "s57", "s58", "s59", "s60", "s61", "s62", "s63", "s64", "s65", "s66", "s67", "s68", "s69", "s70", "s71", "s72", "s73", "s74", "s75", "s76", "s77", "s78", "s79", "s80", "s81", "s82", "s83", "s84", "s85", "s86", "s87", "s88", "s89", "s90", "s91", "s92", "s93", "s94", "s95", "s96", "s97", "s98", "s99", "vcc", "scc", "memory");
}

extern "C" void kernel_launch(void* const* d_in, const int* in_sizes, int n_in, void* d_out, int out_size, void* d_ws, size_t ws_size,
                              hipStream_t stream) {
  Params p;
  p.x = d_in[0]; p.wqkv = d_in[1]; p.wo = d_in[2];
  p.lar = d_in[4]; p.aim = d_in[5]; p.cre = d_in[6]; p.cim = d_in[7]; p.ldt = d_in[8]; p.dsk = d_in[9];
  p.out = d_out; p.ws = d_ws;
  (void)hipFuncSetAttribute((const void*)fused_ab2, hipFuncAttributeMaxDynamicSharedMemorySize, 162592);
  fused_ab2<<<256, 256, 162592, stream>>>(p);
}
```
